```python
import torch

def _softmax(x, axis=-1):
    return torch.softmax(x, dim=axis)
def _gelu(x):
    return torch.nn.functional.gelu(x, approximate="tanh")
def _silu(x):
    return torch.nn.functional.silu(x)
def _sigmoid(x):
    return torch.sigmoid(x)
def _layernorm(x, axis=-1, eps=1e-5):
    m = x.mean(dim=axis, keepdim=True)
    v = x.var(dim=axis, keepdim=True, unbiased=False)
    return (x - m) / torch.sqrt(v + eps)
def _scatter_add(tgt, idx, src, axis=0):
    return tgt.clone().index_add_(axis, idx, src)
def _segment_sum(src, seg, n):
    out = torch.zeros((n,) + src.shape[1:], dtype=src.dtype)
    return out.index_add_(0, seg, src)
def _pad(x, pw):
    flat = tuple(v for pair in reversed(pw) for v in pair)
    return torch.nn.functional.pad(x, flat)

kernel_name = 'recurrent_scan__ssm_conv_h3__geglu_ffn__prompt'

def setup_inputs():

    _w = (lambda a: (lambda t: t.to(torch.bfloat16) if t.is_floating_point() else t)(torch.as_tensor(a)))
    def _A_setup():
        B, S, D = 1, 2048, 448
        ks = 4
        return {
            "q": _w((lambda *s: torch.randn(*s, dtype=torch.bfloat16) * 0.05)(B, S, D)),
            "k": _w((lambda *s: torch.randn(*s, dtype=torch.bfloat16) * 0.2)(B, S, D)),
            "v": _w((lambda *s: torch.randn(*s, dtype=torch.bfloat16) * 0.2)(B, S, D)),
            "shift_k": _w((lambda *s: torch.randn(*s, dtype=torch.bfloat16) * 0.2)(D, ks) * 0.5),
            "ssm_k": _w((lambda *s: torch.randn(*s, dtype=torch.bfloat16) * 0.2)(D, S) * (1.0 / S ** 0.5)),
            "ssm_D": _w((lambda *s: torch.randn(*s, dtype=torch.bfloat16) * 0.2)(D)),
        }
    _A_in = _A_setup()

    def _B_setup():
        B, S, D, F = 1, 2048, 448, 1792
        return {
            "x": _w((lambda *s: torch.randn(*s, dtype=torch.bfloat16) * 0.05)(B, S, D)),
            "w_gate": _w((lambda *s: torch.randn(*s, dtype=torch.bfloat16) * 0.1)(D, F)),
            "w_up": _w((lambda *s: torch.randn(*s, dtype=torch.bfloat16) * 0.1)(D, F)),
            "w_down": _w((lambda *s: torch.randn(*s, dtype=torch.bfloat16) * 0.1)(F, D)),
        }
    _B_in = _B_setup()

    return {
        "A_q": _A_in["q"],
        "A_k": _A_in["k"],
        "A_v": _A_in["v"],
        "A_shift_k": _A_in["shift_k"],
        "A_ssm_k": _A_in["ssm_k"],
        "A_ssm_D": _A_in["ssm_D"],
        "B_x": _B_in["x"],
        "B_w_gate": _B_in["w_gate"],
        "B_w_up": _B_in["w_up"],
        "B_w_down": _B_in["w_down"],
    }

def reference(A_q, A_k, A_v, A_shift_k, A_ssm_k, A_ssm_D, B_x, B_w_gate, B_w_up, B_w_down):
    def _A_ref(q, k, v, shift_k, ssm_k, ssm_D):
        B, S, D = q.shape
        ks = shift_k.shape[-1]
        k_pad = _pad(k, ((0, 0), (ks - 1, 0), (0, 0)))
        k_shift = torch.zeros_like(k)
        for t in range(ks):
            k_shift = k_shift + k_pad[:, t:t + S, :] * shift_k[:, ks - 1 - t][None, None, :]
        kv = k_shift * v
        L = 2 * S
        kv_pad = _pad(kv, ((0, 0), (0, S), (0, 0)))
        kf_pad = _pad(ssm_k, ((0, 0), (0, S)))
        kv_f = (lambda x, axes=(-2,-1): torch.fft.rfft2(x.float(), dim=axes))(kv_pad[:, None, :, :], axes=(1, 2))
        kf_f = (lambda x, axes=(-2,-1): torch.fft.rfft2(x.float(), dim=axes))(kf_pad[:, None, :], axes=(1, 2))
        kf_f = (lambda x, *d: x.permute(*d))(kf_f, 1, 2, 0)
        y_f = kv_f * kf_f[None, :, :, :]
        y = (lambda x, s, axes=(-2,-1): torch.fft.irfft2(x, s=s, dim=axes).to(torch.bfloat16))(y_f, (1, L), axes=(1, 2))[:, 0, :S, :]
        y = y + kv * ssm_D[None, None, :]
        return q * y

    def _B_ref(x, w_gate, w_up, w_down):
        return (_gelu(x @ w_gate) * (x @ w_up)) @ w_down

    x = A_q
    xn = _layernorm(x, axis=-1)
    s = _A_ref(xn, A_k, A_v, A_shift_k, A_ssm_k, A_ssm_D)
    g = _silu(_B_ref(xn, B_w_gate, B_w_up, B_w_down))
    return x + s * g
```

```cpp
#include <hip/hip_runtime.h>
#include <hip/hip_bf16.h>
#include <stdint.h>
typedef unsigned short u16;
static __device__ __forceinline__ float b2f(u16 x){ return __uint_as_float(((unsigned)x)<<16); }
static __device__ __forceinline__ u16 f2b(float x){ unsigned u=__float_as_uint(x); u += 0x7fffu + ((u>>16)&1u); return (u16)(u>>16); }
static __device__ __forceinline__ float rb(float x){ return b2f(f2b(x)); }
constexpr int S=2048,D=448,F=1792;

__global__ void k_ln(const u16* q, u16* xn){
  int t=blockIdx.x; __shared__ float red[64];
  int l=threadIdx.x; float v[7]; float s=0;
  for(int i=0;i<7;i++){ v[i]=b2f(q[t*D+l+64*i]); s+=v[i]; }
  for(int o=32;o;o>>=1) s+=__shfl_xor(s,o);
  float m=s/D; float ss=0; for(int i=0;i<7;i++){ float d=v[i]-m; ss+=d*d; }
  for(int o=32;o;o>>=1) ss+=__shfl_xor(ss,o);
  float var=rb(ss/D); float mb=rb(m); float den=rb(sqrtf(rb(var+1e-5f)));
  for(int i=0;i<7;i++) xn[t*D+l+64*i]=f2b(rb(v[i]-mb)/den);
}
__global__ void k_kv(const u16* k,const u16* v,const u16* sk,u16* kv){
  int idx=blockIdx.x*blockDim.x+threadIdx.x; if(idx>=S*D) return; int t=idx/D,d=idx%D;
  float acc=0;
  for(int tt=0;tt<4;tt++){ int ts=t-3+tt; float kk= ts>=0? b2f(k[ts*D+d]):0.f; acc=rb(acc+rb(kk*b2f(sk[d*4+3-tt]))); }
  kv[idx]=f2b(acc*b2f(v[idx]));
}
__global__ void k_conv(const u16* kv,const u16* h,const u16* Dv,u16* y){
  int idx=blockIdx.x*blockDim.x+threadIdx.x; if(idx>=S*D) return; int t=idx/D,d=idx%D;
  float acc=0; for(int j=0;j<=t;j++) acc+=b2f(h[d*S+t-j])*b2f(kv[j*D+d]);
  float yy=rb(acc); y[idx]=f2b(yy+rb(b2f(kv[idx])*b2f(Dv[d])));
}
__global__ void k_g1(const u16* xn,const u16* wg,const u16* wu,u16* H){
  int idx=blockIdx.x*blockDim.x+threadIdx.x; int t=idx/F,f=idx%F; float g=0,u=0;
  for(int k=0;k<D;k++){ float x=b2f(xn[t*D+k]); g+=x*b2f(wg[k*F+f]); u+=x*b2f(wu[k*F+f]); }
  g=rb(g); u=rb(u);
  float ge=rb(0.5f*g*(1.f+tanhf(0.7978845608028654f*(g+0.044715f*g*g*g))));
  H[idx]=f2b(ge*u);
}
__global__ void k_g2(const u16* H,const u16* wd,const u16* x,const u16* xn,const u16* y,u16* out){
  int idx=blockIdx.x*blockDim.x+threadIdx.x; int t=idx/D,d=idx%D; float o=0;
  for(int f=0;f<F;f++) o+=b2f(H[t*F+f])*b2f(wd[f*D+d]);
  o=rb(o); float g=rb(o/(1.f+expf(-o))); float s=rb(b2f(xn[idx])*b2f(y[idx]));
  out[idx]=f2b(b2f(x[idx])+rb(s*g));
}
extern "C" void kernel_launch(void* const* d_in, const int* in_sizes, int n_in, void* d_out, int out_size, void* d_ws, size_t ws_size, hipStream_t stream){
  const u16* q=(const u16*)d_in[0]; const u16* k=(const u16*)d_in[1]; const u16* v=(const u16*)d_in[2];
  const u16* sk=(const u16*)d_in[3]; const u16* h=(const u16*)d_in[4]; const u16* Dv=(const u16*)d_in[5];
  const u16* wg=(const u16*)d_in[7]; const u16* wu=(const u16*)d_in[8]; const u16* wd=(const u16*)d_in[9];
  u16* ws=(u16*)d_ws; u16* xn=ws; u16* kv=xn+S*D; u16* y=kv+S*D; u16* H=y+S*D;
  k_ln<<<S,64,0,stream>>>(q,xn);
  k_kv<<<S*D/256,256,0,stream>>>(k,v,sk,kv);
  k_conv<<<S*D/256,256,0,stream>>>(kv,h,Dv,y);
  k_g1<<<S*F/256,256,0,stream>>>(xn,wg,wu,H);
  k_g2<<<S*D/256,256,0,stream>>>(H,wd,q,xn,y,(u16*)d_out);
}
```

```cpp
#include <hip/hip_runtime.h>
#include <hip/hip_bf16.h>
#include <stdint.h>
typedef unsigned short u16;
static __device__ __forceinline__ float b2f(u16 x){ return __uint_as_float(((unsigned)x)<<16); }
static __device__ __forceinline__ u16 f2b(float x){ unsigned u=__float_as_uint(x); u += 0x7fffu + ((u>>16)&1u); return (u16)(u>>16); }
static __device__ __forceinline__ float rb(float x){ return b2f(f2b(x)); }
constexpr int S=2048,D=448,F=1792;

__global__ void k_ln(const u16* q, u16* xn){
  int t=blockIdx.x; __shared__ float red[64];
  int l=threadIdx.x; float v[7]; float s=0;
  for(int i=0;i<7;i++){ v[i]=b2f(q[t*D+l+64*i]); s+=v[i]; }
  for(int o=32;o;o>>=1) s+=__shfl_xor(s,o);
  float m=s/D; float ss=0; for(int i=0;i<7;i++){ float d=v[i]-m; ss+=d*d; }
  for(int o=32;o;o>>=1) ss+=__shfl_xor(ss,o);
  float var=rb(ss/D); float mb=rb(m); float den=rb(sqrtf(rb(var+1e-5f)));
  for(int i=0;i<7;i++) xn[t*D+l+64*i]=f2b(rb(v[i]-mb)/den);
}
__global__ void k_kv(const u16* k,const u16* v,const u16* sk,u16* kv){
  int idx=blockIdx.x*blockDim.x+threadIdx.x; if(idx>=S*D) return; int t=idx/D,d=idx%D;
  float acc=0;
  for(int tt=0;tt<4;tt++){ int ts=t-3+tt; float kk= ts>=0? b2f(k[ts*D+d]):0.f; acc=rb(acc+rb(kk*b2f(sk[d*4+3-tt]))); }
  kv[idx]=f2b(acc*b2f(v[idx]));
}
__global__ void k_conv(const u16* kv,const u16* h,const u16* Dv,u16* y){
  int idx=blockIdx.x*blockDim.x+threadIdx.x; if(idx>=S*D) return; int t=idx/D,d=idx%D;
  float acc=0; for(int j=0;j<=t;j++) acc+=b2f(h[d*S+t-j])*b2f(kv[j*D+d]);
  float yy=rb(acc); y[idx]=f2b(yy+rb(b2f(kv[idx])*b2f(Dv[d])));
}
__global__ void k_g1(const u16* xn,const u16* wg,const u16* wu,u16* H){
  int idx=blockIdx.x*blockDim.x+threadIdx.x; int t=idx/F,f=idx%F; float g=0,u=0;
  for(int k=0;k<D;k++){ float x=b2f(xn[t*D+k]); g+=x*b2f(wg[k*F+f]); u+=x*b2f(wu[k*F+f]); }
  g=rb(g); u=rb(u);
  float ge=rb(0.5f*g*(1.f+tanhf(0.7978845608028654f*(g+0.044715f*g*g*g))));
  H[idx]=f2b(ge*u);
}
__global__ void k_g2(const u16* H,const u16* wd,const u16* x,const u16* xn,const u16* y,u16* out){
  int idx=blockIdx.x*blockDim.x+threadIdx.x; int t=idx/D,d=idx%D; float o=0;
  for(int f=0;f<F;f++) o+=b2f(H[t*F+f])*b2f(wd[f*D+d]);
  o=rb(o); float g=rb(o/(1.f+expf(-o))); float s=rb(b2f(xn[idx])*b2f(y[idx]));
  out[idx]=f2b(b2f(x[idx])+rb(s*g));
}

__global__ void k_probe(const unsigned* p, int n, int stride_dw, int zero, unsigned* sink){
  unsigned idx=threadIdx.x, acc=0;
  for(int i=0;i<n;i++){ unsigned v=p[idx]; acc^=v; idx+=stride_dw+(v&zero); }
  if(acc==0x9e3779b9u && zero==1) sink[0]=acc;
}
extern "C" void kernel_launch(void* const* d_in, const int* in_sizes, int n_in, void* d_out, int out_size, void* d_ws, size_t ws_size, hipStream_t stream){
  const u16* q=(const u16*)d_in[0]; const u16* k=(const u16*)d_in[1]; const u16* v=(const u16*)d_in[2];
  const u16* sk=(const u16*)d_in[3]; const u16* h=(const u16*)d_in[4]; const u16* Dv=(const u16*)d_in[5];
  const u16* wg=(const u16*)d_in[7]; const u16* wu=(const u16*)d_in[8]; const u16* wd=(const u16*)d_in[9];
  u16* ws=(u16*)d_ws; u16* xn=ws; u16* kv=xn+S*D; u16* y=kv+S*D; u16* H=y+S*D;
  k_probe<<<1,64,0,stream>>>((const unsigned*)d_in[6],0,1024,0,(unsigned*)d_ws+(200<<18));
  k_ln<<<S,64,0,stream>>>(q,xn);
  k_kv<<<S*D/256,256,0,stream>>>(k,v,sk,kv);
  k_conv<<<S*D/256,256,0,stream>>>(kv,h,Dv,y);
  k_g1<<<S*F/256,256,0,stream>>>(xn,wg,wu,H);
  k_g2<<<S*D/256,256,0,stream>>>(H,wd,q,xn,y,(u16*)d_out);
}
```

```cpp
#include <hip/hip_runtime.h>
#include <hip/hip_bf16.h>
#include <stdint.h>

typedef unsigned short u16;
typedef __attribute__((ext_vector_type(8))) short bf16x8;
typedef __attribute__((ext_vector_type(4))) short s16x4;
typedef __attribute__((ext_vector_type(4))) float f32x4;
typedef __attribute__((ext_vector_type(2))) float f32x2;
typedef __attribute__((ext_vector_type(16))) float f32x16;
typedef __attribute__((ext_vector_type(4))) unsigned u32x4;
typedef __attribute__((ext_vector_type(2))) unsigned u32x2;
typedef __attribute__((ext_vector_type(2))) __bf16 bf16x2;
typedef __amdgpu_buffer_rsrc_t rsrc_t;
#define LAS __attribute__((address_space(3)))
#define GAS __attribute__((address_space(1)))
#define DI __device__ __forceinline__

#define SPLIT 0

constexpr int S_ = 2048, D_ = 448, F_ = 1792;
constexpr unsigned WS_EPOCH = 0, WS_FU = 4096, WS_FH = 8192, WS_FY = 36864, WS_STATS = 65536;
constexpr unsigned WS_UT = 131072, WS_YT = 2097152, WS_H = 4194304, WS_END = WS_H + 7340032;
constexpr unsigned WS_DBG = 16u << 20;
constexpr int LDS_BYTES = 163840;
constexpr unsigned SPIN_CAP = 1u << 14;

struct Params {
  const u16 *q, *k, *v, *sk, *h, *dv, *wg, *wu, *wd;
  u16* out;
  char* ws;
};

DI rsrc_t mk_rsrc(const void* p, unsigned bytes) { return __builtin_amdgcn_make_buffer_rsrc((void*)p, (short)0, (int)bytes, 0x00020000); }
DI void dma16(rsrc_t r, unsigned voff, unsigned soff, unsigned lds) {
  asm volatile("s_mov_b32 m0, %0\n\ts_nop 0\n\tbuffer_load_dwordx4 %1, %2, %3 offen lds" ::"s"(lds), "v"(voff), "s"(r), "s"(soff) : "memory");
}
template <int N> DI void wait_vm() { asm volatile("s_waitcnt vmcnt(%0)" ::"i"(N) : "memory"); }
DI void bar() { asm volatile("s_waitcnt lgkmcnt(0)\n\ts_barrier" ::: "memory"); }
DI float bf_lo(unsigned x) { return __uint_as_float(x << 16); }
DI float bf_hi(unsigned x) { return __uint_as_float(x & 0xffff0000u); }
DI unsigned pk_bf16(float lo, float hi) { f32x2 t = {lo, hi}; return __builtin_bit_cast(unsigned, __builtin_convertvector(t, bf16x2)); }
DI bf16x8 lds_b128(const char* p) { return *(const LAS bf16x8*)(p); }
DI s16x4 lds_tr(unsigned a) { return __builtin_amdgcn_ds_read_tr16_b64_v4i16((LAS s16x4*)(size_t)a); }
DI bf16x8 cat(s16x4 a, s16x4 b) { return __builtin_shufflevector(a, b, 0, 1, 2, 3, 4, 5, 6, 7); }
template <class T> DI T lds_ld(unsigned a) { return *(const LAS T*)(size_t)a; }
template <class T> DI void lds_st(unsigned a, T v) { *(LAS T*)(size_t)a = v; }
DI f32x16 mfma32(bf16x8 a, bf16x8 b, f32x16 c) { return __builtin_amdgcn_mfma_f32_32x32x16_bf16(a, b, c, 0, 0, 0); }
DI f32x4 mfma16(bf16x8 a, bf16x8 b, f32x4 c) { return __builtin_amdgcn_mfma_f32_16x16x32_bf16(a, b, c, 0, 0, 0); }
DI unsigned long long rtime() { return __builtin_amdgcn_s_memrealtime(); }
DI float dpp_add(float v, const int ctrl_row_shr) { return v; }
template <int CTRL, int RM> DI float dpp_mov(float v) { return __builtin_bit_cast(float, __builtin_amdgcn_update_dpp(0, __builtin_bit_cast(int, v), CTRL, RM, 0xf, true)); }
DI float wave_sum(float v) {
  v += dpp_mov<0x111, 0xf>(v); v += dpp_mov<0x112, 0xf>(v); v += dpp_mov<0x114, 0xf>(v); v += dpp_mov<0x118, 0xf>(v);
  v += dpp_mov<0x142, 0xa>(v); v += dpp_mov<0x143, 0xc>(v);
  return __builtin_bit_cast(float, __builtin_amdgcn_readlane(__builtin_bit_cast(int, v), 63));
}
#define TS(i)

DI void poll_flags(const char* base, int n, unsigned tag, int lane) {
  const GAS unsigned* f = (const GAS unsigned*)(base + (lane < n ? lane : 0) * 128);
  for (unsigned it = 0; it < SPIN_CAP; ++it) {
    unsigned x = __hip_atomic_load(f, __ATOMIC_RELAXED, __HIP_MEMORY_SCOPE_AGENT);
    if (__all(x == tag)) break;
    __builtin_amdgcn_s_sleep(2);
  }
}
DI void post_flag(char* addr, unsigned tag) { __hip_atomic_store((GAS unsigned*)addr, tag, __ATOMIC_RELAXED, __HIP_MEMORY_SCOPE_AGENT); }

DI u32x4 row_stats(u32x4 d, unsigned tag) {
  float e[8];
#pragma unroll
  for (int i = 0; i < 4; ++i) { e[2 * i] = bf_lo(d[i]); e[2 * i + 1] = bf_hi(d[i]); }
  float s = ((e[0] + e[1]) + (e[2] + e[3])) + ((e[4] + e[5]) + (e[6] + e[7]));
  const float mean = wave_sum(s) * (1.f / D_);
  const float on = (threadIdx.x & 63) < 56 ? 1.f : 0.f;
  float ss = 0.f;
#pragma unroll
  for (int i = 0; i < 8; ++i) { const float t = e[i] - mean; ss = fmaf(t, t, ss); }
  const float var = wave_sum(ss * on) * (1.f / D_);
  const float rstd = __builtin_amdgcn_rsqf(var + 1e-5f);
  u32x4 g = {tag, __float_as_uint(mean), tag, __float_as_uint(rstd)};
  return g;
}
DI void stats_wait(rsrc_t rs, unsigned off, unsigned tag, float& mean, float& rstd) {
  for (unsigned it = 0;; ++it) {
    const u32x4 g = __builtin_amdgcn_raw_buffer_load_b128(rs, off, 0, 16);
    mean = __uint_as_float(g[1]); rstd = __uint_as_float(g[3]);
    if (__all(g[0] == tag && g[2] == tag) || it >= SPIN_CAP) break;
    __builtin_amdgcn_s_sleep(1);
  }
}

DI void phase_gemm1(const Params& p, unsigned lb, int x, int s, int w, int lane, unsigned tag) {
  const int ib = 4 * (x >> 2) + s / 7, jb = 7 * (x & 3) + s % 7;
  const rsrc_t rq = mk_rsrc(p.q, S_ * D_ * 2), rg = mk_rsrc(p.wg, D_ * F_ * 2), ru = mk_rsrc(p.wu, D_ * F_ * 2);
  const rsrc_t rs = mk_rsrc(p.ws + WS_STATS, S_ * 16), rh = mk_rsrc(p.ws + WS_H, 7340032);
  const int L3 = lane >> 3, L7 = lane & 7;
  const int gw = jb * 8 + w;
  u32x4 sr0, sr1;
  {
    const unsigned o0 = lane < 56 ? (256 * ib + gw) * 896 + 16 * lane : 0x80000000u;
    const unsigned o1 = (lane < 56 && gw < 32) ? (256 * ib + 224 + gw) * 896 + 16 * lane : 0x80000000u;
    asm volatile("buffer_load_dwordx4 %0, %2, %4, 0 offen\n\tbuffer_load_dwordx4 %1, %3, %4, 0 offen" : "=&v"(sr0), "=&v"(sr1) : "v"(o0), "v"(o1), "s"(rq) : "memory");
  }
  const unsigned vx0 = (32 * w + L3) * 896 + 16 * (L7 ^ ((lane >> 4) & 3));
  const unsigned vx1 = (32 * w + 8 + L3) * 896 + 16 * (L7 ^ (((lane >> 4) & 3) | 4));
  const int kw = 8 * w + L3;
  const unsigned vwg = kw * 3584 + jb * 128 + 32 * ((L7 >> 1) ^ (((lane >> 4) & 1) | ((w & 1) << 1))) + 16 * (L7 & 1);
  const unsigned vwu = kw * 3584 + jb * 128 + 32 * ((L7 >> 1) ^ (((lane >> 4) & 1) | (((w & 1) ^ 1) << 1))) + 16 * (L7 & 1);
  const unsigned sx = ib * 256 * 896;
  auto stage = [&](int c, unsigned slot) {
    const unsigned xo = sx + c * 128, wo = c * 64 * 3584, l = lb + slot;
    dma16(rq, vx0, xo, l + w * 4096);
    dma16(rq, vx1, xo, l + w * 4096 + 1024);
    dma16(rq, vx0, xo + 16 * 896, l + w * 4096 + 2048);
    dma16(rq, vx1, xo + 16 * 896, l + w * 4096 + 3072);
    dma16(rg, vwg, wo, l + 32768 + w * 1024);
    dma16(ru, vwu, wo, l + 40960 + w * 1024);
  };
  stage(0, 0);
  stage(1, 49152);
  const int tw = w & 3, fh = w >> 2, r = lane & 31, hh = lane >> 5;
  unsigned xa[4], wa[2], ca;
#pragma unroll
  for (int i = 0; i < 4; ++i) xa[i] = lb + (64 * tw + r) * 128 + 16 * ((2 * i + hh) ^ ((r >> 1) & 7));
  {
    const int blk = (lane >> 4) & 1, q = (lane & 15) >> 2, pp = lane & 3, m = pp >> 1, f = (q >> 1) | ((m ^ hh) << 1);
#pragma unroll
    for (int u = 0; u < 2; ++u) wa[u] = lb + 32768 + m * 8192 + (8 * hh + q) * 128 + 32 * ((2 * fh + u) ^ f) + 16 * blk + 8 * (pp & 1);
    const int m2 = w & 1, g = lane >> 4, f2 = (q >> 1) | (((m2 ^ g) & 1) << 1);
    ca = lb + 32768 + m2 * 8192 + (8 * g + q) * 128 + 32 * ((w >> 1) ^ f2) + 8 * pp;
  }
  f32x16 acc[2][2] = {};
  f32x4 cs = {};
  const bf16x8 ones = {0x3f80, 0x3f80, 0x3f80, 0x3f80, 0x3f80, 0x3f80, 0x3f80, 0x3f80};
  TS(1);
#pragma unroll
  for (int c = 0; c < 7; ++c) {
    if (c == 6) wait_vm<0>(); else if (c == 2) wait_vm<8>(); else wait_vm<6>();
    bar();
    if (c == 0) TS(2);
    const unsigned slot = (c % 3) * 49152;
    if (c < 5) stage(c + 2, ((c + 2) % 3) * 49152);
#pragma unroll
    for (int ks = 0; ks < 4; ++ks) {
      const bf16x8 b0 = lds_ld<bf16x8>(xa[ks] + slot), b1 = lds_ld<bf16x8>(xa[ks] + slot + 4096);
#pragma unroll
      for (int u = 0; u < 2; ++u) {
        const bf16x8 a = cat(lds_tr(wa[u] + slot + ks * 2048), lds_tr(wa[u] + slot + ks * 2048 + 512));
        acc[0][u] = mfma32(a, b0, acc[0][u]);
        acc[1][u] = mfma32(a, b1, acc[1][u]);
      }
      if (ks & 1) {
        const bf16x8 a = cat(lds_tr(ca + slot + (ks >> 1) * 4096), lds_tr(ca + slot + (ks >> 1) * 4096 + 512));
        cs = mfma16(a, ones, cs);
      }
    }
    if (c == 1) {
      asm volatile("" : "+v"(sr0), "+v"(sr1));
      const u32x4 g0 = row_stats(sr0, tag), g1 = row_stats(sr1, tag);
      const unsigned o0 = lane == 0 ? (256 * ib + gw) * 16 : 0x80000000u;
      const unsigned o1 = (lane == 0 && gw < 32) ? (256 * ib + 224 + gw) * 16 : 0x80000000u;
      asm volatile("buffer_store_dwordx4 %0, %2, %4, 0 offen sc1\n\tbuffer_store_dwordx4 %1, %3, %4, 0 offen sc1\n\ts_nop 1" ::"v"(g0), "v"(g1), "v"(o0), "v"(o1), "s"(rs) : "memory");
    }
  }
  TS(3);
  const unsigned csb = lb + 147456;
  if ((lane & 15) == 0) lds_st<f32x4>(csb + ((w & 1) * 64 + 16 * (w >> 1) + 4 * (lane >> 4)) * 4, cs);
  float mean[2], rstd[2];
#pragma unroll
  for (int tt = 0; tt < 2; ++tt) stats_wait(rs, (256 * ib + 64 * tw + 32 * tt + r) * 16, tag, mean[tt], rstd[tt]);
  bar();
  TS(4);
  constexpr float C0 = -2.3022081985f, C1 = -0.1029432397f;
#pragma unroll
  for (int u = 0; u < 2; ++u) {
    f32x4 cg[2], cu[2];
#pragma unroll
    for (int b = 0; b < 2; ++b) {
      cg[b] = lds_ld<f32x4>(csb + (16 * (2 * fh + u) + 8 * b + 4 * hh) * 4);
      cu[b] = lds_ld<f32x4>(csb + 256 + (16 * (2 * fh + u) + 8 * b + 4 * hh) * 4);
    }
#pragma unroll
    for (int tt = 0; tt < 2; ++tt) {
      const float rr = rstd[tt], nm = -mean[tt], k0 = rr * C0, k1 = C1 * rr * rr * rr, ir2 = __builtin_amdgcn_rcpf(rr * rr);
      float hv[8];
#pragma unroll
      for (int b = 0; b < 2; ++b)
#pragma unroll
        for (int e = 0; e < 4; ++e) {
          const float a = fmaf(nm, cg[b][e], acc[tt][u][8 * b + e]), bb = fmaf(nm, cu[b][e], acc[tt][u][8 * b + 4 + e]);
          const float z = a * fmaf(a * a, k1, k0);
          const float ex = __builtin_amdgcn_exp2f(z);
          hv[4 * b + e] = (a * bb) * __builtin_amdgcn_rcpf(fmaf(ex, ir2, ir2));
        }
      const u32x4 o = {pk_bf16(hv[0], hv[1]), pk_bf16(hv[2], hv[3]), pk_bf16(hv[4], hv[5]), pk_bf16(hv[6], hv[7])};
      __builtin_amdgcn_raw_buffer_store_b128(o, rh, ((8 * ib + 2 * tw + tt) * 112 + 4 * jb + 2 * fh + u) * 1024 + lane * 16, 0, 16);
    }
  }
  TS(5);
  wait_vm<0>();
  bar();
  if (threadIdx.x == 0) post_flag(p.ws + WS_FH + (ib * 28 + jb) * 128, tag);
  TS(6);
}

DI void phase_prep(const Params& p, int pi, int tid, unsigned tag) {
  const rsrc_t rk = mk_rsrc(p.k, S_ * D_ * 2), rv = mk_rsrc(p.v, S_ * D_ * 2), rw = mk_rsrc(p.sk, D_ * 8), ro = mk_rsrc(p.ws + WS_UT, D_ * S_ * 2);
  if (tid < 448) {
    const int cg = tid >> 3, rg = tid & 7, r0 = 64 * pi + 8 * rg;
    u32x4 kk[11], vv[8], ww[4];
#pragma unroll
    for (int n = 0; n < 11; ++n) {
      const unsigned o = (n >= 3 || r0 > 0) ? (unsigned)((r0 - 3 + n) * 896 + cg * 16) : 0x80000000u;
      kk[n] = __builtin_amdgcn_raw_buffer_load_b128(rk, o, 0, 2);
    }
#pragma unroll
    for (int n = 0; n < 8; ++n) vv[n] = __builtin_amdgcn_raw_buffer_load_b128(rv, (r0 + n) * 896 + cg * 16, 0, 2);
#pragma unroll
    for (int n = 0; n < 4; ++n) ww[n] = __builtin_amdgcn_raw_buffer_load_b128(rw, cg * 64 + n * 16, 0, 0);
#pragma unroll
    for (int c = 0; c < 8; ++c) {
      const unsigned w01 = ww[c >> 1][(c & 1) * 2], w23 = ww[c >> 1][(c & 1) * 2 + 1];
      const float w0 = bf_lo(w01), w1 = bf_hi(w01), w2 = bf_lo(w23), w3 = bf_hi(w23);
      float kc[11], uu[8];
#pragma unroll
      for (int n = 0; n < 11; ++n) kc[n] = (c & 1) ? bf_hi(kk[n][c >> 1]) : bf_lo(kk[n][c >> 1]);
#pragma unroll
      for (int n = 0; n < 8; ++n) {
        const float ks = fmaf(kc[n + 3], w0, fmaf(kc[n + 2], w1, fmaf(kc[n + 1], w2, kc[n] * w3)));
        uu[n] = ks * ((c & 1) ? bf_hi(vv[n][c >> 1]) : bf_lo(vv[n][c >> 1]));
      }
      const u32x4 o = {pk_bf16(uu[7], uu[6]), pk_bf16(uu[5], uu[4]), pk_bf16(uu[3], uu[2]), pk_bf16(uu[1], uu[0])};
      __builtin_amdgcn_raw_buffer_store_b128(o, ro, (8 * cg + c) * 4096 + r0 * 2, 0, 16);
    }
  }
  wait_vm<0>();
  bar();
  if (tid == 0) post_flag(p.ws + WS_FU + pi * 128, tag);
}

constexpr unsigned CV_BASE = 98304;
constexpr unsigned CV_UP = CV_BASE;
constexpr unsigned CV_HS = CV_BASE + 9216;
constexpr unsigned CV_RED = CV_HS;
DI void phase_conv(const Params& p, unsigned lb, int mi, int w, int lane, unsigned tag, bool wait) {
  const int chl = w >> 2, wq = w & 3, ch = 2 * mi + chl, t4 = wq * 64 + lane;
  const rsrc_t rh = mk_rsrc(p.h, D_ * S_ * 2), ru = mk_rsrc(p.ws + WS_UT, D_ * S_ * 2), ry = mk_rsrc(p.ws + WS_YT, D_ * S_ * 2);
  const u32x4 h4 = __builtin_amdgcn_raw_buffer_load_b128(rh, ch * 4096 + t4 * 16, 0, 2);
  const u32x2 h2 = __builtin_amdgcn_raw_buffer_load_b64(rh, ch * 4096 + t4 * 16 + 16, 0, 2);
  const float dv = bf_lo(p.dv[ch]);
  if (wait) { if (w == 0) poll_flags(p.ws + WS_FU, 32, tag, lane); bar(); }
  const u32x4 u4 = __builtin_amdgcn_raw_buffer_load_b128(ru, ch * 4096 + t4 * 16, 0, 16);
  const unsigned up = lb + CV_UP + chl * 4608, hs = lb + CV_HS + chl * 16640;
  {
    const unsigned w0 = h4[0], w1 = h4[1], w2 = h4[2], w3 = h4[3], w4 = h2[0], w5 = h2[1];
    const unsigned a0 = __builtin_amdgcn_alignbit(w1, w0, 16), a1 = __builtin_amdgcn_alignbit(w2, w1, 16), a2 = __builtin_amdgcn_alignbit(w3, w2, 16),
                   a3 = __builtin_amdgcn_alignbit(w4, w3, 16), a4 = __builtin_amdgcn_alignbit(w5, w4, 16);
    const unsigned o = hs + 32 + 16 * t4;
    lds_st<u32x4>(o, (u32x4){w0, w1, w2, w3});
    lds_st<u32x4>(o + 4160, (u32x4){a0, a1, a2, a3});
    lds_st<u32x4>(o + 8320, (u32x4){w1, w2, w3, w4});
    lds_st<u32x4>(o + 12480, (u32x4){a1, a2, a3, a4});
    if (t4 == 0) {
      const u32x4 z = {0, 0, 0, 0};
#pragma unroll
      for (int sg = 0; sg < 4; ++sg) lds_st<u32x4>(hs + sg * 4160, z);
      lds_st<u32x4>(hs + 16, z);
      lds_st<u32x4>(hs + 4160 + 16, (u32x4){0, 0, 0, w0 << 16});
      lds_st<u32x4>(hs + 8320 + 16, (u32x4){0, 0, 0, w0});
      lds_st<u32x4>(hs + 12480 + 16, (u32x4){0, 0, w0 << 16, a0});
    }
    if (t4 < 30) lds_st<u32x4>(up + 16 * t4, (u32x4){0, 0, 0, 0});
    lds_st<u32x4>(up + 480 + 16 * t4, u4);
  }
  bar();
  const int i = lane & 15, kg = lane >> 4, sg = (i + 1) & 3;
  f32x4 acc[8] = {};
#pragma unroll 1
  for (int rr = 0; rr < 2; ++rr) {
    const int rho = 2 * wq + rr;
    const unsigned ga = up + 448 - 64 * rho + 32 * i + 16 * kg;
    const unsigned fa = hs + sg * 4160 + 64 * rho + 2 * (25 + i - 8 * kg - sg);
    bf16x8 G[8];
#pragma unroll
    for (int b = 0; b < 8; ++b) G[b] = lds_ld<bf16x8>(ga + 512 * b);
#pragma unroll
    for (int a = 0; a < 8; ++a) {
      const bf16x8 Fa = cat(lds_ld<s16x4>(fa + 512 * a), lds_ld<s16x4>(fa + 512 * a + 8));
#pragma unroll
      for (int b = 0; b + a < 8; ++b) acc[a + b] = mfma16(Fa, G[b], acc[a + b]);
    }
  }
  bar();
  const unsigned red = lb + CV_RED + chl * 24576;
#pragma unroll
  for (int o = 1; o < 4; ++o) {
    const int dst = (wq + o) & 3;
    f32x4 t0, t1;
    t0 = dst == 0 ? acc[0] : dst == 1 ? acc[2] : dst == 2 ? acc[4] : acc[6];
    t1 = dst == 0 ? acc[1] : dst == 1 ? acc[3] : dst == 2 ? acc[5] : acc[7];
    lds_st<f32x4>(red + (dst * 3 + (o - 1)) * 2048 + lane * 16, t0);
    lds_st<f32x4>(red + (dst * 3 + (o - 1)) * 2048 + 1024 + lane * 16, t1);
  }
  f32x4 y0 = wq == 0 ? acc[0] : wq == 1 ? acc[2] : wq == 2 ? acc[4] : acc[6];
  f32x4 y1 = wq == 0 ? acc[1] : wq == 1 ? acc[3] : wq == 2 ? acc[5] : acc[7];
  bar();
#pragma unroll
  for (int o = 0; o < 3; ++o) {
    y0 += lds_ld<f32x4>(red + (wq * 3 + o) * 2048 + lane * 16);
    y1 += lds_ld<f32x4>(red + (wq * 3 + o) * 2048 + 1024 + lane * 16);
  }
  {
    const unsigned ua = up + 480 + 1024 * wq + 32 * i + 16 * (kg >> 1) + 8 * (1 - (kg & 1));
    const u32x2 ua0 = lds_ld<u32x2>(ua), ua1 = lds_ld<u32x2>(ua + 512);
    y0[0] = fmaf(dv, bf_hi(ua0[1]), y0[0]); y0[1] = fmaf(dv, bf_lo(ua0[1]), y0[1]); y0[2] = fmaf(dv, bf_hi(ua0[0]), y0[2]); y0[3] = fmaf(dv, bf_lo(ua0[0]), y0[3]);
    y1[0] = fmaf(dv, bf_hi(ua1[1]), y1[0]); y1[1] = fmaf(dv, bf_lo(ua1[1]), y1[1]); y1[2] = fmaf(dv, bf_hi(ua1[0]), y1[2]); y1[3] = fmaf(dv, bf_lo(ua1[0]), y1[3]);
  }
  unsigned d0 = pk_bf16(y0[0], y0[1]), d1 = pk_bf16(y0[2], y0[3]), e0 = pk_bf16(y1[0], y1[1]), e1 = pk_bf16(y1[2], y1[3]);
  {
    auto s0 = __builtin_amdgcn_permlane16_swap(d0, e0, false, false);
    auto s1 = __builtin_amdgcn_permlane16_swap(d1, e1, false, false);
    const u32x4 o = {s0[0], s1[0], s0[1], s1[1]};
    __builtin_amdgcn_raw_buffer_store_b128(o, ry, ch * 4096 + 512 * (2 * wq + (kg & 1)) + 32 * i + 8 * (kg & 2), 0, 16);
  }
  wait_vm<0>();
  bar();
  if (threadIdx.x == 0) post_flag(p.ws + WS_FY + mi * 128, tag);
}

DI void g2_stage(rsrc_t rw, unsigned vw, int db, int kq, int th, int grp, int rot, unsigned lb) {
#pragma unroll
  for (int n = 0; n < 4; ++n) {
    int st = 4 * grp + n + rot; st -= st >= 28 ? 28 : 0;
    dma16(rw, vw, (unsigned)((28 * kq + st) * 16 + 8 * th) * 896u, lb + (grp & 3) * 32768 + kq * 8192 + n * 2048 + th * 1024);
  }
}
DI void phase_gemm2(const Params& p, unsigned lb, int x, int s, int w, int lane, unsigned tag, bool wait) {
  const int i2 = s / 7, db = s % 7, th = w >> 2, kq = w & 3, rot = 4 * db;
  const rsrc_t rw = mk_rsrc(p.wd, F_ * D_ * 2), rh = mk_rsrc(p.ws + WS_H, 7340032), ry = mk_rsrc(p.ws + WS_YT, D_ * S_ * 2);
  const rsrc_t rq = mk_rsrc(p.q, S_ * D_ * 2), rs = mk_rsrc(p.ws + WS_STATS, S_ * 16), ro = mk_rsrc(p.out, S_ * D_ * 2);
  const int L3 = lane >> 3, L7 = lane & 7;
  const unsigned vw = L3 * 896 + db * 128 + 16 * (L7 ^ (((L3 >> 1) & 1) << 2));
  g2_stage(rw, vw, db, kq, th, 0, rot, lb);
  g2_stage(rw, vw, db, kq, th, 1, rot, lb);
  g2_stage(rw, vw, db, kq, th, 2, rot, lb);
  if (wait) { if (w == 0) poll_flags(p.ws + WS_FH + x * 28 * 128, 28, tag, lane); bar(); }
  TS(10);
  g2_stage(rw, vw, db, kq, th, 3, rot, lb);
  const int TT = 8 * x + 2 * i2 + th;
  const unsigned hb = (TT * 112 + 28 * kq) * 1024;
  const unsigned vh = lane * 16;
  u32x4 hb0, hb1, hb2, hb3;
#define HLOAD(dst, st_) { int st = (st_) + rot; st -= st >= 28 ? 28 : 0; asm volatile("buffer_load_dwordx4 %0, %1, %2, %3 offen sc1" : "=v"(dst) : "v"(vh), "s"(rh), "s"(hb + st * 1024) : "memory"); }
  HLOAD(hb0, 0) HLOAD(hb1, 1) HLOAD(hb2, 2) HLOAD(hb3, 3)
  const int hh = lane >> 5, blk = (lane >> 4) & 1, q = (lane & 15) >> 2, pp = lane & 3;
  unsigned aa[2];
#pragma unroll
  for (int dt = 0; dt < 2; ++dt) aa[dt] = lb + kq * 8192 + (4 * hh + q) * 128 + 64 * (dt ^ ((q >> 1) & 1)) + 32 * blk + 8 * pp;
  f32x16 acc[2] = {};
#define G2_STEP(hreg, n, so, NEXT, st_) { \
    asm volatile("s_waitcnt vmcnt(%1)" : "+v"(hreg) : "i"(NEXT) : "memory"); \
    const bf16x8 b = __builtin_bit_cast(bf16x8, hreg); \
    const bf16x8 a0 = cat(lds_tr(aa[0] + (so) + (n) * 2048), lds_tr(aa[0] + (so) + (n) * 2048 + 1024)); \
    const bf16x8 a1 = cat(lds_tr(aa[1] + (so) + (n) * 2048), lds_tr(aa[1] + (so) + (n) * 2048 + 1024)); \
    acc[0] = mfma32(a0, b, acc[0]); acc[1] = mfma32(a1, b, acc[1]); \
    asm volatile("" : "+v"(acc[0]), "+v"(acc[1]));   \
    if (st_ < 28) HLOAD(hreg, st_) }
#pragma unroll
  for (int g = 0; g < 7; ++g) {
    wait_vm<4>();
    bar();
    const unsigned so = (g & 3) * 32768;
    const int nx = 4 * g + 4;
    if (g >= 1 && g < 4) {
      g2_stage(rw, vw, db, kq, th, g + 3, rot, lb);
      G2_STEP(hb0, 0, so, 7, nx) G2_STEP(hb1, 1, so, 7, nx + 1) G2_STEP(hb2, 2, so, 7, nx + 2) G2_STEP(hb3, 3, so, 7, nx + 3)
    } else if (g < 6) {
      G2_STEP(hb0, 0, so, 3, nx) G2_STEP(hb1, 1, so, 3, nx + 1) G2_STEP(hb2, 2, so, 3, nx + 2) G2_STEP(hb3, 3, so, 3, nx + 3)
    } else {
      G2_STEP(hb0, 0, so, 3, 28) G2_STEP(hb1, 1, so, 2, 28) G2_STEP(hb2, 2, so, 1, 28) G2_STEP(hb3, 3, so, 0, 28)
    }
  }
  TS(11);
  const int tk = 16 * (w >> 1) + (lane & 15), ck = 4 * (w & 1) + (lane >> 4), tok = 256 * x + 64 * i2 + tk;
  const u32x4 xq = __builtin_amdgcn_raw_buffer_load_b128(rq, tok * 896 + db * 128 + ck * 16, 0, 0);
  if (wait) { if (w == 0) { poll_flags(p.ws + WS_FY + db * 32 * 128, 32, tag, lane); } }
  bar();
  const u32x4 yv = __builtin_amdgcn_raw_buffer_load_b128(ry, (64 * db + (threadIdx.x >> 3)) * 4096 + (256 * x + 64 * i2) * 2 + (threadIdx.x & 7) * 16, 0, 16);
  float mean, rstd;
  stats_wait(rs, tok * 16, tag, mean, rstd);
  const unsigned pb = lb, yb = lb + 73728;
  {
    const int r = lane & 31;
#pragma unroll
    for (int dt = 0; dt < 2; ++dt)
#pragma unroll
      for (int G = 0; G < 4; ++G) {
        const f32x4 t = {acc[dt][4 * G], acc[dt][4 * G + 1], acc[dt][4 * G + 2], acc[dt][4 * G + 3]};
        lds_st<f32x4>(pb + kq * 17408 + (32 * th + r) * 272 + (32 * dt + 8 * G + 4 * hh) * 4, t);
      }
  }
  lds_st<u32x4>(yb + (threadIdx.x >> 3) * 144 + (threadIdx.x & 7) * 16, yv);
  bar();
  f32x4 o0 = {}, o1 = {};
#pragma unroll
  for (int k4 = 0; k4 < 4; ++k4) {
    o0 += lds_ld<f32x4>(pb + k4 * 17408 + tk * 272 + ck * 32);
    o1 += lds_ld<f32x4>(pb + k4 * 17408 + tk * 272 + ck * 32 + 16);
  }
  const unsigned ya = yb + (8 * ck + q) * 144 + (16 * (w >> 1) + 4 * pp) * 2;
  const s16x4 y0 = lds_tr(ya), y1 = lds_tr(ya + 4 * 144);
  float res[8];
#pragma unroll
  for (int e = 0; e < 8; ++e) {
    const float o = e < 4 ? o0[e & 3] : o1[e & 3];
    const float xv = (e & 1) ? bf_hi(xq[e >> 1]) : bf_lo(xq[e >> 1]);
    const float yy = __uint_as_float(((unsigned)(u16)(e < 4 ? y0[e & 3] : y1[e & 3])) << 16);
    const float g = o * __builtin_amdgcn_rcpf(1.f + __builtin_amdgcn_exp2f(-1.4426950409f * o));
    res[e] = fmaf((xv - mean) * rstd * yy, g, xv);
  }
  const u32x4 ov = {pk_bf16(res[0], res[1]), pk_bf16(res[2], res[3]), pk_bf16(res[4], res[5]), pk_bf16(res[6], res[7])};
  __builtin_amdgcn_raw_buffer_store_b128(ov, ro, tok * 896 + db * 128 + ck * 16, 0, 0);
  TS(12);
}

template <int MASK>
__global__ __launch_bounds__(512, 2) void h3_geglu_kernel(Params p) {
  __shared__ __attribute__((aligned(16))) char smem[LDS_BYTES];
  const unsigned lb = (unsigned)(size_t)(LAS char*)smem;
  const int tid = threadIdx.x, lane = tid & 63, w = __builtin_amdgcn_readfirstlane(tid >> 6);
  const int b = blockIdx.x, x = b & 7, s = b >> 3;
  TS(0);
  unsigned tag;
  if (MASK == 15) tag = *(const GAS unsigned*)(p.ws + WS_EPOCH) + 1u; else tag = 0x1234567u;
  tag = __builtin_amdgcn_readfirstlane(tag);
  if (s < 28) {
    if (MASK & 1) phase_gemm1(p, lb, x, s, w, lane, tag);
    if (MASK & 4) phase_conv(p, lb, x * 28 + s, w, lane, tag, MASK == 15);
    TS(9);
    if (MASK & 8) { if (MASK & 4) bar(); phase_gemm2(p, lb, x, s, w, lane, tag, MASK == 15); }
  } else {
    const int pi = (s - 28) * 8 + x;
    if (MASK & 2) phase_prep(p, pi, tid, tag);
    if (MASK == 15 && pi == 0 && w == 0) {
#pragma unroll 1
      for (int n = 0; n < 4; ++n) poll_flags(p.ws + WS_FH + n * 56 * 128, 56, tag, lane);
      poll_flags(p.ws + WS_FU, 32, tag, lane);
      if (lane == 0) post_flag(p.ws + WS_EPOCH, tag);
    }
  }
  TS(15);
}

extern "C" void kernel_launch(void* const* d_in, const int* in_sizes, int n_in, void* d_out, int out_size, void* d_ws, size_t ws_size, hipStream_t stream) {
  Params p;
  p.q = (const u16*)d_in[0]; p.k = (const u16*)d_in[1]; p.v = (const u16*)d_in[2]; p.sk = (const u16*)d_in[3];
  p.h = (const u16*)d_in[4]; p.dv = (const u16*)d_in[5];
  p.wg = (const u16*)d_in[7]; p.wu = (const u16*)d_in[8]; p.wd = (const u16*)d_in[9];
  p.out = (u16*)d_out; p.ws = (char*)d_ws;
  h3_geglu_kernel<15><<<256, 512, 0, stream>>>(p);
}
```

```cpp
#include <hip/hip_runtime.h>
#include <hip/hip_bf16.h>
#include <stdint.h>

typedef unsigned short u16;
typedef __attribute__((ext_vector_type(8))) short bf16x8;
typedef __attribute__((ext_vector_type(4))) short s16x4;
typedef __attribute__((ext_vector_type(4))) float f32x4;
typedef __attribute__((ext_vector_type(2))) float f32x2;
typedef __attribute__((ext_vector_type(16))) float f32x16;
typedef __attribute__((ext_vector_type(4))) unsigned u32x4;
typedef __attribute__((ext_vector_type(2))) unsigned u32x2;
typedef __attribute__((ext_vector_type(2))) __bf16 bf16x2;
typedef __amdgpu_buffer_rsrc_t rsrc_t;
#define LAS __attribute__((address_space(3)))
#define GAS __attribute__((address_space(1)))
#define DI __device__ __forceinline__

#define SPLIT 0

constexpr int S_ = 2048, D_ = 448, F_ = 1792;
constexpr unsigned WS_EPOCH = 0, WS_FU = 4096, WS_FH = 8192, WS_FY = 36864, WS_STATS = 65536;
constexpr unsigned WS_UT = 131072, WS_YT = 2097152, WS_H = 4194304, WS_END = WS_H + 7340032;
constexpr unsigned WS_DBG = 16u << 20;
constexpr int LDS_BYTES = 163840;
constexpr unsigned SPIN_CAP = 1u << 14;

struct Params {
  const u16 *q, *k, *v, *sk, *h, *dv, *wg, *wu, *wd;
  u16* out;
  char* ws;
};

DI rsrc_t mk_rsrc(const void* p, unsigned bytes) { return __builtin_amdgcn_make_buffer_rsrc((void*)p, (short)0, (int)bytes, 0x00020000); }
DI void dma16(rsrc_t r, unsigned voff, unsigned soff, unsigned lds) {
  asm volatile("s_mov_b32 m0, %0\n\ts_nop 0\n\tbuffer_load_dwordx4 %1, %2, %3 offen lds" ::"s"(lds), "v"(voff), "s"(r), "s"(soff) : "memory");
}
template <int N> DI void wait_vm() { asm volatile("s_waitcnt vmcnt(%0)" ::"i"(N) : "memory"); }
DI void bar() { asm volatile("s_waitcnt lgkmcnt(0)\n\ts_barrier" ::: "memory"); }
DI float bf_lo(unsigned x) { return __uint_as_float(x << 16); }
DI float bf_hi(unsigned x) { return __uint_as_float(x & 0xffff0000u); }
DI unsigned pk_bf16(float lo, float hi) { f32x2 t = {lo, hi}; return __builtin_bit_cast(unsigned, __builtin_convertvector(t, bf16x2)); }
DI bf16x8 lds_b128(const char* p) { return *(const LAS bf16x8*)(p); }
DI s16x4 lds_tr(unsigned a) { return __builtin_amdgcn_ds_read_tr16_b64_v4i16((LAS s16x4*)(size_t)a); }
DI bf16x8 cat(s16x4 a, s16x4 b) { return __builtin_shufflevector(a, b, 0, 1, 2, 3, 4, 5, 6, 7); }
template <class T> DI T lds_ld(unsigned a) { return *(const LAS T*)(size_t)a; }
template <class T> DI void lds_st(unsigned a, T v) { *(LAS T*)(size_t)a = v; }
DI f32x16 mfma32(bf16x8 a, bf16x8 b, f32x16 c) { return __builtin_amdgcn_mfma_f32_32x32x16_bf16(a, b, c, 0, 0, 0); }
DI f32x4 mfma16(bf16x8 a, bf16x8 b, f32x4 c) { return __builtin_amdgcn_mfma_f32_16x16x32_bf16(a, b, c, 0, 0, 0); }
DI unsigned long long rtime() { return __builtin_amdgcn_s_memrealtime(); }
DI float dpp_add(float v, const int ctrl_row_shr) { return v; }
template <int CTRL, int RM> DI float dpp_mov(float v) { return __builtin_bit_cast(float, __builtin_amdgcn_update_dpp(0, __builtin_bit_cast(int, v), CTRL, RM, 0xf, true)); }
DI float wave_sum(float v) {
  v += dpp_mov<0x111, 0xf>(v); v += dpp_mov<0x112, 0xf>(v); v += dpp_mov<0x114, 0xf>(v); v += dpp_mov<0x118, 0xf>(v);
  v += dpp_mov<0x142, 0xa>(v); v += dpp_mov<0x143, 0xc>(v);
  return __builtin_bit_cast(float, __builtin_amdgcn_readlane(__builtin_bit_cast(int, v), 63));
}
#define TS(i)

DI void poll_flags(const char* base, int n, unsigned tag, int lane) {
  const GAS unsigned* f = (const GAS unsigned*)(base + (lane < n ? lane : 0) * 128);
  for (unsigned it = 0; it < SPIN_CAP; ++it) {
    unsigned x = __hip_atomic_load(f, __ATOMIC_RELAXED, __HIP_MEMORY_SCOPE_AGENT);
    if (__all(x == tag)) break;
    __builtin_amdgcn_s_sleep(2);
  }
}
DI void post_flag(char* addr, unsigned tag) { __hip_atomic_store((GAS unsigned*)addr, tag, __ATOMIC_RELAXED, __HIP_MEMORY_SCOPE_AGENT); }

DI u32x4 row_stats(u32x4 d, unsigned tag) {
  float e[8];
#pragma unroll
  for (int i = 0; i < 4; ++i) { e[2 * i] = bf_lo(d[i]); e[2 * i + 1] = bf_hi(d[i]); }
  float s = ((e[0] + e[1]) + (e[2] + e[3])) + ((e[4] + e[5]) + (e[6] + e[7]));
  const float mean = wave_sum(s) * (1.f / D_);
  const float on = (threadIdx.x & 63) < 56 ? 1.f : 0.f;
  float ss = 0.f;
#pragma unroll
  for (int i = 0; i < 8; ++i) { const float t = e[i] - mean; ss = fmaf(t, t, ss); }
  const float var = wave_sum(ss * on) * (1.f / D_);
  const float rstd = __builtin_amdgcn_rsqf(var + 1e-5f);
  u32x4 g = {tag, __float_as_uint(mean), tag, __float_as_uint(rstd)};
  return g;
}
DI void stats_wait(rsrc_t rs, unsigned off, unsigned tag, float& mean, float& rstd) {
  for (unsigned it = 0;; ++it) {
    const u32x4 g = __builtin_amdgcn_raw_buffer_load_b128(rs, off, 0, 16);
    mean = __uint_as_float(g[1]); rstd = __uint_as_float(g[3]);
    if (__all(g[0] == tag && g[2] == tag) || it >= SPIN_CAP) break;
    __builtin_amdgcn_s_sleep(1);
  }
}

DI void phase_gemm1(const Params& p, unsigned lb, int x, int s, int w, int lane, unsigned tag) {
  const int ib = 4 * (x >> 2) + s / 7, jb = 7 * (x & 3) + s % 7;
  const rsrc_t rq = mk_rsrc(p.q, S_ * D_ * 2), rg = mk_rsrc(p.wg, D_ * F_ * 2), ru = mk_rsrc(p.wu, D_ * F_ * 2);
  const rsrc_t rs = mk_rsrc(p.ws + WS_STATS, S_ * 16), rh = mk_rsrc(p.ws + WS_H, 7340032);
  const int L3 = lane >> 3, L7 = lane & 7;
  const int gw = jb * 8 + w;
  u32x4 sr0, sr1;
  {
    const unsigned o0 = lane < 56 ? (256 * ib + gw) * 896 + 16 * lane : 0x80000000u;
    const unsigned o1 = (lane < 56 && gw < 32) ? (256 * ib + 224 + gw) * 896 + 16 * lane : 0x80000000u;
    asm volatile("buffer_load_dwordx4 %0, %2, %4, 0 offen\n\tbuffer_load_dwordx4 %1, %3, %4, 0 offen" : "=&v"(sr0), "=&v"(sr1) : "v"(o0), "v"(o1), "s"(rq) : "memory");
  }
  const unsigned vx0 = (32 * w + L3) * 896 + 16 * (L7 ^ ((lane >> 4) & 3));
  const unsigned vx1 = (32 * w + 8 + L3) * 896 + 16 * (L7 ^ (((lane >> 4) & 3) | 4));
  const int kw = 8 * w + L3;
  const unsigned vwg = kw * 3584 + jb * 128 + 32 * ((L7 >> 1) ^ (((lane >> 4) & 1) | ((w & 1) << 1))) + 16 * (L7 & 1);
  const unsigned vwu = kw * 3584 + jb * 128 + 32 * ((L7 >> 1) ^ (((lane >> 4) & 1) | (((w & 1) ^ 1) << 1))) + 16 * (L7 & 1);
  const unsigned sx = ib * 256 * 896;
  auto stage = [&](int c, unsigned slot) {
    const unsigned xo = sx + c * 128, wo = c * 64 * 3584, l = lb + slot;
    dma16(rq, vx0, xo, l + w * 4096);
    dma16(rq, vx1, xo, l + w * 4096 + 1024);
    dma16(rq, vx0, xo + 16 * 896, l + w * 4096 + 2048);
    dma16(rq, vx1, xo + 16 * 896, l + w * 4096 + 3072);
    dma16(rg, vwg, wo, l + 32768 + w * 1024);
    dma16(ru, vwu, wo, l + 40960 + w * 1024);
  };
  stage(0, 0);
  stage(1, 49152);
  const int tw = w & 3, fh = w >> 2, r = lane & 31, hh = lane >> 5;
  unsigned xa[4], wa[2], ca;
#pragma unroll
  for (int i = 0; i < 4; ++i) xa[i] = lb + (64 * tw + r) * 128 + 16 * ((2 * i + hh) ^ ((r >> 1) & 7));
  {
    const int blk = (lane >> 4) & 1, q = (lane & 15) >> 2, pp = lane & 3, m = pp >> 1, f = (q >> 1) | ((m ^ hh) << 1);
#pragma unroll
    for (int u = 0; u < 2; ++u) wa[u] = lb + 32768 + m * 8192 + (8 * hh + q) * 128 + 32 * ((2 * fh + u) ^ f) + 16 * blk + 8 * (pp & 1);
    const int m2 = w & 1, g = lane >> 4, f2 = (q >> 1) | (((m2 ^ g) & 1) << 1);
    ca = lb + 32768 + m2 * 8192 + (8 * g + q) * 128 + 32 * ((w >> 1) ^ f2) + 8 * pp;
  }
  f32x16 acc[2][2] = {};
  f32x4 cs = {};
  const bf16x8 ones = {0x3f80, 0x3f80, 0x3f80, 0x3f80, 0x3f80, 0x3f80, 0x3f80, 0x3f80};
  TS(1);
#pragma unroll
  for (int c = 0; c < 7; ++c) {
    if (c == 6) wait_vm<0>(); else if (c == 2) wait_vm<8>(); else wait_vm<6>();
    bar();
    if (c == 0) TS(2);
    const unsigned slot = (c % 3) * 49152;
    if (c < 5) stage(c + 2, ((c + 2) % 3) * 49152);
#pragma unroll
    for (int ks = 0; ks < 4; ++ks) {
      const bf16x8 b0 = lds_ld<bf16x8>(xa[ks] + slot), b1 = lds_ld<bf16x8>(xa[ks] + slot + 4096);
#pragma unroll
      for (int u = 0; u < 2; ++u) {
        const bf16x8 a = cat(lds_tr(wa[u] + slot + ks * 2048), lds_tr(wa[u] + slot + ks * 2048 + 512));
        acc[0][u] = mfma32(a, b0, acc[0][u]);
        acc[1][u] = mfma32(a, b1, acc[1][u]);
      }
      if (ks & 1) {
        const bf16x8 a = cat(lds_tr(ca + slot + (ks >> 1) * 4096), lds_tr(ca + slot + (ks >> 1) * 4096 + 512));
        cs = mfma16(a, ones, cs);
      }
    }
    if (c == 1) {
      asm volatile("" : "+v"(sr0), "+v"(sr1));
      const u32x4 g0 = row_stats(sr0, tag), g1 = row_stats(sr1, tag);
      const unsigned o0 = lane == 0 ? (256 * ib + gw) * 16 : 0x80000000u;
      const unsigned o1 = (lane == 0 && gw < 32) ? (256 * ib + 224 + gw) * 16 : 0x80000000u;
      asm volatile("buffer_store_dwordx4 %0, %2, %4, 0 offen sc1\n\tbuffer_store_dwordx4 %1, %3, %4, 0 offen sc1\n\ts_nop 1" ::"v"(g0), "v"(g1), "v"(o0), "v"(o1), "s"(rs) : "memory");
    }
  }
  TS(3);
  const unsigned csb = lb + 147456;
  if ((lane & 15) == 0) lds_st<f32x4>(csb + ((w & 1) * 64 + 16 * (w >> 1) + 4 * (lane >> 4)) * 4, cs);
  float mean[2], rstd[2];
#pragma unroll
  for (int tt = 0; tt < 2; ++tt) stats_wait(rs, (256 * ib + 64 * tw + 32 * tt + r) * 16, tag, mean[tt], rstd[tt]);
  bar();
  TS(4);
  constexpr float C0 = -2.3022081985f, C1 = -0.1029432397f;
#pragma unroll
  for (int u = 0; u < 2; ++u) {
    f32x4 cg[2], cu[2];
#pragma unroll
    for (int b = 0; b < 2; ++b) {
      cg[b] = lds_ld<f32x4>(csb + (16 * (2 * fh + u) + 8 * b + 4 * hh) * 4);
      cu[b] = lds_ld<f32x4>(csb + 256 + (16 * (2 * fh + u) + 8 * b + 4 * hh) * 4);
    }
#pragma unroll
    for (int tt = 0; tt < 2; ++tt) {
      const float rr = rstd[tt], nm = -mean[tt], k0 = rr * C0, k1 = C1 * rr * rr * rr, ir2 = __builtin_amdgcn_rcpf(rr * rr);
      float hv[8];
#pragma unroll
      for (int b = 0; b < 2; ++b)
#pragma unroll
        for (int e = 0; e < 4; ++e) {
          const float a = fmaf(nm, cg[b][e], acc[tt][u][8 * b + e]), bb = fmaf(nm, cu[b][e], acc[tt][u][8 * b + 4 + e]);
          const float z = a * fmaf(a * a, k1, k0);
          const float ex = __builtin_amdgcn_exp2f(z);
          hv[4 * b + e] = (a * bb) * __builtin_amdgcn_rcpf(fmaf(ex, ir2, ir2));
        }
      const u32x4 o = {pk_bf16(hv[0], hv[1]), pk_bf16(hv[2], hv[3]), pk_bf16(hv[4], hv[5]), pk_bf16(hv[6], hv[7])};
      __builtin_amdgcn_raw_buffer_store_b128(o, rh, ((8 * ib + 2 * tw + tt) * 112 + 4 * jb + 2 * fh + u) * 1024 + lane * 16, 0, 16);
    }
  }
  TS(5);
  wait_vm<0>();
  bar();
  if (threadIdx.x == 0) post_flag(p.ws + WS_FH + (ib * 28 + jb) * 128, tag);
  TS(6);
}

DI void phase_prep(const Params& p, int pi, int tid, unsigned tag) {
  const rsrc_t rk = mk_rsrc(p.k, S_ * D_ * 2), rv = mk_rsrc(p.v, S_ * D_ * 2), rw = mk_rsrc(p.sk, D_ * 8), ro = mk_rsrc(p.ws + WS_UT, D_ * S_ * 2);
  if (tid < 448) {
    const int cg = tid >> 3, rg = tid & 7, r0 = 64 * pi + 8 * rg;
    u32x4 kk[11], vv[8], ww[4];
#pragma unroll
    for (int n = 0; n < 11; ++n) {
      const unsigned o = (n >= 3 || r0 > 0) ? (unsigned)((r0 - 3 + n) * 896 + cg * 16) : 0x80000000u;
      kk[n] = __builtin_amdgcn_raw_buffer_load_b128(rk, o, 0, 2);
    }
#pragma unroll
    for (int n = 0; n < 8; ++n) vv[n] = __builtin_amdgcn_raw_buffer_load_b128(rv, (r0 + n) * 896 + cg * 16, 0, 2);
#pragma unroll
    for (int n = 0; n < 4; ++n) ww[n] = __builtin_amdgcn_raw_buffer_load_b128(rw, cg * 64 + n * 16, 0, 0);
#pragma unroll
    for (int c = 0; c < 8; ++c) {
      const unsigned w01 = ww[c >> 1][(c & 1) * 2], w23 = ww[c >> 1][(c & 1) * 2 + 1];
      const float w0 = bf_lo(w01), w1 = bf_hi(w01), w2 = bf_lo(w23), w3 = bf_hi(w23);
      float kc[11], uu[8];
#pragma unroll
      for (int n = 0; n < 11; ++n) kc[n] = (c & 1) ? bf_hi(kk[n][c >> 1]) : bf_lo(kk[n][c >> 1]);
#pragma unroll
      for (int n = 0; n < 8; ++n) {
        const float ks = fmaf(kc[n + 3], w0, fmaf(kc[n + 2], w1, fmaf(kc[n + 1], w2, kc[n] * w3)));
        uu[n] = ks * ((c & 1) ? bf_hi(vv[n][c >> 1]) : bf_lo(vv[n][c >> 1]));
      }
      const u32x4 o = {pk_bf16(uu[7], uu[6]), pk_bf16(uu[5], uu[4]), pk_bf16(uu[3], uu[2]), pk_bf16(uu[1], uu[0])};
      __builtin_amdgcn_raw_buffer_store_b128(o, ro, (8 * cg + c) * 4096 + r0 * 2, 0, 16);
    }
  }
  wait_vm<0>();
  bar();
  if (tid == 0) post_flag(p.ws + WS_FU + pi * 128, tag);
}

constexpr unsigned CV_BASE = 98304;
constexpr unsigned CV_UP = CV_BASE;
constexpr unsigned CV_HS = CV_BASE + 9216;
constexpr unsigned CV_RED = CV_HS;
DI void phase_conv(const Params& p, unsigned lb, int mi, int w, int lane, unsigned tag, bool wait) {
  const int chl = w >> 2, wq = w & 3, ch = 2 * mi + chl, t4 = wq * 64 + lane;
  const rsrc_t rh = mk_rsrc(p.h, D_ * S_ * 2), ru = mk_rsrc(p.ws + WS_UT, D_ * S_ * 2), ry = mk_rsrc(p.ws + WS_YT, D_ * S_ * 2);
  const u32x4 h4 = __builtin_amdgcn_raw_buffer_load_b128(rh, ch * 4096 + t4 * 16, 0, 2);
  const u32x2 h2 = __builtin_amdgcn_raw_buffer_load_b64(rh, ch * 4096 + t4 * 16 + 16, 0, 2);
  const float dv = bf_lo(p.dv[ch]);
  if (wait) { if (w == 0) poll_flags(p.ws + WS_FU, 32, tag, lane); bar(); }
  const u32x4 u4 = __builtin_amdgcn_raw_buffer_load_b128(ru, ch * 4096 + t4 * 16, 0, 16);
  const unsigned up = lb + CV_UP + chl * 4608, hs = lb + CV_HS + chl * 16640;
  {
    const unsigned w0 = h4[0], w1 = h4[1], w2 = h4[2], w3 = h4[3], w4 = h2[0], w5 = h2[1];
    const unsigned a0 = __builtin_amdgcn_alignbit(w1, w0, 16), a1 = __builtin_amdgcn_alignbit(w2, w1, 16), a2 = __builtin_amdgcn_alignbit(w3, w2, 16),
                   a3 = __builtin_amdgcn_alignbit(w4, w3, 16), a4 = __builtin_amdgcn_alignbit(w5, w4, 16);
    const unsigned o = hs + 32 + 16 * t4;
    lds_st<u32x4>(o, (u32x4){w0, w1, w2, w3});
    lds_st<u32x4>(o + 4160, (u32x4){a0, a1, a2, a3});
    lds_st<u32x4>(o + 8320, (u32x4){w1, w2, w3, w4});
    lds_st<u32x4>(o + 12480, (u32x4){a1, a2, a3, a4});
    if (t4 == 0) {
      const u32x4 z = {0, 0, 0, 0};
#pragma unroll
      for (int sg = 0; sg < 4; ++sg) lds_st<u32x4>(hs + sg * 4160, z);
      lds_st<u32x4>(hs + 16, z);
      lds_st<u32x4>(hs + 4160 + 16, (u32x4){0, 0, 0, w0 << 16});
      lds_st<u32x4>(hs + 8320 + 16, (u32x4){0, 0, 0, w0});
      lds_st<u32x4>(hs + 12480 + 16, (u32x4){0, 0, w0 << 16, a0});
    }
    if (t4 < 30) lds_st<u32x4>(up + 16 * t4, (u32x4){0, 0, 0, 0});
    lds_st<u32x4>(up + 480 + 16 * t4, u4);
  }
  bar();
  const int i = lane & 15, kg = lane >> 4, sg = (i + 1) & 3;
  f32x4 acc[8] = {};
#pragma unroll 1
  for (int rr = 0; rr < 2; ++rr) {
    const int rho = 2 * wq + rr;
    const unsigned ga = up + 448 - 64 * rho + 32 * i + 16 * kg;
    const unsigned fa = hs + sg * 4160 + 64 * rho + 2 * (25 + i - 8 * kg - sg);
    bf16x8 G[8];
#pragma unroll
    for (int b = 0; b < 8; ++b) G[b] = lds_ld<bf16x8>(ga + 512 * b);
#pragma unroll
    for (int a = 0; a < 8; ++a) {
      const bf16x8 Fa = cat(lds_ld<s16x4>(fa + 512 * a), lds_ld<s16x4>(fa + 512 * a + 8));
#pragma unroll
      for (int b = 0; b + a < 8; ++b) acc[a + b] = mfma16(Fa, G[b], acc[a + b]);
    }
  }
  bar();
  const unsigned red = lb + CV_RED + chl * 24576;
#pragma unroll
  for (int o = 1; o < 4; ++o) {
    const int dst = (wq + o) & 3;
    f32x4 t0, t1;
    t0 = dst == 0 ? acc[0] : dst == 1 ? acc[2] : dst == 2 ? acc[4] : acc[6];
    t1 = dst == 0 ? acc[1] : dst == 1 ? acc[3] : dst == 2 ? acc[5] : acc[7];
    lds_st<f32x4>(red + (dst * 3 + (o - 1)) * 2048 + lane * 16, t0);
    lds_st<f32x4>(red + (dst * 3 + (o - 1)) * 2048 + 1024 + lane * 16, t1);
  }
  f32x4 y0 = wq == 0 ? acc[0] : wq == 1 ? acc[2] : wq == 2 ? acc[4] : acc[6];
  f32x4 y1 = wq == 0 ? acc[1] : wq == 1 ? acc[3] : wq == 2 ? acc[5] : acc[7];
  bar();
#pragma unroll
  for (int o = 0; o < 3; ++o) {
    y0 += lds_ld<f32x4>(red + (wq * 3 + o) * 2048 + lane * 16);
    y1 += lds_ld<f32x4>(red + (wq * 3 + o) * 2048 + 1024 + lane * 16);
  }
  {
    const unsigned ua = up + 480 + 1024 * wq + 32 * i + 16 * (kg >> 1) + 8 * (1 - (kg & 1));
    const u32x2 ua0 = lds_ld<u32x2>(ua), ua1 = lds_ld<u32x2>(ua + 512);
    y0[0] = fmaf(dv, bf_hi(ua0[1]), y0[0]); y0[1] = fmaf(dv, bf_lo(ua0[1]), y0[1]); y0[2] = fmaf(dv, bf_hi(ua0[0]), y0[2]); y0[3] = fmaf(dv, bf_lo(ua0[0]), y0[3]);
    y1[0] = fmaf(dv, bf_hi(ua1[1]), y1[0]); y1[1] = fmaf(dv, bf_lo(ua1[1]), y1[1]); y1[2] = fmaf(dv, bf_hi(ua1[0]), y1[2]); y1[3] = fmaf(dv, bf_lo(ua1[0]), y1[3]);
  }
  unsigned d0 = pk_bf16(y0[0], y0[1]), d1 = pk_bf16(y0[2], y0[3]), e0 = pk_bf16(y1[0], y1[1]), e1 = pk_bf16(y1[2], y1[3]);
  {
    auto s0 = __builtin_amdgcn_permlane16_swap(d0, e0, false, false);
    auto s1 = __builtin_amdgcn_permlane16_swap(d1, e1, false, false);
    const u32x4 o = {s0[0], s1[0], s0[1], s1[1]};
    __builtin_amdgcn_raw_buffer_store_b128(o, ry, ch * 4096 + 512 * (2 * wq + (kg & 1)) + 32 * i + 8 * (kg & 2), 0, 16);
  }
  wait_vm<0>();
  bar();
  if (threadIdx.x == 0) post_flag(p.ws + WS_FY + mi * 128, tag);
}

DI void g2_stage(rsrc_t rw, unsigned vw, int db, int kq, int th, int grp, int rot, unsigned lb) {
#pragma unroll
  for (int n = 0; n < 4; ++n) {
    int st = 4 * grp + n + rot; st -= st >= 28 ? 28 : 0;
    dma16(rw, vw, (unsigned)((28 * kq + st) * 16 + 8 * th) * 896u, lb + (grp & 3) * 32768 + kq * 8192 + n * 2048 + th * 1024);
  }
}
DI void phase_gemm2(const Params& p, unsigned lb, int x, int s, int w, int lane, unsigned tag, bool wait) {
  const int i2 = s / 7, db = s % 7, th = w >> 2, kq = w & 3, rot = 4 * db;
  const rsrc_t rw = mk_rsrc(p.wd, F_ * D_ * 2), rh = mk_rsrc(p.ws + WS_H, 7340032), ry = mk_rsrc(p.ws + WS_YT, D_ * S_ * 2);
  const rsrc_t rq = mk_rsrc(p.q, S_ * D_ * 2), rs = mk_rsrc(p.ws + WS_STATS, S_ * 16), ro = mk_rsrc(p.out, S_ * D_ * 2);
  const int L3 = lane >> 3, L7 = lane & 7;
  const unsigned vw = L3 * 896 + db * 128 + 16 * (L7 ^ (((L3 >> 1) & 1) << 2));
  g2_stage(rw, vw, db, kq, th, 0, rot, lb);
  g2_stage(rw, vw, db, kq, th, 1, rot, lb);
  g2_stage(rw, vw, db, kq, th, 2, rot, lb);
  if (wait) { if (w == 0) poll_flags(p.ws + WS_FH + x * 28 * 128, 28, tag, lane); bar(); }
  TS(10);
  g2_stage(rw, vw, db, kq, th, 3, rot, lb);
  const int TT = 8 * x + 2 * i2 + th;
  const unsigned hb = (TT * 112 + 28 * kq) * 1024;
  const unsigned vh = lane * 16;
  u32x4 hb0, hb1, hb2, hb3;
#define HLOAD(dst, st_) { int st = (st_) + rot; st -= st >= 28 ? 28 : 0; asm volatile("buffer_load_dwordx4 %0, %1, %2, %3 offen sc1" : "=v"(dst) : "v"(vh), "s"(rh), "s"(hb + st * 1024) : "memory"); }
  HLOAD(hb0, 0) HLOAD(hb1, 1) HLOAD(hb2, 2) HLOAD(hb3, 3)
  const int hh = lane >> 5, blk = (lane >> 4) & 1, q = (lane & 15) >> 2, pp = lane & 3;
  unsigned aa[2];
#pragma unroll
  for (int dt = 0; dt < 2; ++dt) aa[dt] = lb + kq * 8192 + (4 * hh + q) * 128 + 64 * (dt ^ ((q >> 1) & 1)) + 32 * blk + 8 * pp;
  f32x16 acc[2] = {};
#define G2_STEP(hreg, n, so, NEXT, st_) { \
    asm volatile("s_waitcnt vmcnt(%1)" : "+v"(hreg) : "i"(NEXT) : "memory"); \
    const bf16x8 b = __builtin_bit_cast(bf16x8, hreg); \
    const bf16x8 a0 = cat(lds_tr(aa[0] + (so) + (n) * 2048), lds_tr(aa[0] + (so) + (n) * 2048 + 1024)); \
    const bf16x8 a1 = cat(lds_tr(aa[1] + (so) + (n) * 2048), lds_tr(aa[1] + (so) + (n) * 2048 + 1024)); \
    acc[0] = mfma32(a0, b, acc[0]); acc[1] = mfma32(a1, b, acc[1]); \
    asm volatile("" : "+v"(acc[0]), "+v"(acc[1]));   \
    if (st_ < 28) HLOAD(hreg, st_) }
#pragma unroll
  for (int g = 0; g < 7; ++g) {
    wait_vm<4>();
    bar();
    const unsigned so = (g & 3) * 32768;
    const int nx = 4 * g + 4;
    if (g >= 1 && g < 4) {
      g2_stage(rw, vw, db, kq, th, g + 3, rot, lb);
      G2_STEP(hb0, 0, so, 7, nx) G2_STEP(hb1, 1, so, 7, nx + 1) G2_STEP(hb2, 2, so, 7, nx + 2) G2_STEP(hb3, 3, so, 7, nx + 3)
    } else if (g < 6) {
      G2_STEP(hb0, 0, so, 3, nx) G2_STEP(hb1, 1, so, 3, nx + 1) G2_STEP(hb2, 2, so, 3, nx + 2) G2_STEP(hb3, 3, so, 3, nx + 3)
    } else {
      G2_STEP(hb0, 0, so, 3, 28) G2_STEP(hb1, 1, so, 2, 28) G2_STEP(hb2, 2, so, 1, 28) G2_STEP(hb3, 3, so, 0, 28)
    }
  }
  TS(11);
  const int tk = 16 * (w >> 1) + (lane & 15), ck = 4 * (w & 1) + (lane >> 4), tok = 256 * x + 64 * i2 + tk;
  const u32x4 xq = __builtin_amdgcn_raw_buffer_load_b128(rq, tok * 896 + db * 128 + ck * 16, 0, 0);
  if (wait) { if (w == 0) { poll_flags(p.ws + WS_FY + db * 32 * 128, 32, tag, lane); } }
  bar();
  const u32x4 yv = __builtin_amdgcn_raw_buffer_load_b128(ry, (64 * db + (threadIdx.x >> 3)) * 4096 + (256 * x + 64 * i2) * 2 + (threadIdx.x & 7) * 16, 0, 16);
  float mean, rstd;
  stats_wait(rs, tok * 16, tag, mean, rstd);
  const unsigned pb = lb, yb = lb + 73728;
  {
    const int r = lane & 31;
#pragma unroll
    for (int dt = 0; dt < 2; ++dt)
#pragma unroll
      for (int G = 0; G < 4; ++G) {
        const f32x4 t = {acc[dt][4 * G], acc[dt][4 * G + 1], acc[dt][4 * G + 2], acc[dt][4 * G + 3]};
        lds_st<f32x4>(pb + kq * 17408 + (32 * th + r) * 272 + (32 * dt + 8 * G + 4 * hh) * 4, t);
      }
  }
  lds_st<u32x4>(yb + (threadIdx.x >> 3) * 144 + (threadIdx.x & 7) * 16, yv);
  bar();
  f32x4 o0 = {}, o1 = {};
#pragma unroll
  for (int k4 = 0; k4 < 4; ++k4) {
    o0 += lds_ld<f32x4>(pb + k4 * 17408 + tk * 272 + ck * 32);
    o1 += lds_ld<f32x4>(pb + k4 * 17408 + tk * 272 + ck * 32 + 16);
  }
  const unsigned ya = yb + (8 * ck + q) * 144 + (16 * (w >> 1) + 4 * pp) * 2;
  const s16x4 y0 = lds_tr(ya), y1 = lds_tr(ya + 4 * 144);
  float res[8];
#pragma unroll
  for (int e = 0; e < 8; ++e) {
    const float o = e < 4 ? o0[e & 3] : o1[e & 3];
    const float xv = (e & 1) ? bf_hi(xq[e >> 1]) : bf_lo(xq[e >> 1]);
    const float yy = __uint_as_float(((unsigned)(u16)(e < 4 ? y0[e & 3] : y1[e & 3])) << 16);
    const float g = o * __builtin_amdgcn_rcpf(1.f + __builtin_amdgcn_exp2f(-1.4426950409f * o));
    res[e] = fmaf((xv - mean) * rstd * yy, g, xv);
  }
  const u32x4 ov = {pk_bf16(res[0], res[1]), pk_bf16(res[2], res[3]), pk_bf16(res[4], res[5]), pk_bf16(res[6], res[7])};
  __builtin_amdgcn_raw_buffer_store_b128(ov, ro, tok * 896 + db * 128 + ck * 16, 0, 0);
  TS(12);
}

template <int MASK>
__global__ __launch_bounds__(512, 2) void h3_geglu_kernel(Params p) {
  __shared__ __attribute__((aligned(16))) char smem[LDS_BYTES];
  const unsigned lb = (unsigned)(size_t)(LAS char*)smem;
  const int tid = threadIdx.x, lane = tid & 63, w = __builtin_amdgcn_readfirstlane(tid >> 6);
  const int b = blockIdx.x, x = b & 7, s = b >> 3;
  const unsigned long long t_start = rtime();
  unsigned tag;
  if (MASK == 15) tag = *(const GAS unsigned*)(p.ws + WS_EPOCH) + 1u; else tag = 0x1234567u;
  tag = __builtin_amdgcn_readfirstlane(tag);
  if (blockIdx.x == 0 && threadIdx.x == 0) __hip_atomic_store((GAS unsigned long long*)(p.ws + 2048), ((unsigned long long)tag << 32) | (unsigned)t_start, __ATOMIC_RELAXED, __HIP_MEMORY_SCOPE_AGENT);
  if (s < 28) {
    if (MASK & 1) phase_gemm1(p, lb, x, s, w, lane, tag);
    if (MASK & 4) phase_conv(p, lb, x * 28 + s, w, lane, tag, MASK == 15);
    TS(9);
    if (MASK & 8) { if (MASK & 4) bar(); phase_gemm2(p, lb, x, s, w, lane, tag, MASK == 15); }
  } else {
    const int pi = (s - 28) * 8 + x;
    if (MASK & 2) phase_prep(p, pi, tid, tag);
    if (MASK == 15 && pi == 0 && w == 0) {
#pragma unroll 1
      for (int n = 0; n < 4; ++n) poll_flags(p.ws + WS_FH + n * 56 * 128, 56, tag, lane);
      poll_flags(p.ws + WS_FU, 32, tag, lane);
      if (lane == 0) post_flag(p.ws + WS_EPOCH, tag);
    }
  }
  {
    unsigned long long g = 0;
    for (int it = 0; it < 100000; ++it) { g = __hip_atomic_load((GAS unsigned long long*)(p.ws + 2048), __ATOMIC_RELAXED, __HIP_MEMORY_SCOPE_AGENT); if ((unsigned)(g >> 32) == tag) break; }
    const unsigned t0 = (unsigned)g;
    for (int it = 0; it < 1000000 && (unsigned)rtime() - t0 < 6000u; ++it) __builtin_amdgcn_s_sleep(1);
  }
}

extern "C" void kernel_launch(void* const* d_in, const int* in_sizes, int n_in, void* d_out, int out_size, void* d_ws, size_t ws_size, hipStream_t stream) {
  Params p;
  p.q = (const u16*)d_in[0]; p.k = (const u16*)d_in[1]; p.v = (const u16*)d_in[2]; p.sk = (const u16*)d_in[3];
  p.h = (const u16*)d_in[4]; p.dv = (const u16*)d_in[5];
  p.wg = (const u16*)d_in[7]; p.wu = (const u16*)d_in[8]; p.wd = (const u16*)d_in[9];
  p.out = (u16*)d_out; p.ws = (char*)d_ws;
  h3_geglu_kernel<15><<<256, 512, 0, stream>>>(p);
}
```

```cpp
#include <hip/hip_runtime.h>
#include <hip/hip_bf16.h>
#include <stdint.h>

typedef unsigned short u16;
typedef __attribute__((ext_vector_type(8))) short bf16x8;
typedef __attribute__((ext_vector_type(4))) short s16x4;
typedef __attribute__((ext_vector_type(4))) float f32x4;
typedef __attribute__((ext_vector_type(2))) float f32x2;
typedef __attribute__((ext_vector_type(16))) float f32x16;
typedef __attribute__((ext_vector_type(4))) unsigned u32x4;
typedef __attribute__((ext_vector_type(2))) unsigned u32x2;
typedef __attribute__((ext_vector_type(2))) __bf16 bf16x2;
typedef __amdgpu_buffer_rsrc_t rsrc_t;
#define HSPOL " sc1"
#define LAS __attribute__((address_space(3)))
#define GAS __attribute__((address_space(1)))
#define DI __device__ __forceinline__

#define OPT_OWN 0
#define KVT 1
#define KVFIRST 0
#define WDLATE 1
#define PAUX 2
#define USTAUX 16
#define HSLEEP 10
#define LPRIO 0
#define WARM 1
#define UPRIO 2
#define KVPOL " nt"
#define WPRIO 0
#define XPRIO 0
#define PFN 17

constexpr int S_ = 2048, D_ = 448, F_ = 1792;
constexpr unsigned WS_EPOCH = 0, WS_FH = 8192, WS_FY = 36864, WS_STATS = 65536, WS_FU = 98304, WS_CS = 2031616;
constexpr unsigned TROW = 4224;
constexpr unsigned WS_UT = 131072, WS_YT = 2097152, WS_H = 4194304, WS_END = WS_H + 7340032;
constexpr unsigned WS_DBG = 16u << 20;
constexpr int LDS_BYTES = 163840;
constexpr unsigned SPIN_CAP = 1u << 14;

struct Params {
  const u16 *q, *k, *v, *sk, *h, *dv, *wg, *wu, *wd;
  u16* out;
  char* ws;
};

DI rsrc_t mk_rsrc(const void* p, unsigned bytes) { return __builtin_amdgcn_make_buffer_rsrc((void*)p, (short)0, (int)bytes, 0x00020000); }
DI void dma16(rsrc_t r, unsigned voff, unsigned soff, unsigned lds) {
  asm volatile("s_mov_b32 m0, %0\n\ts_nop 0\n\tbuffer_load_dwordx4 %1, %2, %3 offen lds" ::"s"(lds), "v"(voff), "s"(r), "s"(soff) : "memory");
}
DI void touch(rsrc_t r, unsigned voff, unsigned lds) {
  asm volatile("s_mov_b32 m0, %0\n\ts_nop 0\n\tbuffer_load_dword %1, %2, 0 offen lds" ::"s"(lds), "v"(voff), "s"(r) : "memory");
}
template <int NT> DI void touch_p(rsrc_t r, unsigned voff, unsigned lds) {
  if (NT) asm volatile("s_mov_b32 m0, %0\n\ts_nop 0\n\tbuffer_load_dword %1, %2, 0 offen nt lds" ::"s"(lds), "v"(voff), "s"(r) : "memory");
  else touch(r, voff, lds);
}
DI void touch_g(const char* a, unsigned lds) {
  asm volatile("s_mov_b32 m0, %0\n\ts_nop 0\n\tglobal_load_lds_dword %1, off" ::"s"(lds), "v"(a) : "memory");
}
template <unsigned LA, unsigned SA>
DI void dma16c(rsrc_t r, unsigned voff, unsigned sbase, unsigned lbase) {
  unsigned t;
  asm volatile("s_add_u32 m0, %2, %5\n\ts_add_u32 %0, %3, %6\n\tbuffer_load_dwordx4 %1, %4, %0 offen lds"
               : "=&s"(t) : "v"(voff), "s"(lbase), "s"(sbase), "s"(r), "i"(LA), "i"(SA) : "memory", "scc");
}
template <int N> DI void wait_vm() { asm volatile("s_waitcnt vmcnt(%0)" ::"i"(N) : "memory"); }
DI void bar() { asm volatile("s_waitcnt lgkmcnt(0)\n\ts_barrier" ::: "memory"); }
DI float bf_lo(unsigned x) { return __uint_as_float(x << 16); }
DI float bf_hi(unsigned x) { return __uint_as_float(x & 0xffff0000u); }
DI unsigned pk_bf16(float lo, float hi) { f32x2 t = {lo, hi}; return __builtin_bit_cast(unsigned, __builtin_convertvector(t, bf16x2)); }
DI bf16x8 lds_b128(const char* p) { return *(const LAS bf16x8*)(p); }
DI s16x4 lds_tr(unsigned a) { return __builtin_amdgcn_ds_read_tr16_b64_v4i16((LAS s16x4*)(size_t)a); }
DI bf16x8 cat(s16x4 a, s16x4 b) { return __builtin_shufflevector(a, b, 0, 1, 2, 3, 4, 5, 6, 7); }
template <class T> DI T lds_ld(unsigned a) { return *(const LAS T*)(size_t)a; }
template <class T> DI void lds_st(unsigned a, T v) { *(LAS T*)(size_t)a = v; }
DI f32x16 mfma32(bf16x8 a, bf16x8 b, f32x16 c) { return __builtin_amdgcn_mfma_f32_32x32x16_bf16(a, b, c, 0, 0, 0); }
DI f32x4 mfma16(bf16x8 a, bf16x8 b, f32x4 c) { return __builtin_amdgcn_mfma_f32_16x16x32_bf16(a, b, c, 0, 0, 0); }
DI unsigned long long rtime() { return __builtin_amdgcn_s_memrealtime(); }
DI float dpp_add(float v, const int ctrl_row_shr) { return v; }
template <int CTRL, int RM> DI float dpp_mov(float v) { return __builtin_bit_cast(float, __builtin_amdgcn_update_dpp(0, __builtin_bit_cast(int, v), CTRL, RM, 0xf, true)); }
DI float wave_sum(float v) {
  v += dpp_mov<0x111, 0xf>(v); v += dpp_mov<0x112, 0xf>(v); v += dpp_mov<0x114, 0xf>(v); v += dpp_mov<0x118, 0xf>(v);
  v += dpp_mov<0x142, 0xa>(v); v += dpp_mov<0x143, 0xc>(v);
  return __builtin_bit_cast(float, __builtin_amdgcn_readlane(__builtin_bit_cast(int, v), 63));
}
#define TS(i)
#define TSL(i)
#define TSL_DECL
#define TSL_DUMP()
#define TS2(i)

DI void poll_flags(const char* base, int n, unsigned tag, int lane) {
  const GAS unsigned* f = (const GAS unsigned*)(base + (lane < n ? lane : 0) * 128);
  for (unsigned it = 0; it < SPIN_CAP; ++it) {
    unsigned x = __hip_atomic_load(f, __ATOMIC_RELAXED, __HIP_MEMORY_SCOPE_AGENT);
    if (__all(x == tag)) break;
    __builtin_amdgcn_s_sleep(2);
  }
}
DI void poll_flags_slow(const char* base, int n, unsigned tag, int lane) {
  const GAS unsigned* f = (const GAS unsigned*)(base + (lane < n ? lane : 0) * 128);
  for (unsigned it = 0; it < SPIN_CAP; ++it) {
    unsigned x = __hip_atomic_load(f, __ATOMIC_RELAXED, __HIP_MEMORY_SCOPE_AGENT);
    if (__all(x == tag)) break;
    __builtin_amdgcn_s_sleep(32);
  }
}
DI void post_flag(char* addr, unsigned tag) { __hip_atomic_store((GAS unsigned*)addr, tag, __ATOMIC_RELAXED, __HIP_MEMORY_SCOPE_AGENT); }

DI bool flags_ok(unsigned v, unsigned tag) { return __all(v == tag); }
DI unsigned flag_off(int n, int lane) { return (lane < n ? lane : 0) * 128; }
#define SB() __builtin_amdgcn_sched_barrier(0)
template <int V> struct IC { static constexpr int value = V; };
DI void asm_pin(unsigned& v) { asm volatile("" : "+v"(v)); }
DI float rbf(float x) { return __uint_as_float(pk_bf16(x, 0.f) << 16); }

DI f32x2 rbf2(f32x2 v) { const unsigned u = pk_bf16(v[0], v[1]); return (f32x2){bf_lo(u), bf_hi(u)}; }
DI u32x4 row_stats(u32x4 d, unsigned tag) {
  float e[8];
#pragma unroll
  for (int i = 0; i < 4; ++i) { e[2 * i] = bf_lo(d[i]); e[2 * i + 1] = bf_hi(d[i]); }
  float s = ((e[0] + e[1]) + (e[2] + e[3])) + ((e[4] + e[5]) + (e[6] + e[7]));
  const float mean = wave_sum(s) * (1.f / D_);
  const float on = (threadIdx.x & 63) < 56 ? 1.f : 0.f;
  float ss = 0.f;
#pragma unroll
  for (int i = 0; i < 8; ++i) { const float t = e[i] - mean; ss = fmaf(t, t, ss); }
  const float var = wave_sum(ss * on) * (1.f / D_);
  const float den = rbf(__builtin_amdgcn_sqrtf(rbf(rbf(var) + 1e-5f)));
  u32x4 g = {tag, __float_as_uint(rbf(mean)), tag, __float_as_uint(__builtin_amdgcn_rcpf(den))};
  return g;
}
DI void stats_wait(rsrc_t rs, unsigned off, unsigned tag, float& mean, float& rstd) {
  for (unsigned it = 0;; ++it) {
    const u32x4 g = __builtin_amdgcn_raw_buffer_load_b128(rs, off, 0, 16);
    mean = __uint_as_float(g[1]); rstd = __uint_as_float(g[3]);
    if (__all(g[0] == tag && g[2] == tag) || it >= SPIN_CAP) break;
    __builtin_amdgcn_s_sleep(1);
  }
}

constexpr unsigned CV_BASE = 98304;
constexpr unsigned CV_UP = CV_BASE;
constexpr unsigned CV_HS = CV_BASE + 9216;
constexpr unsigned CV_RED = CV_HS;
constexpr unsigned L_UT = 147456, L_SK = 155648;
constexpr unsigned L_CS = 157696, L_OK = 158720, L_CNT = 158728, L_TOUCH = 158976;

#define HPOL " sc1"
#define HDEPTH 4
constexpr int HD = HDEPTH;
struct Ctx {
  unsigned tag; int hok; float dv; u32x4 hr[HD], xq, sg;
};
DI void g2_stage(rsrc_t rw, unsigned vw, int kq, int th, int grp, int rot, unsigned lb) {
#pragma unroll
  for (int n = 0; n < 4; ++n) {
    int st = 4 * grp + n + rot; st -= st >= 28 ? 28 : 0;
    dma16(rw, vw, (unsigned)((28 * kq + st) * 16 + 8 * th) * 896u, lb + (grp & 3) * 32768 + kq * 8192 + n * 2048 + th * 1024);
  }
}
DI void g2_piece(rsrc_t rw, unsigned vw, int kq, int th, int step, int rot, unsigned lb) {
  int st = step + rot; st -= st >= 28 ? 28 : 0;
  dma16(rw, vw, (unsigned)((28 * kq + st) * 16 + 8 * th) * 896u, lb + ((step >> 2) & 3) * 32768 + kq * 8192 + (step & 3) * 2048 + th * 1024);
}
DI void h_load(const Params& p, u32x4& dst, unsigned hb, int step, int rot, unsigned vh) {
  int st = step + rot; st -= st >= 28 ? 28 : 0;
  asm volatile("buffer_load_dwordx4 %0, %1, %2, %3 offen" HPOL : "=v"(dst) : "v"(vh), "s"(mk_rsrc(p.ws, WS_END)), "s"(hb + st * 1024) : "memory");
}
DI int g2_rot(int s);
DI void h_first(const Params& p, int x, int s, int w, int lane, Ctx& cx) {
  const unsigned hb = WS_H + ((8 * x + 2 * (s / 7) + (w >> 2)) * 112 + 28 * (w & 3)) * 1024;
#pragma unroll
  for (int n = 0; n < HD; ++n) h_load(p, cx.hr[n], hb, n, g2_rot(s), lane * 16);
}
DI unsigned g2_vw(int lane, int db) { const int L3 = lane >> 3, L7 = lane & 7; return L3 * 896 + db * 128 + 16 * (L7 ^ (((L3 >> 1) & 1) << 2)); }
DI int g2_rot(int s) { const int r = 4 * (s % 7) + 7 * (s / 7); return r >= 28 ? r - 28 : r; }

DI void phase_gemm1(const Params& p, unsigned lb, int x, int s, int w, int lane, Ctx& cx) {
  const int ib = 4 * (x >> 2) + s / 7, jb = 7 * (x & 3) + s % 7;
  const rsrc_t rq = mk_rsrc(p.q, S_ * D_ * 2), rws = mk_rsrc(p.ws, WS_END);
  const int gw = jb * 8 + w;
  unsigned tag;
  {
    u32x4 sr0, sr1; unsigned etag;
    const unsigned o0 = lane < 56 ? (256 * ib + gw) * 896 + 16 * lane : 0x80000000u;
    const unsigned o1 = (lane < 56 && gw < 32) ? (256 * ib + 224 + gw) * 896 + 16 * lane : 0x80000000u;
    const unsigned oz = WS_EPOCH;
    unsigned dvb; const unsigned od = (2 * (x * 28 + s) + (w >> 2)) * 2;
    asm volatile("buffer_load_dword %2, %6, %9, 0 offen\n\tbuffer_load_dwordx4 %0, %4, %8, 0 offen\n\tbuffer_load_dwordx4 %1, %5, %8, 0 offen\n\t"
                 "buffer_load_ushort %3, %7, %10, 0 offen\n\ts_waitcnt vmcnt(0)"
                 : "=&v"(sr0), "=&v"(sr1), "=&v"(etag), "=&v"(dvb) : "v"(o0), "v"(o1), "v"(oz), "v"(od), "s"(rq), "s"(rws), "s"(mk_rsrc(p.dv, D_ * 2)) : "memory");
    cx.dv = bf_lo(dvb);
    tag = __builtin_amdgcn_readfirstlane(etag) + 1u;
    cx.tag = tag;
    const u32x4 g0 = row_stats(sr0, tag), g1 = row_stats(sr1, tag);
    const unsigned p0 = lane == 0 ? WS_STATS + (256 * ib + gw) * 16 : 0x80000000u;
    const unsigned p1 = (lane == 0 && gw < 32) ? WS_STATS + (256 * ib + 224 + gw) * 16 : 0x80000000u;
    asm volatile("buffer_store_dwordx4 %0, %2, %4, 0 offen sc1\n\tbuffer_store_dwordx4 %1, %3, %4, 0 offen sc1\n\ts_nop 1" ::"v"(g0), "v"(g1), "v"(p0), "v"(p1), "s"(rws) : "memory");
  }
  const int tw = w & 3, fh = w >> 2, r = lane & 31, hh = lane >> 5;
  unsigned xa[4], wa[2];
#pragma unroll
  for (int i = 0; i < 4; ++i) xa[i] = lb + (64 * tw + r) * 128 + 16 * ((2 * i + hh) ^ ((r >> 1) & 7));
  {
    const int blk = (lane >> 4) & 1, q = (lane & 15) >> 2, pp = lane & 3, m = pp >> 1, f = (q >> 1) | ((m ^ hh) << 1);
#pragma unroll
    for (int u = 0; u < 2; ++u) wa[u] = lb + 32768 + m * 8192 + (8 * hh + q) * 128 + 32 * ((2 * fh + u) ^ f) + 16 * blk + 8 * (pp & 1);
  }
  f32x16 acc[2][2] = {};
  u32x4 sg0, sg1;
  TS(1);
  bar();
  TS(2);
  bf16x8 fb[2][2] = {}, fa[2][2] = {};
  auto rdA = [&](int ks, int u) { fa[ks & 1][u] = cat(lds_tr(wa[u] + (ks & 3) * 2048), lds_tr(wa[u] + (ks & 3) * 2048 + 512)); };
  auto rdB = [&](int ks) { fb[ks & 1][0] = lds_ld<bf16x8>(xa[ks & 3]); fb[ks & 1][1] = lds_ld<bf16x8>(xa[ks & 3] + 4096); };
  auto step = [&](int ks, bool ahead) {
    const int pb = ks & 1;
    SB();
    acc[0][0] = mfma32(fa[pb][0], fb[pb][0], acc[0][0]);
    SB();
    if (ahead) rdA(ks + 1, 0);
    SB();
    acc[1][0] = mfma32(fa[pb][0], fb[pb][1], acc[1][0]);
    SB();
    if (ahead) rdB(ks + 1);
    SB();
    acc[0][1] = mfma32(fa[pb][1], fb[pb][0], acc[0][1]);
    SB();
    if (ahead) rdA(ks + 1, 1);
    SB();
    acc[1][1] = mfma32(fa[pb][1], fb[pb][1], acc[1][1]);
    SB();
  };
  rdA(0, 0); rdB(0); rdA(0, 1);
  int sl = 0;
#pragma unroll 1
  for (int c = 0; c < 6; ++c) {
    step(0, true);
    step(1, true);
    step(2, true);
    {
      const unsigned d = __builtin_amdgcn_readfirstlane(sl == 2 ? -98304 : 49152);
      sl = sl == 2 ? 0 : sl + 1;
#pragma unroll
      for (int i = 0; i < 4; ++i) xa[i] += d;
      wa[0] += d; wa[1] += d;
    }
    bar();
    TS2(16 + c);
    step(3, true);
    if (c == 5 && threadIdx.x == 0) post_flag(p.ws + WS_FU + (((x * 28 + s) % 7) * 32 + (x * 28 + s) / 7) * 128, tag);
  }
  {
    const unsigned o0 = WS_STATS + (256 * ib + 64 * tw + r) * 16;
    asm volatile("buffer_load_dwordx4 %0, %2, %3, 0 offen sc1\n\tbuffer_load_dwordx4 %1, %2, %3, 0 offen offset:512 sc1"
                 : "=&v"(sg0), "=&v"(sg1) : "v"(o0), "s"(rws) : "memory");
  }
  step(0, true); step(1, true); step(2, true); step(3, false);
  TS(3);
  wait_vm<0>();
  asm volatile("" : "+v"(sg0), "+v"(sg1));
  const unsigned csb = lb + L_CS;
  float mean[2] = {__uint_as_float(sg0[1]), __uint_as_float(sg1[1])}, rstd[2] = {__uint_as_float(sg0[3]), __uint_as_float(sg1[3])};
  if (!__all(sg0[0] == tag && sg0[2] == tag && sg1[0] == tag && sg1[2] == tag)) {
#pragma unroll
    for (int tt = 0; tt < 2; ++tt) stats_wait(rws, WS_STATS + (256 * ib + 64 * tw + 32 * tt + r) * 16, tag, mean[tt], rstd[tt]);
  }
  bar();
  TS(4);
  constexpr float C0 = -2.3022081985f, C1 = -0.1029432397f;
#pragma unroll
  for (int u = 0; u < 2; ++u) {
    f32x4 cg[2], cu[2];
#pragma unroll
    for (int b = 0; b < 2; ++b) {
      cg[b] = lds_ld<f32x4>(csb + (16 * (2 * fh + u) + 8 * b + 4 * hh) * 4);
      cu[b] = lds_ld<f32x4>(csb + 256 + (16 * (2 * fh + u) + 8 * b + 4 * hh) * 4);
    }
#pragma unroll
    for (int tt = 0; tt < 2; ++tt) {
      const float rr = rstd[tt], k0s = rr * C0, k1s = C1 * rr * rr * rr, ir2s = __builtin_amdgcn_rcpf(rr * rr);
      const f32x2 nm = {-mean[tt], -mean[tt]}, k0 = {k0s, k0s}, k1 = {k1s, k1s}, ir2 = {ir2s, ir2s};
      u32x4 o;
#pragma unroll
      for (int b = 0; b < 2; ++b)
#pragma unroll
        for (int e = 0; e < 4; e += 2) {
          const f32x2 ag = {acc[tt][u][8 * b + e], acc[tt][u][8 * b + e + 1]}, au = {acc[tt][u][8 * b + 4 + e], acc[tt][u][8 * b + 5 + e]};
          const f32x2 c2g = {cg[b][e], cg[b][e + 1]}, c2u = {cu[b][e], cu[b][e + 1]};
          const f32x2 a = __builtin_elementwise_fma(nm, c2g, ag), bb = __builtin_elementwise_fma(nm, c2u, au);
          const f32x2 z = a * __builtin_elementwise_fma(a * a, k1, k0);
          const f32x2 ex = {__builtin_amdgcn_exp2f(z[0]), __builtin_amdgcn_exp2f(z[1])};
          const f32x2 dn = __builtin_elementwise_fma(ex, ir2, ir2);
          const f32x2 rc = {__builtin_amdgcn_rcpf(dn[0]), __builtin_amdgcn_rcpf(dn[1])};
          const f32x2 hv = (a * bb) * rc;
          o[2 * b + (e >> 1)] = pk_bf16(hv[0], hv[1]);
        }
      const unsigned oh = WS_H + ((8 * ib + 2 * tw + tt) * 112 + 4 * jb + 2 * fh + u) * 1024 + lane * 16;
      asm volatile("buffer_store_dwordx4 %0, %1, %2, 0 offen" HSPOL "\n\ts_nop 1" ::"v"(o), "v"(oh), "s"(rws) : "memory");
    }
  }
  TS(5);
}

constexpr int CODE_LINES = 208;
struct WdStage {
  rsrc_t rw; unsigned vw, lb; int kq, th, rot;
  DI void group(int g) const {
#pragma unroll
    for (int n = 0; n < 4; ++n) g2_piece(rw, vw, kq, th, 4 * g + n, rot, lb);
  }
};
DI void loader_tail(const Params& p, unsigned lb, int s, int lane, const WdStage& wd, bool poller, unsigned tag) {
  bar();
  wd.group(3);
  bar();
  wd.group(4);
  bar();
  wd.group(5);
  wait_vm<8>();
  bar();
  wd.group(6);
  wait_vm<8>();
  bar();
  wait_vm<4>();
  bar();
  int yok = 0;
  if (poller) {
    const unsigned yf = __hip_atomic_load((const GAS unsigned*)(p.ws + WS_FY + (s % 7) * 32 * 128 + flag_off(32, lane)), __ATOMIC_RELAXED, __HIP_MEMORY_SCOPE_AGENT);
    yok = flags_ok(yf, tag) ? 1 : 0;
    if (lane == 0) lds_st<unsigned>(lb + 131072, (unsigned)yok);
  }
  wait_vm<0>();
  bar();
  if (poller && !yok) poll_flags(p.ws + WS_FY + (s % 7) * 32 * 128, 32, tag, lane);
  bar();
  bar();
}

DI void loader_x(const Params& p, unsigned lb, int x, int s, int lw, int lane) {
  const int ib = 4 * (x >> 2) + s / 7;
  const rsrc_t rq = mk_rsrc(p.q, S_ * D_ * 2);
  const int L3 = lane >> 3, L7 = lane & 7;
  const unsigned vx0 = (64 * lw + L3) * 896 + 16 * (L7 ^ ((lane >> 4) & 3));
  const unsigned vx1 = (64 * lw + 8 + L3) * 896 + 16 * (L7 ^ (((lane >> 4) & 3) | 4));
  const unsigned sx = ib * 256 * 896, lx = lb + lw * 8192;
  auto piece = [&](auto C, auto I) {
    constexpr int c = decltype(C)::value, i = decltype(I)::value;
    dma16c<(c % 3) * 49152 + i * 1024, c * 128 + (i >> 1) * 16 * 896>(rq, (i & 1) ? vx1 : vx0, sx, lx);
  };
  auto stage = [&](auto C) {
    piece(C, IC<0>{}); piece(C, IC<1>{}); piece(C, IC<2>{}); piece(C, IC<3>{}); piece(C, IC<4>{}); piece(C, IC<5>{}); piece(C, IC<6>{}); piece(C, IC<7>{});
  };
  if (XPRIO) __builtin_amdgcn_s_setprio(XPRIO);
  const int orow = 37 * (s % 7) + 10 * lw + lane;
  const unsigned tx = (OPT_OWN && lane < 10 && 10 * lw + lane < 37 && orow < 256) ? (256 * ib + orow) * 896u : 0x80000000u;
  auto own = [&](int c) { touch(rq, tx + c * 128, lb + L_TOUCH); };
  TSL_DECL
  TS(8);
  stage(IC<0>{});
  own(1); own(2);
  stage(IC<1>{});
  wait_vm<10>();
  TS(1);
  bar();
  own(3);
  stage(IC<2>{});
  auto lstage = [&](auto C) {
    constexpr int c = decltype(C)::value;
    TSL(16 + c);
    if (c < 4) wait_vm<9>(); else if (c == 4) wait_vm<8>(); else wait_vm<0>();
    TSL(24 + c);
    bar();
    TSL(2 + c);
    if (c + 4 <= 6) own(c + 4);
    if constexpr (c + 3 <= 6) stage(IC<c + 3>{});
  };
  lstage(IC<0>{}); lstage(IC<1>{}); lstage(IC<2>{}); lstage(IC<3>{}); lstage(IC<4>{}); lstage(IC<5>{});
  bar();
  bar();
  const WdStage wd = {mk_rsrc(p.wd, F_ * D_ * 2), g2_vw(lane, s % 7), lb, lw, 0, g2_rot(s)};
  wd.group(0); wd.group(1); wd.group(2);
  bar();
  bar();
  wait_vm<0>();
  loader_tail(p, lb, s, lane, wd, false, 0u);
  TSL_DUMP();
}

DI void loader_w(const Params& p, unsigned lb, int x, int s, int lw, int lane) {
  const int jb = 7 * (x & 3) + s % 7;
  const rsrc_t rg = mk_rsrc(p.wg, D_ * F_ * 2), ru = mk_rsrc(p.wu, D_ * F_ * 2);
  const rsrc_t rw = mk_rsrc(p.wd, F_ * D_ * 2), rws = mk_rsrc(p.ws, WS_END);
  const int L3 = lane >> 3, L7 = lane & 7;
  const unsigned vwA = (16 * lw + L3) * 3584 + jb * 128 + 32 * ((L7 >> 1) ^ ((lane >> 4) & 1)) + 16 * (L7 & 1);
  const unsigned vwB = (16 * lw + L3) * 3584 + jb * 128 + 32 * ((L7 >> 1) ^ (((lane >> 4) & 1) | 2)) + 16 * (L7 & 1);
  const unsigned lx = lb + 32768 + lw * 2048;
  auto stage = [&](auto C) {
    constexpr int c = decltype(C)::value;
    constexpr int cm = c;
    dma16c<(c % 3) * 49152, cm * 64 * 3584>(rg, vwA, 0, lx);
    dma16c<(c % 3) * 49152 + 1024, cm * 64 * 3584 + 8 * 3584>(rg, vwB, 0, lx);
    dma16c<(c % 3) * 49152 + 8192, cm * 64 * 3584>(ru, vwB, 0, lx);
    dma16c<(c % 3) * 49152 + 8192 + 1024, cm * 64 * 3584 + 8 * 3584>(ru, vwA, 0, lx);
  };
  if (WPRIO) __builtin_amdgcn_s_setprio(WPRIO);
  const char* const tw = (const char*)((lane & 4) ? p.wu : p.wg) + (16 * (s / 7) + 4 * lw + (lane & 3)) * 3584 + jb * 128;
  auto own = [&](int c) { if (OPT_OWN) touch_g(tw + c * 64 * 3584, lb + L_TOUCH); else touch(rg, 0x80000000u, lb + L_TOUCH); };
  unsigned etag;
  { const unsigned oz = WS_EPOCH; asm volatile("buffer_load_dword %0, %1, %2, 0 offen" : "=&v"(etag) : "v"(oz), "s"(rws) : "memory"); }
  stage(IC<0>{});
  own(1); own(2);
  stage(IC<1>{});
  wait_vm<6>();
  asm volatile("" : "+v"(etag));
  const unsigned tag = __builtin_amdgcn_readfirstlane(etag) + 1u;
  bar();
  const int mi = x * 28 + s, cb = mi % 7, tb = mi / 7, ucg = lane >> 3, urp = lane & 7, ut0 = 64 * tb + 16 * lw + 2 * urp;
  u32x4 kk[5], vv[2];
  own(3);
  stage(IC<2>{});
  {
    const rsrc_t rk = mk_rsrc(p.k, S_ * D_ * 2), rv = mk_rsrc(p.v, S_ * D_ * 2);
    unsigned ok[5];
#pragma unroll
    for (int n = 0; n < 5; ++n) ok[n] = ut0 - 3 + n >= 0 ? (unsigned)((ut0 - 3 + n) * 896 + cb * 128 + ucg * 16) : 0x80000000u;
    const unsigned ov = ut0 * 896 + cb * 128 + ucg * 16;
    asm volatile("buffer_load_dwordx4 %0, %7, %13, 0 offen" KVPOL "\n\tbuffer_load_dwordx4 %1, %8, %13, 0 offen" KVPOL "\n\tbuffer_load_dwordx4 %2, %9, %13, 0 offen" KVPOL "\n\t"
                 "buffer_load_dwordx4 %3, %10, %13, 0 offen" KVPOL "\n\tbuffer_load_dwordx4 %4, %11, %13, 0 offen" KVPOL "\n\t"
                 "buffer_load_dwordx4 %5, %12, %14, 0 offen" KVPOL "\n\tbuffer_load_dwordx4 %6, %12, %14, 0 offen offset:896" KVPOL
                 : "=&v"(kk[0]), "=&v"(kk[1]), "=&v"(kk[2]), "=&v"(kk[3]), "=&v"(kk[4]), "=&v"(vv[0]), "=&v"(vv[1])
                 : "v"(ok[0]), "v"(ok[1]), "v"(ok[2]), "v"(ok[3]), "v"(ok[4]), "v"(ov), "s"(rk), "s"(rv) : "memory");
    dma16(mk_rsrc(p.sk, D_ * 8), lane < 32 ? cb * 512 + lane * 16 : 0x80000000u, 0, lb + L_SK);
  }
  auto wd_touch = [&](int i) {
    const int sh = 2 * i + (lw & 1);
    touch(rw, ((lw >> 1) == (i & 1) && sh < 7) ? (unsigned)((s * 7 + sh) * 64 + lane) * 128u : 0x80000000u, lb + L_TOUCH);
  };
  const int chl = lw >> 1, ch = 2 * mi + chl, tA = (lw & 1) * 128 + lane;
  u32x4 hA, hB; u32x2 gA, gB, cg2; unsigned uf = 0;
  auto upairs = [&](const int j0) {
    const int tl = 16 * lw + 2 * urp, e = (tl & ~7) + 6 - (tl & 7);
    const unsigned ta = lb + L_UT + 8 * ucg * 128 + (((e >> 3) ^ ucg) * 16) + 2 * (e & 7);
    u32x4 wq[2];
#pragma unroll
    for (int j = 0; j < 2; ++j) wq[j] = lds_ld<u32x4>(lb + L_SK + ucg * 64 + (j0 + j) * 16);
    f32x2 kf[2][5], vf[2][2], wt[2][4], s0[2], s1[2];
#pragma unroll
    for (int j = 0; j < 2; ++j) {
#pragma unroll
      for (int n = 0; n < 5; ++n) kf[j][n] = (f32x2){bf_lo(kk[n][j0 + j]), bf_hi(kk[n][j0 + j])};
#pragma unroll
      for (int n = 0; n < 2; ++n) vf[j][n] = (f32x2){bf_lo(vv[n][j0 + j]), bf_hi(vv[n][j0 + j])};
    }
#pragma unroll
    for (int j = 0; j < 2; ++j) {
      wt[j][0] = (f32x2){bf_lo(wq[j][0]), bf_lo(wq[j][2])}; wt[j][1] = (f32x2){bf_hi(wq[j][0]), bf_hi(wq[j][2])};
      wt[j][2] = (f32x2){bf_lo(wq[j][1]), bf_lo(wq[j][3])}; wt[j][3] = (f32x2){bf_hi(wq[j][1]), bf_hi(wq[j][3])};
    }
    SB();
#pragma unroll
    for (int j = 0; j < 2; ++j) { s0[j] = kf[j][0] * wt[j][3]; s1[j] = kf[j][1] * wt[j][3]; }
    SB();
#pragma unroll
    for (int t = 2; t >= 0; --t) {
#pragma unroll
      for (int j = 0; j < 2; ++j) { s0[j] = __builtin_elementwise_fma(kf[j][3 - t], wt[j][t], s0[j]); s1[j] = __builtin_elementwise_fma(kf[j][4 - t], wt[j][t], s1[j]); }
      SB();
    }
#pragma unroll
    for (int j = 0; j < 2; ++j) { s0[j] *= vf[j][0]; s1[j] *= vf[j][1]; }
    SB();
#pragma unroll
    for (int j = 0; j < 2; ++j) {
      lds_st<unsigned>(ta + (2 * (j0 + j)) * 128, pk_bf16(s1[j][0], s0[j][0]));
      lds_st<unsigned>(ta + (2 * (j0 + j) + 1) * 128, pk_bf16(s1[j][1], s0[j][1]));
    }
  };
  TSL_DECL
  auto lstage = [&](auto C) {
    constexpr int c = decltype(C)::value;
    TSL(16 + c);
    if (c == 0) wait_vm<13>(); else if (c < 3) wait_vm<5>(); else if (c == 3) wait_vm<6>(); else if (c == 4) wait_vm<8>(); else wait_vm<7>();
    TSL(24 + c);
    bar();
    TSL(2 + c);
    if (c == 3) {
#pragma unroll
      for (int n = 0; n < 2; ++n) {
        const int cl = 16 * lw + 8 * n + (lane >> 3);
        const u32x4 t = lds_ld<u32x4>(lb + L_UT + cl * 128 + (((lane & 7) ^ (cl >> 3)) * 16));
        const unsigned o = WS_UT + (64 * cb + cl) * TROW + 128 * tb + (lane & 7) * 16;
        asm volatile("buffer_store_dwordx4 %0, %1, %2, 0 offen sc1\n\ts_nop 1" ::"v"(t), "v"(o), "s"(rws) : "memory");
      }
    }
    if (c == 4) {
      const rsrc_t rh = mk_rsrc(p.h, D_ * S_ * 2);
      const unsigned oh = ch * 4096 + tA * 16;
      asm volatile("buffer_load_dwordx4 %0, %4, %5, 0 offen nt\n\tbuffer_load_dwordx2 %1, %4, %5, 0 offen offset:16 nt\n\t"
                   "buffer_load_dwordx4 %2, %4, %5, 0 offen offset:1024 nt\n\tbuffer_load_dwordx2 %3, %4, %5, 0 offen offset:1040 nt"
                   : "=&v"(hA), "=&v"(gA), "=&v"(hB), "=&v"(gB) : "v"(oh), "s"(rh) : "memory");
      const unsigned oc = lw < 2 ? WS_CS + (lw * 1792 + jb * 64 + lane) * 8 : 0x80000000u;
      asm volatile("buffer_load_dwordx2 %0, %1, %2, 0 offen sc1" : "=&v"(cg2) : "v"(oc), "s"(rws) : "memory");
    }
    if (c == 5) {
      const unsigned of = WS_FU + (mi >> 5) * 32 * 128 + flag_off(32, lane);
      asm volatile("buffer_load_dword %0, %1, %2, 0 offen sc1" : "=&v"(uf) : "v"(of), "s"(rws) : "memory");
    }
    if (c == 1) {
      asm volatile("" : "+v"(kk[0]), "+v"(kk[1]), "+v"(kk[2]), "+v"(kk[3]), "+v"(kk[4]), "+v"(vv[0]), "+v"(vv[1]));
      upairs(0);
    }
    if (c == 2) upairs(2);
    if (c + 4 <= 6) own(c + 4);
    if constexpr (c + 3 <= 6) stage(IC<c + 3>{});
    if (c >= 2) wd_touch(c - 2);
  };
  lstage(IC<0>{}); lstage(IC<1>{}); lstage(IC<2>{}); lstage(IC<3>{}); lstage(IC<4>{}); lstage(IC<5>{});
  asm volatile("s_waitcnt vmcnt(3)" : "+v"(cg2)::"memory");
  if (lw < 2) {
    if (!__all(cg2[0] == tag)) {
      for (unsigned it = 0; it < SPIN_CAP; ++it) {
        cg2 = __builtin_amdgcn_raw_buffer_load_b64(rws, WS_CS + (lw * 1792 + jb * 64 + lane) * 8, 0, 16);
        if (__all(cg2[0] == tag)) break;
        __builtin_amdgcn_s_sleep(1);
      }
    }
    lds_st<unsigned>(lb + L_CS + lw * 256 + lane * 4, cg2[1]);
  }
  if (lw == 0 && lane == 0) { lds_st<unsigned>(lb + L_CNT, 0u); }
  bar();
  wait_vm<1>();
  asm volatile("" : "+v"(hA), "+v"(gA), "+v"(hB), "+v"(gB), "+v"(uf));
  if (!flags_ok(uf, tag)) poll_flags(p.ws + WS_FU + (mi >> 5) * 32 * 128, 32, tag, lane);
  u32x4 uA, uB;
  {
    const unsigned ou = WS_UT + ch * TROW + tA * 16;
    asm volatile("buffer_load_dwordx4 %0, %2, %3, 0 offen sc1\n\tbuffer_load_dwordx4 %1, %2, %3, 0 offen offset:1024 sc1" : "=&v"(uA), "=&v"(uB) : "v"(ou), "s"(rws) : "memory");
  }
  const unsigned up = lb + CV_UP + chl * 4608, hs = lb + CV_HS + chl * 16640;
  {
    auto copies = [&](const u32x4& h4, const u32x2& h2, int t4) {
      const unsigned w0 = h4[0], w1 = h4[1], w2 = h4[2], w3 = h4[3], w4 = h2[0], w5 = h2[1];
      const unsigned a0 = __builtin_amdgcn_alignbit(w1, w0, 16), a1 = __builtin_amdgcn_alignbit(w2, w1, 16), a2 = __builtin_amdgcn_alignbit(w3, w2, 16),
                     a3 = __builtin_amdgcn_alignbit(w4, w3, 16), a4 = __builtin_amdgcn_alignbit(w5, w4, 16);
      const unsigned o = hs + 32 + 16 * t4;
      lds_st<u32x4>(o, (u32x4){w0, w1, w2, w3});
      lds_st<u32x4>(o + 4160, (u32x4){a0, a1, a2, a3});
      lds_st<u32x4>(o + 8320, (u32x4){w1, w2, w3, w4});
      lds_st<u32x4>(o + 12480, (u32x4){a1, a2, a3, a4});
    };
    copies(hA, gA, tA); copies(hB, gB, tA + 64);
    if (tA == 0) {
      const unsigned w0 = hA[0], a0 = __builtin_amdgcn_alignbit(hA[1], hA[0], 16);
      const u32x4 z = {0, 0, 0, 0};
#pragma unroll
      for (int sg = 0; sg < 4; ++sg) lds_st<u32x4>(hs + sg * 4160, z);
      lds_st<u32x4>(hs + 16, z);
      lds_st<u32x4>(hs + 4160 + 16, (u32x4){0, 0, 0, w0 << 16});
      lds_st<u32x4>(hs + 8320 + 16, (u32x4){0, 0, 0, w0});
      lds_st<u32x4>(hs + 12480 + 16, (u32x4){0, 0, w0 << 16, a0});
    }
    if (tA < 30) lds_st<u32x4>(up + 16 * tA, (u32x4){0, 0, 0, 0});
  }
  wait_vm<0>();
  asm volatile("" : "+v"(uA), "+v"(uB));
  lds_st<u32x4>(up + 480 + 16 * tA, uA);
  lds_st<u32x4>(up + 480 + 16 * (tA + 64), uB);
  bar();
  const WdStage wd = {rw, g2_vw(lane, s % 7), lb, lw, 1, g2_rot(s)};
  wd.group(0); wd.group(1); wd.group(2);
  unsigned hf = 0;
  if (lw == 0) __builtin_amdgcn_s_sleep(HSLEEP);
  if (lw == 0) hf = __hip_atomic_load((const GAS unsigned*)(p.ws + WS_FH + x * 28 * 128 + flag_off(28, lane)), __ATOMIC_RELAXED, __HIP_MEMORY_SCOPE_AGENT);
  bar();
  const int hok = flags_ok(hf, tag);
  if (lw == 0 && lane == 0) lds_st<unsigned>(lb + L_OK, hok ? 1u : 0u);
  bar();
  if (lw == 0 && !hok) poll_flags(p.ws + WS_FH + x * 28 * 128, 28, tag, lane);
  wait_vm<0>();
  loader_tail(p, lb, s, lane, wd, lw == 0, tag);
  TSL_DUMP();
}

DI void phase_conv(const Params& p, unsigned lb, int x, int s, int w, int lane, Ctx& cx) {
  const int mi = x * 28 + s, chl = w >> 2, wq = w & 3, ch = 2 * mi + chl, t4 = wq * 64 + lane;
  const rsrc_t rws = mk_rsrc(p.ws, WS_END);
  const unsigned tag = cx.tag;
  const float dv = cx.dv;
  const unsigned up = lb + CV_UP + chl * 4608, hs = lb + CV_HS + chl * 16640;
  bar();
  TS(6);
  const int i = lane & 15, kg = lane >> 4, sg = (i + 1) & 3;
  f32x4 acc[8] = {};
  {
    const unsigned ga = up + 448 - 128 * wq + 32 * i + 16 * kg;
    unsigned f0 = hs + sg * 4160 + 128 * wq + 2 * (25 + i - 8 * kg - sg), f1 = f0 + 8;
    asm volatile("" : "+v"(f1));
    bf16x8 G[8], Fq[2];
    unsigned gb = ga;
    auto rdF = [&](int a) { Fq[a & 1] = cat(lds_ld<s16x4>(f0 + 512 * a), lds_ld<s16x4>(f1 + 512 * a)); };
    rdF(0);
#pragma unroll
    for (int b = 7; b >= 0; --b) G[b] = lds_ld<bf16x8>(gb + 512 * b);
#pragma unroll 1
    for (int rr = 0; rr < 2; ++rr) {
      gb -= 64;
#pragma unroll
      for (int a = 0; a < 8; ++a) {
        if (a == 5 && rr == 0) {
          wait_vm<0>();
          if (lane == 0) {
            const unsigned old = __hip_atomic_fetch_add((LAS unsigned*)(size_t)(lb + L_CNT), 1u, __ATOMIC_RELAXED, __HIP_MEMORY_SCOPE_WORKGROUP);
            if (old == 7u) post_flag(p.ws + WS_FH + ((4 * (x >> 2) + s / 7) * 28 + 7 * (x & 3) + s % 7) * 128, tag);
          }
        }
#pragma unroll
        for (int b = 7 - a; b >= 0; --b) {
          acc[a + b] = mfma16(Fq[a & 1], G[b], acc[a + b]);
          SB();
          if (b == 7 - a) {
            if (a == 7) { f0 += 64; f1 += 64; rdF(0); } else rdF(a + 1);
            G[7 - a] = lds_ld<bf16x8>(gb + 512 * (7 - a));
          }
          SB();
        }
      }
    }
  }
  bar();
  TS(7);
  const unsigned red = lb + CV_RED + chl * 24576;
  f32x4 y0, y1;
  {
    auto scatter = [&](auto WQ) {
      constexpr int q0 = decltype(WQ)::value;
#pragma unroll
      for (int o = 1; o < 4; ++o) {
        constexpr int dummy = 0; (void)dummy;
        const int dst = (q0 + o) & 3;
        lds_st<f32x4>(red + (dst * 3 + (o - 1)) * 2048 + lane * 16, acc[2 * dst]);
        lds_st<f32x4>(red + (dst * 3 + (o - 1)) * 2048 + 1024 + lane * 16, acc[2 * dst + 1]);
      }
      y0 = acc[2 * q0]; y1 = acc[2 * q0 + 1];
    };
    if (wq == 0) scatter(IC<0>{}); else if (wq == 1) scatter(IC<1>{}); else if (wq == 2) scatter(IC<2>{}); else scatter(IC<3>{});
  }
  const unsigned ua = up + 480 + 1024 * wq + 32 * i + 16 * (kg >> 1) + 8 * (1 - (kg & 1));
  const u32x2 ua0 = lds_ld<u32x2>(ua), ua1 = lds_ld<u32x2>(ua + 512);
  bar();
  cx.hok = __builtin_amdgcn_readfirstlane(lds_ld<unsigned>(lb + L_OK));
  {
    const int tok = 256 * x + 64 * (s / 7) + 16 * (w >> 1) + (lane & 15);
    const unsigned ox = tok * 896 + (s % 7) * 128 + (4 * (w & 1) + (lane >> 4)) * 16, os = WS_STATS + tok * 16;
    asm volatile("buffer_load_dwordx4 %0, %2, %4, 0 offen\n\tbuffer_load_dwordx4 %1, %3, %5, 0 offen sc1"
                 : "=&v"(cx.xq), "=&v"(cx.sg) : "v"(ox), "v"(os), "s"(mk_rsrc(p.q, S_ * D_ * 2)), "s"(rws) : "memory");
  }
  if (cx.hok) h_first(p, x, s, w, lane, cx);
#pragma unroll
  for (int o = 0; o < 3; ++o) {
    y0 += lds_ld<f32x4>(red + (wq * 3 + o) * 2048 + lane * 16);
    y1 += lds_ld<f32x4>(red + (wq * 3 + o) * 2048 + 1024 + lane * 16);
  }
  y0[0] = rbf(y0[0]) + rbf(dv * bf_hi(ua0[1])); y0[1] = rbf(y0[1]) + rbf(dv * bf_lo(ua0[1])); y0[2] = rbf(y0[2]) + rbf(dv * bf_hi(ua0[0])); y0[3] = rbf(y0[3]) + rbf(dv * bf_lo(ua0[0]));
  y1[0] = rbf(y1[0]) + rbf(dv * bf_hi(ua1[1])); y1[1] = rbf(y1[1]) + rbf(dv * bf_lo(ua1[1])); y1[2] = rbf(y1[2]) + rbf(dv * bf_hi(ua1[0])); y1[3] = rbf(y1[3]) + rbf(dv * bf_lo(ua1[0]));
  const unsigned d0 = pk_bf16(y0[0], y0[1]), d1 = pk_bf16(y0[2], y0[3]), e0 = pk_bf16(y1[0], y1[1]), e1 = pk_bf16(y1[2], y1[3]);
  const auto s0 = __builtin_amdgcn_permlane16_swap(d0, e0, false, false);
  const auto s1 = __builtin_amdgcn_permlane16_swap(d1, e1, false, false);
  const u32x4 o = {s0[0], s1[0], s0[1], s1[1]};
  const unsigned oy = WS_YT + ch * TROW + 512 * (2 * wq + (kg & 1)) + 32 * i + 8 * (kg & 2);
  asm volatile("buffer_store_dwordx4 %0, %1, %2, 0 offen sc1\n\ts_nop 1" ::"v"(o), "v"(oy), "s"(rws) : "memory");
}

DI void phase_gemm2(const Params& p, unsigned lb, int x, int s, int w, int lane, Ctx& cx) {
  const int i2 = s / 7, db = s % 7, th = w >> 2, kq = w & 3, rot = g2_rot(s);
  const unsigned tag = cx.tag;
  const rsrc_t rws = mk_rsrc(p.ws, WS_END);
  const rsrc_t rq = mk_rsrc(p.q, S_ * D_ * 2), ro = mk_rsrc(p.out, S_ * D_ * 2);
  const int tk = 16 * (w >> 1) + (lane & 15), ck = 4 * (w & 1) + (lane >> 4), tok = 256 * x + 64 * i2 + tk;
  bar();
  TS(10);
  const unsigned hb = WS_H + ((8 * x + 2 * i2 + th) * 112 + 28 * kq) * 1024;
  const unsigned vh = lane * 16;
  if (!cx.hok) h_first(p, x, s, w, lane, cx);
  u32x4 (&hr)[HD] = cx.hr;
  const int hh = lane >> 5, blk = (lane >> 4) & 1, q = (lane & 15) >> 2, pp = lane & 3;
  unsigned aa[2];
#pragma unroll
  for (int dt = 0; dt < 2; ++dt) aa[dt] = lb + kq * 8192 + (4 * hh + q) * 128 + 64 * (dt ^ ((q >> 1) & 1)) + 32 * blk + 8 * pp;
  f32x16 acc[2] = {};
  bf16x8 fa[2][2];
  static_assert(HD == 4, "one group of four k-steps per loop iteration");
  auto rd1 = [&](int j, int dt) { fa[j & 1][dt] = cat(lds_tr(aa[dt] + (j & 3) * 2048), lds_tr(aa[dt] + (j & 3) * 2048 + 1024)); };
  auto step = [&](int j, bool ahead) {
    SB();
    const bf16x8 b = __builtin_bit_cast(bf16x8, hr[j]);
    acc[0] = mfma32(fa[j & 1][0], b, acc[0]);
    SB();
    if (ahead) rd1(j + 1, 0);
    SB();
    acc[1] = mfma32(fa[j & 1][1], b, acc[1]);
    SB();
    if (ahead) rd1(j + 1, 1);
    SB();
  };
  u32x4 yv; int yok;
  rd1(0, 0); rd1(0, 1);
  int st = rot + 4; st -= st >= 28 ? 28 : 0;
#pragma unroll 1
  for (int g = 0; g < 6; ++g) {
#pragma unroll
    for (int j = 0; j < 4; ++j) {
      if (j == 3) {
        const unsigned d = (g & 3) == 3 ? (unsigned)-98304 : 32768u;
        aa[0] += d; aa[1] += d;
        bar();
        TS2(24 + g);
      }
      asm volatile("s_waitcnt vmcnt(3)" : "+v"(hr[j])::"memory");
      step(j, true);
      asm volatile("buffer_load_dwordx4 %0, %1, %2, %3 offen" HPOL : "=v"(hr[j]) : "v"(vh), "s"(rws), "s"(hb + st * 1024) : "memory");
      st = st == 27 ? 0 : st + 1;
      if (j == 3 && g == 2 && threadIdx.x == 0) post_flag(p.ws + WS_FY + (x * 28 + s) * 128, tag);
      SB();
    }
  }
  yok = __builtin_amdgcn_readfirstlane(lds_ld<unsigned>(lb + 131072));
  {
    const unsigned oy = yok ? WS_YT + (64 * db + (threadIdx.x >> 3)) * TROW + (256 * x + 64 * i2) * 2 + (threadIdx.x & 7) * 16 : 0x80000000u;
    asm volatile("buffer_load_dwordx4 %0, %1, %2, 0 offen sc1" : "=&v"(yv) : "v"(oy), "s"(rws) : "memory");
  }
  asm volatile("s_waitcnt vmcnt(4)" : "+v"(hr[0])::"memory"); step(0, true);
  asm volatile("s_waitcnt vmcnt(3)" : "+v"(hr[1])::"memory"); step(1, true);
  asm volatile("s_waitcnt vmcnt(2)" : "+v"(hr[2])::"memory"); step(2, true);
  asm volatile("s_waitcnt vmcnt(1)" : "+v"(hr[3])::"memory"); step(3, false);
  u32x4 xq = cx.xq, sg = cx.sg;
  TS(11);
  wait_vm<0>();
  asm volatile("" : "+v"(xq), "+v"(sg), "+v"(yv));
  bar();
  if (!yok) yv = __builtin_amdgcn_raw_buffer_load_b128(rws, WS_YT + (64 * db + (threadIdx.x >> 3)) * TROW + (256 * x + 64 * i2) * 2 + (threadIdx.x & 7) * 16, 0, 16);
  float mean = __uint_as_float(sg[1]), rstd = __uint_as_float(sg[3]);
  if (!__all(sg[0] == tag && sg[2] == tag)) stats_wait(rws, WS_STATS + tok * 16, tag, mean, rstd);
  const unsigned pb = lb, yb = lb + 73728;
  {
    const int r = lane & 31;
#pragma unroll
    for (int dt = 0; dt < 2; ++dt)
#pragma unroll
      for (int G = 0; G < 4; ++G) {
        const f32x4 t = {acc[dt][4 * G], acc[dt][4 * G + 1], acc[dt][4 * G + 2], acc[dt][4 * G + 3]};
        lds_st<f32x4>(pb + kq * 17408 + (32 * th + r) * 272 + (32 * dt + 8 * G + 4 * hh) * 4, t);
      }
  }
  lds_st<u32x4>(yb + (threadIdx.x >> 3) * 144 + (threadIdx.x & 7) * 16, yv);
  bar();
  f32x4 o0 = {}, o1 = {};
#pragma unroll
  for (int k4 = 0; k4 < 4; ++k4) {
    o0 += lds_ld<f32x4>(pb + k4 * 17408 + tk * 272 + ck * 32);
    o1 += lds_ld<f32x4>(pb + k4 * 17408 + tk * 272 + ck * 32 + 16);
  }
  const unsigned ya = yb + (8 * ck + q) * 144 + (16 * (w >> 1) + 4 * pp) * 2;
  const s16x4 y0 = lds_tr(ya), y1 = lds_tr(ya + 4 * 144);
  u32x4 ov;
#pragma unroll
  for (int e = 0; e < 8; e += 2) {
    const f32x2 o = e < 4 ? (f32x2){o0[e & 3], o0[(e & 3) + 1]} : (f32x2){o1[e & 3], o1[(e & 3) + 1]};
    const f32x2 xv = {bf_lo(xq[e >> 1]), bf_hi(xq[e >> 1])};
    const unsigned yp = e < 4 ? __builtin_bit_cast(u32x2, y0)[(e & 3) >> 1] : __builtin_bit_cast(u32x2, y1)[(e & 3) >> 1];
    const f32x2 yy = {bf_lo(yp), bf_hi(yp)};
    const f32x2 t = o * (f32x2){-1.4426950409f, -1.4426950409f};
    const f32x2 dn = (f32x2){__builtin_amdgcn_exp2f(t[0]), __builtin_amdgcn_exp2f(t[1])} + (f32x2){1.f, 1.f};
    const f32x2 g = o * (f32x2){__builtin_amdgcn_rcpf(dn[0]), __builtin_amdgcn_rcpf(dn[1])};
    const f32x2 xn = rbf2(rbf2(xv - (f32x2){mean, mean}) * (f32x2){rstd, rstd});
    const f32x2 r2 = __builtin_elementwise_fma(rbf2(xn * yy), g, xv);
    ov[e >> 1] = pk_bf16(r2[0], r2[1]);
  }
  __builtin_amdgcn_raw_buffer_store_b128(ov, ro, tok * 896 + db * 128 + ck * 16, 0, 0);
  TS(12);
}

__global__ __launch_bounds__(1024, 4) void h3_geglu_kernel(Params p) {
  __shared__ __attribute__((aligned(16))) char smem[LDS_BYTES];
  const unsigned lb = (unsigned)(size_t)(LAS char*)smem;
  const int tid = threadIdx.x, lane = tid & 63, w = __builtin_amdgcn_readfirstlane(tid >> 6);
  const int b = blockIdx.x, x = b & 7, s = b >> 3;
  TS(0);
  if (s < 28) {
    if (w >= 12) {
      loader_w(p, lb, x, s, w - 12, lane);
    } else if (w >= 8) {
      loader_x(p, lb, x, s, w - 8, lane);
    } else {
      Ctx cx;
      phase_gemm1(p, lb, x, s, w, lane, cx);
      phase_conv(p, lb, x, s, w, lane, cx);
      TS(9);
      phase_gemm2(p, lb, x, s, w, lane, cx);
    }
  } else {
    const int j = b - 224;
    if (WARM && w < 14) {
      const int q = j >> 3;
      touch(mk_rsrc((w & 1) ? p.v : p.k, S_ * D_ * 2), (unsigned)((256 * x + 64 * q) * 896 + ((w >> 1) * 64 + lane) * 128), lb + 65536);
    }
    if (j < 28) {
      const unsigned tag = __builtin_amdgcn_readfirstlane(*(const GAS unsigned*)(p.ws + WS_EPOCH) + 1u);
      const int m = w & 1, r0 = (w >> 1) * 8 + (lane >> 3);
      const rsrc_t rm = mk_rsrc(m ? p.wu : p.wg, D_ * F_ * 2);
      u32x4 t[7];
#pragma unroll
      for (int n = 0; n < 7; ++n) t[n] = __builtin_amdgcn_raw_buffer_load_b128(rm, (r0 + 64 * n) * 3584 + j * 128 + (lane & 7) * 16, 0, 0);
      float a[8] = {};
#pragma unroll
      for (int n = 0; n < 7; ++n)
#pragma unroll
        for (int i = 0; i < 4; ++i) { a[2 * i] += bf_lo(t[n][i]); a[2 * i + 1] += bf_hi(t[n][i]); }
      lds_st<f32x4>(lb + (w * 8 + (lane >> 3)) * 256 + (lane & 7) * 32, (f32x4){a[0], a[1], a[2], a[3]});
      lds_st<f32x4>(lb + (w * 8 + (lane >> 3)) * 256 + (lane & 7) * 32 + 16, (f32x4){a[4], a[5], a[6], a[7]});
      bar();
      if (w < 2) {
        float sum = 0.f;
#pragma unroll
        for (int q = 0; q < 64; ++q) sum += lds_ld<float>(lb + (((q >> 3) * 2 + w) * 8 + (q & 7)) * 256 + lane * 4);
        const u32x2 g = {tag, __float_as_uint(sum)};
        __builtin_amdgcn_raw_buffer_store_b64(g, mk_rsrc(p.ws, WS_END), WS_CS + (w * 1792 + j * 64 + lane) * 8, 0, 16);
      }
    }
    if (WARM && w < 14) {
      const int blk = w * 32 + j;
      if (blk < 196) touch(mk_rsrc(p.wd, F_ * D_ * 2), (unsigned)(blk * 64 + lane) * 128u, lb + 65536);
      else touch(mk_rsrc(p.h, D_ * S_ * 2), (unsigned)((blk - 196) * 64 + lane) * 128u, lb + 65536);
    }
    if (b == 224 && w == 15) {
      const unsigned tag = __builtin_amdgcn_readfirstlane(*(const GAS unsigned*)(p.ws + WS_EPOCH) + 1u);
#pragma unroll 1
      for (int n = 0; n < 4; ++n) poll_flags_slow(p.ws + WS_FH + n * 56 * 128, 56, tag, lane);
      if (lane == 0) post_flag(p.ws + WS_EPOCH, tag);
    }
    wait_vm<0>();
  }
  TS(15);
}

extern "C" void kernel_launch(void* const* d_in, const int* in_sizes, int n_in, void* d_out, int out_size, void* d_ws, size_t ws_size, hipStream_t stream) {
  Params p;
  p.q = (const u16*)d_in[0]; p.k = (const u16*)d_in[1]; p.v = (const u16*)d_in[2]; p.sk = (const u16*)d_in[3];
  p.h = (const u16*)d_in[4]; p.dv = (const u16*)d_in[5];
  p.wg = (const u16*)d_in[7]; p.wu = (const u16*)d_in[8]; p.wd = (const u16*)d_in[9];
  p.out = (u16*)d_out; p.ws = (char*)d_ws;
  h3_geglu_kernel<<<256, 1024, 0, stream>>>(p);
}
```

```cpp
#include <hip/hip_runtime.h>
#include <hip/hip_bf16.h>
#include <stdint.h>

typedef unsigned short u16;
typedef __attribute__((ext_vector_type(8))) short bf16x8;
typedef __attribute__((ext_vector_type(4))) short s16x4;
typedef __attribute__((ext_vector_type(4))) float f32x4;
typedef __attribute__((ext_vector_type(2))) float f32x2;
typedef __attribute__((ext_vector_type(16))) float f32x16;
typedef __attribute__((ext_vector_type(4))) unsigned u32x4;
typedef __attribute__((ext_vector_type(2))) unsigned u32x2;
typedef __attribute__((ext_vector_type(2))) __bf16 bf16x2;
typedef __amdgpu_buffer_rsrc_t rsrc_t;
#define HSPOL " sc1"
#define LAS __attribute__((address_space(3)))
#define GAS __attribute__((address_space(1)))
#define DI __device__ __forceinline__

#define OPT_OWN 0
#define KVT 1
#define KVFIRST 0
#define WDLATE 1
#define PAUX 2
#define USTAUX 16
#define HSLEEP 10
#define LPRIO 0
#define WARM 1
#define UPRIO 2
#define PFN 17

constexpr int S_ = 2048, D_ = 448, F_ = 1792;
constexpr unsigned WS_EPOCH = 0, WS_FH = 8192, WS_FY = 36864, WS_STATS = 65536, WS_FU = 98304, WS_CS = 2031616;
constexpr unsigned TROW = 4224;
constexpr unsigned WS_UT = 131072, WS_YT = 2097152, WS_H = 4194304, WS_END = WS_H + 7340032;
constexpr unsigned WS_DBG = 16u << 20;
constexpr int LDS_BYTES = 163840;
constexpr unsigned SPIN_CAP = 1u << 14;

struct Params {
  const u16 *q, *k, *v, *sk, *h, *dv, *wg, *wu, *wd;
  u16* out;
  char* ws;
};

DI rsrc_t mk_rsrc(const void* p, unsigned bytes) { return __builtin_amdgcn_make_buffer_rsrc((void*)p, (short)0, (int)bytes, 0x00020000); }
DI void dma16(rsrc_t r, unsigned voff, unsigned soff, unsigned lds) {
  asm volatile("s_mov_b32 m0, %0\n\ts_nop 0\n\tbuffer_load_dwordx4 %1, %2, %3 offen lds" ::"s"(lds), "v"(voff), "s"(r), "s"(soff) : "memory");
}
DI void touch(rsrc_t r, unsigned voff, unsigned lds) {
  asm volatile("s_mov_b32 m0, %0\n\ts_nop 0\n\tbuffer_load_dword %1, %2, 0 offen lds" ::"s"(lds), "v"(voff), "s"(r) : "memory");
}
template <int NT> DI void touch_p(rsrc_t r, unsigned voff, unsigned lds) {
  if (NT) asm volatile("s_mov_b32 m0, %0\n\ts_nop 0\n\tbuffer_load_dword %1, %2, 0 offen nt lds" ::"s"(lds), "v"(voff), "s"(r) : "memory");
  else touch(r, voff, lds);
}
DI void touch_g(const char* a, unsigned lds) {
  asm volatile("s_mov_b32 m0, %0\n\ts_nop 0\n\tglobal_load_lds_dword %1, off" ::"s"(lds), "v"(a) : "memory");
}
template <unsigned LA, unsigned SA>
DI void dma16c(rsrc_t r, unsigned voff, unsigned sbase, unsigned lbase) {
  unsigned t;
  asm volatile("s_add_u32 m0, %2, %5\n\ts_add_u32 %0, %3, %6\n\tbuffer_load_dwordx4 %1, %4, %0 offen lds"
               : "=&s"(t) : "v"(voff), "s"(lbase), "s"(sbase), "s"(r), "i"(LA), "i"(SA) : "memory", "scc");
}
template <int N> DI void wait_vm() { asm volatile("s_waitcnt vmcnt(%0)" ::"i"(N) : "memory"); }
DI void bar() { asm volatile("s_waitcnt lgkmcnt(0)\n\ts_barrier" ::: "memory"); }
DI float bf_lo(unsigned x) { return __uint_as_float(x << 16); }
DI float bf_hi(unsigned x) { return __uint_as_float(x & 0xffff0000u); }
DI unsigned pk_bf16(float lo, float hi) { f32x2 t = {lo, hi}; return __builtin_bit_cast(unsigned, __builtin_convertvector(t, bf16x2)); }
DI bf16x8 lds_b128(const char* p) { return *(const LAS bf16x8*)(p); }
DI s16x4 lds_tr(unsigned a) { return __builtin_amdgcn_ds_read_tr16_b64_v4i16((LAS s16x4*)(size_t)a); }
DI bf16x8 cat(s16x4 a, s16x4 b) { return __builtin_shufflevector(a, b, 0, 1, 2, 3, 4, 5, 6, 7); }
template <class T> DI T lds_ld(unsigned a) { return *(const LAS T*)(size_t)a; }
template <class T> DI void lds_st(unsigned a, T v) { *(LAS T*)(size_t)a = v; }
DI f32x16 mfma32(bf16x8 a, bf16x8 b, f32x16 c) { return __builtin_amdgcn_mfma_f32_32x32x16_bf16(a, b, c, 0, 0, 0); }
DI f32x4 mfma16(bf16x8 a, bf16x8 b, f32x4 c) { return __builtin_amdgcn_mfma_f32_16x16x32_bf16(a, b, c, 0, 0, 0); }
DI unsigned long long rtime() { return __builtin_amdgcn_s_memrealtime(); }
DI float dpp_add(float v, const int ctrl_row_shr) { return v; }
template <int CTRL, int RM> DI float dpp_mov(float v) { return __builtin_bit_cast(float, __builtin_amdgcn_update_dpp(0, __builtin_bit_cast(int, v), CTRL, RM, 0xf, true)); }
DI float wave_sum(float v) {
  v += dpp_mov<0x111, 0xf>(v); v += dpp_mov<0x112, 0xf>(v); v += dpp_mov<0x114, 0xf>(v); v += dpp_mov<0x118, 0xf>(v);
  v += dpp_mov<0x142, 0xa>(v); v += dpp_mov<0x143, 0xc>(v);
  return __builtin_bit_cast(float, __builtin_amdgcn_readlane(__builtin_bit_cast(int, v), 63));
}
#define TS(i)
#define TSL(i)
#define TSL_DECL
#define TSL_DUMP()
#define TS2(i)

DI void poll_flags(const char* base, int n, unsigned tag, int lane) {
  const GAS unsigned* f = (const GAS unsigned*)(base + (lane < n ? lane : 0) * 128);
  for (unsigned it = 0; it < SPIN_CAP; ++it) {
    unsigned x = __hip_atomic_load(f, __ATOMIC_RELAXED, __HIP_MEMORY_SCOPE_AGENT);
    if (__all(x == tag)) break;
    __builtin_amdgcn_s_sleep(2);
  }
}
DI void poll_flags_slow(const char* base, int n, unsigned tag, int lane) {
  const GAS unsigned* f = (const GAS unsigned*)(base + (lane < n ? lane : 0) * 128);
  for (unsigned it = 0; it < SPIN_CAP; ++it) {
    unsigned x = __hip_atomic_load(f, __ATOMIC_RELAXED, __HIP_MEMORY_SCOPE_AGENT);
    if (__all(x == tag)) break;
    __builtin_amdgcn_s_sleep(32);
  }
}
DI void post_flag(char* addr, unsigned tag) { __hip_atomic_store((GAS unsigned*)addr, tag, __ATOMIC_RELAXED, __HIP_MEMORY_SCOPE_AGENT); }

DI bool flags_ok(unsigned v, unsigned tag) { return __all(v == tag); }
DI unsigned flag_off(int n, int lane) { return (lane < n ? lane : 0) * 128; }
#define SB() __builtin_amdgcn_sched_barrier(0)
template <int V> struct IC { static constexpr int value = V; };
DI void asm_pin(unsigned& v) { asm volatile("" : "+v"(v)); }
DI float rbf(float x) { return __uint_as_float(pk_bf16(x, 0.f) << 16); }

DI f32x2 rbf2(f32x2 v) { const unsigned u = pk_bf16(v[0], v[1]); return (f32x2){bf_lo(u), bf_hi(u)}; }
DI u32x4 row_stats(u32x4 d, unsigned tag) {
  float e[8];
#pragma unroll
  for (int i = 0; i < 4; ++i) { e[2 * i] = bf_lo(d[i]); e[2 * i + 1] = bf_hi(d[i]); }
  float s = ((e[0] + e[1]) + (e[2] + e[3])) + ((e[4] + e[5]) + (e[6] + e[7]));
  const float mean = wave_sum(s) * (1.f / D_);
  const float on = (threadIdx.x & 63) < 56 ? 1.f : 0.f;
  float ss = 0.f;
#pragma unroll
  for (int i = 0; i < 8; ++i) { const float t = e[i] - mean; ss = fmaf(t, t, ss); }
  const float var = wave_sum(ss * on) * (1.f / D_);
  const float den = rbf(__builtin_amdgcn_sqrtf(rbf(rbf(var) + 1e-5f)));
  u32x4 g = {tag, __float_as_uint(rbf(mean)), tag, __float_as_uint(__builtin_amdgcn_rcpf(den))};
  return g;
}
DI void stats_wait(rsrc_t rs, unsigned off, unsigned tag, float& mean, float& rstd) {
  for (unsigned it = 0;; ++it) {
    const u32x4 g = __builtin_amdgcn_raw_buffer_load_b128(rs, off, 0, 16);
    mean = __uint_as_float(g[1]); rstd = __uint_as_float(g[3]);
    if (__all(g[0] == tag && g[2] == tag) || it >= SPIN_CAP) break;
    __builtin_amdgcn_s_sleep(1);
  }
}

constexpr unsigned CV_BASE = 98304;
constexpr unsigned CV_UP = CV_BASE;
constexpr unsigned CV_HS = CV_BASE + 9216;
constexpr unsigned CV_RED = CV_HS;
constexpr unsigned L_UT = 147456, L_SK = 155648;
constexpr unsigned L_CS = 157696, L_OK = 158720, L_CNT = 158728, L_TOUCH = 158976;

#define HPOL " sc1"
#define HDEPTH 4
constexpr int HD = HDEPTH;
struct Ctx {
  unsigned tag; int hok; float dv; u32x4 hr[HD], xq, sg;
};
DI void g2_stage(rsrc_t rw, unsigned vw, int kq, int th, int grp, int rot, unsigned lb) {
#pragma unroll
  for (int n = 0; n < 4; ++n) {
    int st = 4 * grp + n + rot; st -= st >= 28 ? 28 : 0;
    dma16(rw, vw, (unsigned)((28 * kq + st) * 16 + 8 * th) * 896u, lb + (grp & 3) * 32768 + kq * 8192 + n * 2048 + th * 1024);
  }
}
DI void g2_piece(rsrc_t rw, unsigned vw, int kq, int th, int step, int rot, unsigned lb) {
  int st = step + rot; st -= st >= 28 ? 28 : 0;
  dma16(rw, vw, (unsigned)((28 * kq + st) * 16 + 8 * th) * 896u, lb + ((step >> 2) & 3) * 32768 + kq * 8192 + (step & 3) * 2048 + th * 1024);
}
DI void h_load(const Params& p, u32x4& dst, unsigned hb, int step, int rot, unsigned vh) {
  int st = step + rot; st -= st >= 28 ? 28 : 0;
  asm volatile("buffer_load_dwordx4 %0, %1, %2, %3 offen" HPOL : "=v"(dst) : "v"(vh), "s"(mk_rsrc(p.ws, WS_END)), "s"(hb + st * 1024) : "memory");
}
DI int g2_rot(int s);
DI void h_first(const Params& p, int x, int s, int w, int lane, Ctx& cx) {
  const unsigned hb = WS_H + ((8 * x + 2 * (s / 7) + (w >> 2)) * 112 + 28 * (w & 3)) * 1024;
#pragma unroll
  for (int n = 0; n < HD; ++n) h_load(p, cx.hr[n], hb, n, g2_rot(s), lane * 16);
}
DI unsigned g2_vw(int lane, int db) { const int L3 = lane >> 3, L7 = lane & 7; return L3 * 896 + db * 128 + 16 * (L7 ^ (((L3 >> 1) & 1) << 2)); }
DI int g2_rot(int s) { const int r = 4 * (s % 7) + 7 * (s / 7); return r >= 28 ? r - 28 : r; }

DI void phase_gemm1(const Params& p, unsigned lb, int x, int s, int w, int lane, Ctx& cx) {
  const int ib = 4 * (x >> 2) + s / 7, jb = 7 * (x & 3) + s % 7;
  const rsrc_t rq = mk_rsrc(p.q, S_ * D_ * 2), rws = mk_rsrc(p.ws, WS_END);
  const rsrc_t rg = mk_rsrc(p.wg, D_ * F_ * 2), ru = mk_rsrc(p.wu, D_ * F_ * 2);
  const int L3 = lane >> 3, L7 = lane & 7;
  const unsigned vwg = (8 * w + L3) * 3584 + jb * 128 + 32 * ((L7 >> 1) ^ (((lane >> 4) & 1) | ((w & 1) << 1))) + 16 * (L7 & 1);
  const unsigned vwu = (8 * w + L3) * 3584 + jb * 128 + 32 * ((L7 >> 1) ^ (((lane >> 4) & 1) | (((w & 1) ^ 1) << 1))) + 16 * (L7 & 1);
  auto wpiece = [&](int c, int m) {
    const int c3 = c >= 6 ? c - 6 : c >= 3 ? c - 3 : c;
    if (m == 0) dma16(rg, vwg, c * 64 * 3584, lb + c3 * 49152 + 32768 + w * 1024);
    else dma16(ru, vwu, c * 64 * 3584, lb + c3 * 49152 + 40960 + w * 1024);
  };
  const int gw = jb * 8 + w;
  unsigned tag;
  {
    u32x4 sr0, sr1; unsigned etag;
    const unsigned o0 = lane < 56 ? (256 * ib + gw) * 896 + 16 * lane : 0x80000000u;
    const unsigned o1 = (lane < 56 && gw < 32) ? (256 * ib + 224 + gw) * 896 + 16 * lane : 0x80000000u;
    const unsigned oz = WS_EPOCH;
    wpiece(0, 0); wpiece(0, 1);
    unsigned dvb; const unsigned od = (2 * (x * 28 + s) + (w >> 2)) * 2;
    asm volatile("buffer_load_dword %2, %6, %9, 0 offen\n\tbuffer_load_dwordx4 %0, %4, %8, 0 offen\n\tbuffer_load_dwordx4 %1, %5, %8, 0 offen\n\t"
                 "buffer_load_ushort %3, %7, %10, 0 offen\n\ts_waitcnt vmcnt(0)"
                 : "=&v"(sr0), "=&v"(sr1), "=&v"(etag), "=&v"(dvb) : "v"(o0), "v"(o1), "v"(oz), "v"(od), "s"(rq), "s"(rws), "s"(mk_rsrc(p.dv, D_ * 2)) : "memory");
    cx.dv = bf_lo(dvb);
    wpiece(1, 0); wpiece(1, 1); wpiece(2, 0); wpiece(2, 1);
    tag = __builtin_amdgcn_readfirstlane(etag) + 1u;
    cx.tag = tag;
    const u32x4 g0 = row_stats(sr0, tag), g1 = row_stats(sr1, tag);
    const unsigned p0 = lane == 0 ? WS_STATS + (256 * ib + gw) * 16 : 0x80000000u;
    const unsigned p1 = (lane == 0 && gw < 32) ? WS_STATS + (256 * ib + 224 + gw) * 16 : 0x80000000u;
    asm volatile("buffer_store_dwordx4 %0, %2, %4, 0 offen sc1\n\tbuffer_store_dwordx4 %1, %3, %4, 0 offen sc1\n\ts_nop 1" ::"v"(g0), "v"(g1), "v"(p0), "v"(p1), "s"(rws) : "memory");
  }
  const int tw = w & 3, fh = w >> 2, r = lane & 31, hh = lane >> 5;
  unsigned xa[4], wa[2];
#pragma unroll
  for (int i = 0; i < 4; ++i) xa[i] = lb + (64 * tw + r) * 128 + 16 * ((2 * i + hh) ^ ((r >> 1) & 7));
  {
    const int blk = (lane >> 4) & 1, q = (lane & 15) >> 2, pp = lane & 3, m = pp >> 1, f = (q >> 1) | ((m ^ hh) << 1);
#pragma unroll
    for (int u = 0; u < 2; ++u) wa[u] = lb + 32768 + m * 8192 + (8 * hh + q) * 128 + 32 * ((2 * fh + u) ^ f) + 16 * blk + 8 * (pp & 1);
  }
  f32x16 acc[2][2] = {};
  u32x4 sg0, sg1;
  TS(1);
  bar();
  TS(2);
  bf16x8 fb[2][2], fa[2][2];
  auto rdA = [&](int ks, int u) { fa[ks & 1][u] = cat(lds_tr(wa[u] + (ks & 3) * 2048), lds_tr(wa[u] + (ks & 3) * 2048 + 512)); };
  auto rdB = [&](int ks) { fb[ks & 1][0] = lds_ld<bf16x8>(xa[ks & 3]); fb[ks & 1][1] = lds_ld<bf16x8>(xa[ks & 3] + 4096); };
  auto step = [&](int ks, bool ahead) {
    const int pb = ks & 1;
    SB();
    acc[0][0] = mfma32(fa[pb][0], fb[pb][0], acc[0][0]);
    SB();
    if (ahead) rdA(ks + 1, 0);
    SB();
    acc[1][0] = mfma32(fa[pb][0], fb[pb][1], acc[1][0]);
    SB();
    if (ahead) rdB(ks + 1);
    SB();
    acc[0][1] = mfma32(fa[pb][1], fb[pb][0], acc[0][1]);
    SB();
    if (ahead) rdA(ks + 1, 1);
    SB();
    acc[1][1] = mfma32(fa[pb][1], fb[pb][1], acc[1][1]);
    SB();
  };
  rdA(0, 0); rdB(0); rdA(0, 1);
  int sl = 0;
#pragma unroll 1
  for (int c = 0; c < 6; ++c) {
    step(0, true);
    if (c > 0) wpiece(c + 2, 1);
    step(1, true);
    step(2, true);
    {
      const unsigned d = __builtin_amdgcn_readfirstlane(sl == 2 ? -98304 : 49152);
      sl = sl == 2 ? 0 : sl + 1;
#pragma unroll
      for (int i = 0; i < 4; ++i) xa[i] += d;
      wa[0] += d; wa[1] += d;
    }
    wait_vm<2>();
    bar();
    TS2(16 + c);
    step(3, true);
    if (c < 5) wpiece(c + 3, 0);
    if (c == 5 && threadIdx.x == 0) post_flag(p.ws + WS_FU + (((x * 28 + s) % 7) * 32 + (x * 28 + s) / 7) * 128, tag);
  }
  {
    const unsigned o0 = WS_STATS + (256 * ib + 64 * tw + r) * 16;
    asm volatile("buffer_load_dwordx4 %0, %2, %3, 0 offen sc1\n\tbuffer_load_dwordx4 %1, %2, %3, 0 offen offset:512 sc1"
                 : "=&v"(sg0), "=&v"(sg1) : "v"(o0), "s"(rws) : "memory");
  }
  step(0, true); step(1, true); step(2, true); step(3, false);
  TS(3);
  wait_vm<0>();
  asm volatile("" : "+v"(sg0), "+v"(sg1));
  const unsigned csb = lb + L_CS;
  float mean[2] = {__uint_as_float(sg0[1]), __uint_as_float(sg1[1])}, rstd[2] = {__uint_as_float(sg0[3]), __uint_as_float(sg1[3])};
  if (!__all(sg0[0] == tag && sg0[2] == tag && sg1[0] == tag && sg1[2] == tag)) {
#pragma unroll
    for (int tt = 0; tt < 2; ++tt) stats_wait(rws, WS_STATS + (256 * ib + 64 * tw + 32 * tt + r) * 16, tag, mean[tt], rstd[tt]);
  }
  bar();
  TS(4);
  constexpr float C0 = -2.3022081985f, C1 = -0.1029432397f;
#pragma unroll
  for (int u = 0; u < 2; ++u) {
    f32x4 cg[2], cu[2];
#pragma unroll
    for (int b = 0; b < 2; ++b) {
      cg[b] = lds_ld<f32x4>(csb + (16 * (2 * fh + u) + 8 * b + 4 * hh) * 4);
      cu[b] = lds_ld<f32x4>(csb + 256 + (16 * (2 * fh + u) + 8 * b + 4 * hh) * 4);
    }
#pragma unroll
    for (int tt = 0; tt < 2; ++tt) {
      const float rr = rstd[tt], k0s = rr * C0, k1s = C1 * rr * rr * rr, ir2s = __builtin_amdgcn_rcpf(rr * rr);
      const f32x2 nm = {-mean[tt], -mean[tt]}, k0 = {k0s, k0s}, k1 = {k1s, k1s}, ir2 = {ir2s, ir2s};
      u32x4 o;
#pragma unroll
      for (int b = 0; b < 2; ++b)
#pragma unroll
        for (int e = 0; e < 4; e += 2) {
          const f32x2 ag = {acc[tt][u][8 * b + e], acc[tt][u][8 * b + e + 1]}, au = {acc[tt][u][8 * b + 4 + e], acc[tt][u][8 * b + 5 + e]};
          const f32x2 c2g = {cg[b][e], cg[b][e + 1]}, c2u = {cu[b][e], cu[b][e + 1]};
          const f32x2 a = __builtin_elementwise_fma(nm, c2g, ag), bb = __builtin_elementwise_fma(nm, c2u, au);
          const f32x2 z = a * __builtin_elementwise_fma(a * a, k1, k0);
          const f32x2 ex = {__builtin_amdgcn_exp2f(z[0]), __builtin_amdgcn_exp2f(z[1])};
          const f32x2 dn = __builtin_elementwise_fma(ex, ir2, ir2);
          const f32x2 rc = {__builtin_amdgcn_rcpf(dn[0]), __builtin_amdgcn_rcpf(dn[1])};
          const f32x2 hv = (a * bb) * rc;
          o[2 * b + (e >> 1)] = pk_bf16(hv[0], hv[1]);
        }
      const unsigned oh = WS_H + ((8 * ib + 2 * tw + tt) * 112 + 4 * jb + 2 * fh + u) * 1024 + lane * 16;
      asm volatile("buffer_store_dwordx4 %0, %1, %2, 0 offen" HSPOL "\n\ts_nop 1" ::"v"(o), "v"(oh), "s"(rws) : "memory");
    }
  }
  TS(5);
}

constexpr int CODE_LINES = 208;
DI void loader_main(const Params& p, unsigned lb, int x, int s, int lw, int lane, const char* pc) {
  const int ib = 4 * (x >> 2) + s / 7, jb = 7 * (x & 3) + s % 7;
  const rsrc_t rq = mk_rsrc(p.q, S_ * D_ * 2);
  const rsrc_t rw = mk_rsrc(p.wd, F_ * D_ * 2), rws = mk_rsrc(p.ws, WS_END);
  const int L3 = lane >> 3, L7 = lane & 7;
  const unsigned vx0 = (64 * lw + L3) * 896 + 16 * (L7 ^ ((lane >> 4) & 3));
  const unsigned vx1 = (64 * lw + 8 + L3) * 896 + 16 * (L7 ^ (((lane >> 4) & 3) | 4));
  const unsigned sx = ib * 256 * 896;
  const unsigned lx = lb + lw * 8192;
  auto piece = [&](auto C, auto I) {
    constexpr int c = decltype(C)::value, i = decltype(I)::value;
    dma16c<(c % 3) * 49152 + i * 1024, c * 128 + (i >> 1) * 16 * 896>(rq, (i & 1) ? vx1 : vx0, sx, lx);
  };
  auto stage = [&](auto C) {
    piece(C, IC<0>{}); piece(C, IC<1>{}); piece(C, IC<2>{}); piece(C, IC<3>{}); piece(C, IC<4>{}); piece(C, IC<5>{}); piece(C, IC<6>{}); piece(C, IC<7>{});
  };
  TSL_DECL
  if (LPRIO) __builtin_amdgcn_s_setprio(LPRIO);
  TS(8);
  unsigned etag;
  { const unsigned oz = WS_EPOCH; asm volatile("buffer_load_dword %0, %1, %2, 0 offen" : "=&v"(etag) : "v"(oz), "s"(rws) : "memory"); }
  stage(IC<0>{});
  stage(IC<1>{});
  wait_vm<8>();
  asm volatile("" : "+v"(etag));
  const unsigned tag = __builtin_amdgcn_readfirstlane(etag) + 1u;
  TS(1);
  bar();
  const int mi = x * 28 + s, cb = mi % 7, tb = mi / 7, ucg = lane >> 3, urp = lane & 7, ut0 = 64 * tb + 16 * lw + 2 * urp;
  u32x4 kk[5], vv[2];
  {
    const rsrc_t rk = mk_rsrc(p.k, S_ * D_ * 2), rv = mk_rsrc(p.v, S_ * D_ * 2);
    unsigned ok[5];
#pragma unroll
    for (int n = 0; n < 5; ++n) ok[n] = ut0 - 3 + n >= 0 ? (unsigned)((ut0 - 3 + n) * 896 + cb * 128 + ucg * 16) : 0x80000000u;
    const unsigned ov = ut0 * 896 + cb * 128 + ucg * 16;
    asm volatile("buffer_load_dwordx4 %0, %7, %13, 0 offen nt\n\tbuffer_load_dwordx4 %1, %8, %13, 0 offen nt\n\tbuffer_load_dwordx4 %2, %9, %13, 0 offen nt\n\t"
                 "buffer_load_dwordx4 %3, %10, %13, 0 offen nt\n\tbuffer_load_dwordx4 %4, %11, %13, 0 offen nt\n\t"
                 "buffer_load_dwordx4 %5, %12, %14, 0 offen nt\n\tbuffer_load_dwordx4 %6, %12, %14, 0 offen offset:896 nt"
                 : "=&v"(kk[0]), "=&v"(kk[1]), "=&v"(kk[2]), "=&v"(kk[3]), "=&v"(kk[4]), "=&v"(vv[0]), "=&v"(vv[1])
                 : "v"(ok[0]), "v"(ok[1]), "v"(ok[2]), "v"(ok[3]), "v"(ok[4]), "v"(ov), "s"(rk), "s"(rv) : "memory");
    dma16(mk_rsrc(p.sk, D_ * 8), lane < 32 ? cb * 512 + lane * 16 : 0x80000000u, 0, lb + L_SK);
  }
  auto wd_touch = [&](int i) {
    if (!WDLATE) touch(rw, lw == (i & 3) ? (unsigned)((s * 7 + i) * 64 + lane) * 128u : 0x80000000u, lb + L_TOUCH);
    else {
      const int sh = 2 * (i - 3) + (lw & 1);
      touch(rw, (i >= 3 && (lw >> 1) == (i & 1) && sh < 7) ? (unsigned)((s * 7 + sh) * 64 + lane) * 128u : 0x80000000u, lb + L_TOUCH);
    }
  };
  auto work = [&](auto CN) {
    if constexpr (decltype(CN)::value <= 6) stage(CN);
  };
  piece(IC<2>{}, IC<0>{}); piece(IC<2>{}, IC<1>{}); piece(IC<2>{}, IC<2>{}); piece(IC<2>{}, IC<3>{});
  const int chl = lw >> 1, ch = 2 * mi + chl, tA = (lw & 1) * 128 + lane;
  u32x4 hA, hB; u32x2 gA, gB, cg2; unsigned uf = 0;
  auto upairs = [&](const int j0) {
    const int tl = 16 * lw + 2 * urp, e = (tl & ~7) + 6 - (tl & 7);
    const unsigned ta = lb + L_UT + 8 * ucg * 128 + (((e >> 3) ^ ucg) * 16) + 2 * (e & 7);
    u32x4 wq[2];
#pragma unroll
    for (int j = 0; j < 2; ++j) wq[j] = lds_ld<u32x4>(lb + L_SK + ucg * 64 + (j0 + j) * 16);
    if (UPRIO) __builtin_amdgcn_s_setprio(UPRIO);
    f32x2 kf[2][5], vf[2][2], wt[2][4], s0[2], s1[2];
#pragma unroll
    for (int j = 0; j < 2; ++j) {
#pragma unroll
      for (int n = 0; n < 5; ++n) kf[j][n] = (f32x2){bf_lo(kk[n][j0 + j]), bf_hi(kk[n][j0 + j])};
#pragma unroll
      for (int n = 0; n < 2; ++n) vf[j][n] = (f32x2){bf_lo(vv[n][j0 + j]), bf_hi(vv[n][j0 + j])};
    }
#pragma unroll
    for (int j = 0; j < 2; ++j) {
      wt[j][0] = (f32x2){bf_lo(wq[j][0]), bf_lo(wq[j][2])}; wt[j][1] = (f32x2){bf_hi(wq[j][0]), bf_hi(wq[j][2])};
      wt[j][2] = (f32x2){bf_lo(wq[j][1]), bf_lo(wq[j][3])}; wt[j][3] = (f32x2){bf_hi(wq[j][1]), bf_hi(wq[j][3])};
    }
    SB();
#pragma unroll
    for (int j = 0; j < 2; ++j) { s0[j] = kf[j][0] * wt[j][3]; s1[j] = kf[j][1] * wt[j][3]; }
    SB();
#pragma unroll
    for (int t = 2; t >= 0; --t) {
#pragma unroll
      for (int j = 0; j < 2; ++j) { s0[j] = __builtin_elementwise_fma(kf[j][3 - t], wt[j][t], s0[j]); s1[j] = __builtin_elementwise_fma(kf[j][4 - t], wt[j][t], s1[j]); }
      SB();
    }
#pragma unroll
    for (int j = 0; j < 2; ++j) { s0[j] *= vf[j][0]; s1[j] *= vf[j][1]; }
    SB();
#pragma unroll
    for (int j = 0; j < 2; ++j) {
      lds_st<unsigned>(ta + (2 * (j0 + j)) * 128, pk_bf16(s1[j][0], s0[j][0]));
      lds_st<unsigned>(ta + (2 * (j0 + j) + 1) * 128, pk_bf16(s1[j][1], s0[j][1]));
    }
    if (UPRIO) __builtin_amdgcn_s_setprio(0);
  };
  auto lstage = [&](auto C) {
    constexpr int c = decltype(C)::value;
    TSL(16 + c);
    if (c == 0 || c == 4) wait_vm<12>(); else if (c < 3) wait_vm<8>(); else if (c == 3) wait_vm<9>(); else wait_vm<7>();
    TSL(24 + c);
    bar();
    TSL(2 + c);
    if (c == 0) { piece(IC<2>{}, IC<4>{}); piece(IC<2>{}, IC<5>{}); piece(IC<2>{}, IC<6>{}); piece(IC<2>{}, IC<7>{}); }
    if (c == 3) {
#pragma unroll
      for (int n = 0; n < 2; ++n) {
        const int cl = 16 * lw + 8 * n + (lane >> 3);
        const u32x4 t = lds_ld<u32x4>(lb + L_UT + cl * 128 + (((lane & 7) ^ (cl >> 3)) * 16));
        const unsigned o = WS_UT + (64 * cb + cl) * TROW + 128 * tb + (lane & 7) * 16;
        asm volatile("buffer_store_dwordx4 %0, %1, %2, 0 offen sc1\n\ts_nop 1" ::"v"(t), "v"(o), "s"(rws) : "memory");
      }
    }
    if (c == 4) {
      const rsrc_t rh = mk_rsrc(p.h, D_ * S_ * 2);
      const unsigned oh = ch * 4096 + tA * 16;
      asm volatile("buffer_load_dwordx4 %0, %4, %5, 0 offen nt\n\tbuffer_load_dwordx2 %1, %4, %5, 0 offen offset:16 nt\n\t"
                   "buffer_load_dwordx4 %2, %4, %5, 0 offen offset:1024 nt\n\tbuffer_load_dwordx2 %3, %4, %5, 0 offen offset:1040 nt"
                   : "=&v"(hA), "=&v"(gA), "=&v"(hB), "=&v"(gB) : "v"(oh), "s"(rh) : "memory");
      const unsigned oc = lw < 2 ? WS_CS + (lw * 1792 + jb * 64 + lane) * 8 : 0x80000000u;
      asm volatile("buffer_load_dwordx2 %0, %1, %2, 0 offen sc1" : "=&v"(cg2) : "v"(oc), "s"(rws) : "memory");
    }
    if (c == 5) {
      const unsigned of = WS_FU + (mi >> 5) * 32 * 128 + flag_off(32, lane);
      asm volatile("buffer_load_dword %0, %1, %2, 0 offen sc1" : "=&v"(uf) : "v"(of), "s"(rws) : "memory");
    }
    work(IC<c + 3>{});
    if (c == 1) {
      asm volatile("" : "+v"(kk[0]), "+v"(kk[1]), "+v"(kk[2]), "+v"(kk[3]), "+v"(kk[4]), "+v"(vv[0]), "+v"(vv[1]));
      upairs(0);
    }
    if (c == 2) upairs(2);
    if (c >= 2) wd_touch(c + 1);
  };
  lstage(IC<0>{}); lstage(IC<1>{}); lstage(IC<2>{}); lstage(IC<3>{}); lstage(IC<4>{}); lstage(IC<5>{});
  asm volatile("s_waitcnt vmcnt(3)" : "+v"(cg2)::"memory");
  if (lw < 2) {
    if (!__all(cg2[0] == tag)) {
      for (unsigned it = 0; it < SPIN_CAP; ++it) {
        cg2 = __builtin_amdgcn_raw_buffer_load_b64(rws, WS_CS + (lw * 1792 + jb * 64 + lane) * 8, 0, 16);
        if (__all(cg2[0] == tag)) break;
        __builtin_amdgcn_s_sleep(1);
      }
    }
    lds_st<unsigned>(lb + L_CS + lw * 256 + lane * 4, cg2[1]);
  }
  if (lw == 0 && lane == 0) { lds_st<unsigned>(lb + L_CNT, 0u); }
  bar();
  wait_vm<1>();
  asm volatile("" : "+v"(hA), "+v"(gA), "+v"(hB), "+v"(gB), "+v"(uf));
  if (!flags_ok(uf, tag)) poll_flags(p.ws + WS_FU + (mi >> 5) * 32 * 128, 32, tag, lane);
  u32x4 uA, uB;
  {
    const unsigned ou = WS_UT + ch * TROW + tA * 16;
    asm volatile("buffer_load_dwordx4 %0, %2, %3, 0 offen sc1\n\tbuffer_load_dwordx4 %1, %2, %3, 0 offen offset:1024 sc1" : "=&v"(uA), "=&v"(uB) : "v"(ou), "s"(rws) : "memory");
  }
  const unsigned up = lb + CV_UP + chl * 4608, hs = lb + CV_HS + chl * 16640;
  {
    auto copies = [&](const u32x4& h4, const u32x2& h2, int t4) {
      const unsigned w0 = h4[0], w1 = h4[1], w2 = h4[2], w3 = h4[3], w4 = h2[0], w5 = h2[1];
      const unsigned a0 = __builtin_amdgcn_alignbit(w1, w0, 16), a1 = __builtin_amdgcn_alignbit(w2, w1, 16), a2 = __builtin_amdgcn_alignbit(w3, w2, 16),
                     a3 = __builtin_amdgcn_alignbit(w4, w3, 16), a4 = __builtin_amdgcn_alignbit(w5, w4, 16);
      const unsigned o = hs + 32 + 16 * t4;
      lds_st<u32x4>(o, (u32x4){w0, w1, w2, w3});
      lds_st<u32x4>(o + 4160, (u32x4){a0, a1, a2, a3});
      lds_st<u32x4>(o + 8320, (u32x4){w1, w2, w3, w4});
      lds_st<u32x4>(o + 12480, (u32x4){a1, a2, a3, a4});
    };
    copies(hA, gA, tA); copies(hB, gB, tA + 64);
    if (tA == 0) {
      const unsigned w0 = hA[0], a0 = __builtin_amdgcn_alignbit(hA[1], hA[0], 16);
      const u32x4 z = {0, 0, 0, 0};
#pragma unroll
      for (int sg = 0; sg < 4; ++sg) lds_st<u32x4>(hs + sg * 4160, z);
      lds_st<u32x4>(hs + 16, z);
      lds_st<u32x4>(hs + 4160 + 16, (u32x4){0, 0, 0, w0 << 16});
      lds_st<u32x4>(hs + 8320 + 16, (u32x4){0, 0, 0, w0});
      lds_st<u32x4>(hs + 12480 + 16, (u32x4){0, 0, w0 << 16, a0});
    }
    if (tA < 30) lds_st<u32x4>(up + 16 * tA, (u32x4){0, 0, 0, 0});
  }
  const unsigned vw = g2_vw(lane, s % 7);
  const int rot = g2_rot(s);
  auto group = [&](int g) {
#pragma unroll
    for (int n = 0; n < 4; ++n) { g2_piece(rw, vw, lw, 0, 4 * g + n, rot, lb); g2_piece(rw, vw, lw, 1, 4 * g + n, rot, lb); }
  };
  wait_vm<0>();
  asm volatile("" : "+v"(uA), "+v"(uB));
  lds_st<u32x4>(up + 480 + 16 * tA, uA);
  lds_st<u32x4>(up + 480 + 16 * (tA + 64), uB);
  bar();
  group(0); group(1); group(2);
  unsigned hf = 0;
  if (lw == 0) __builtin_amdgcn_s_sleep(HSLEEP);
  if (lw == 0) hf = __hip_atomic_load((const GAS unsigned*)(p.ws + WS_FH + x * 28 * 128 + flag_off(28, lane)), __ATOMIC_RELAXED, __HIP_MEMORY_SCOPE_AGENT);
  bar();
  const int hok = flags_ok(hf, tag);
  if (lw == 0 && lane == 0) lds_st<unsigned>(lb + L_OK, hok ? 1u : 0u);
  bar();
  if (lw == 0 && !hok) poll_flags(p.ws + WS_FH + x * 28 * 128, 28, tag, lane);
  wait_vm<0>();
  bar();
  group(3);
  bar();
  group(4);
  bar();
  group(5);
  wait_vm<16>();
  bar();
  group(6);
  wait_vm<16>();
  bar();
  wait_vm<8>();
  bar();
  int yok = 0;
  if (lw == 0) {
    const unsigned yf = __hip_atomic_load((const GAS unsigned*)(p.ws + WS_FY + (s % 7) * 32 * 128 + flag_off(32, lane)), __ATOMIC_RELAXED, __HIP_MEMORY_SCOPE_AGENT);
    yok = flags_ok(yf, tag) ? 1 : 0;
    if (lane == 0) lds_st<unsigned>(lb + 131072, (unsigned)yok);
  }
  wait_vm<0>();
  bar();
  if (lw == 0 && !yok) poll_flags(p.ws + WS_FY + (s % 7) * 32 * 128, 32, tag, lane);
  bar();
  bar();
  TSL_DUMP();
}

DI void phase_conv(const Params& p, unsigned lb, int x, int s, int w, int lane, Ctx& cx) {
  const int mi = x * 28 + s, chl = w >> 2, wq = w & 3, ch = 2 * mi + chl, t4 = wq * 64 + lane;
  const rsrc_t rws = mk_rsrc(p.ws, WS_END);
  const unsigned tag = cx.tag;
  const float dv = cx.dv;
  const unsigned up = lb + CV_UP + chl * 4608, hs = lb + CV_HS + chl * 16640;
  bar();
  TS(6);
  const int i = lane & 15, kg = lane >> 4, sg = (i + 1) & 3;
  f32x4 acc[8] = {};
  {
    const unsigned ga = up + 448 - 128 * wq + 32 * i + 16 * kg;
    unsigned f0 = hs + sg * 4160 + 128 * wq + 2 * (25 + i - 8 * kg - sg), f1 = f0 + 8;
    asm volatile("" : "+v"(f1));
    bf16x8 G[8], Fq[2];
    unsigned gb = ga;
    auto rdF = [&](int a) { Fq[a & 1] = cat(lds_ld<s16x4>(f0 + 512 * a), lds_ld<s16x4>(f1 + 512 * a)); };
    rdF(0);
#pragma unroll
    for (int b = 7; b >= 0; --b) G[b] = lds_ld<bf16x8>(gb + 512 * b);
#pragma unroll 1
    for (int rr = 0; rr < 2; ++rr) {
      gb -= 64;
#pragma unroll
      for (int a = 0; a < 8; ++a) {
        if (a == 5 && rr == 0) {
          wait_vm<0>();
          if (lane == 0) {
            const unsigned old = __hip_atomic_fetch_add((LAS unsigned*)(size_t)(lb + L_CNT), 1u, __ATOMIC_RELAXED, __HIP_MEMORY_SCOPE_WORKGROUP);
            if (old == 7u) post_flag(p.ws + WS_FH + ((4 * (x >> 2) + s / 7) * 28 + 7 * (x & 3) + s % 7) * 128, tag);
          }
        }
#pragma unroll
        for (int b = 7 - a; b >= 0; --b) {
          acc[a + b] = mfma16(Fq[a & 1], G[b], acc[a + b]);
          SB();
          if (b == 7 - a) {
            if (a == 7) { f0 += 64; f1 += 64; rdF(0); } else rdF(a + 1);
            G[7 - a] = lds_ld<bf16x8>(gb + 512 * (7 - a));
          }
          SB();
        }
      }
    }
  }
  bar();
  TS(7);
  const unsigned red = lb + CV_RED + chl * 24576;
  f32x4 y0, y1;
  {
    auto scatter = [&](auto WQ) {
      constexpr int q0 = decltype(WQ)::value;
#pragma unroll
      for (int o = 1; o < 4; ++o) {
        constexpr int dummy = 0; (void)dummy;
        const int dst = (q0 + o) & 3;
        lds_st<f32x4>(red + (dst * 3 + (o - 1)) * 2048 + lane * 16, acc[2 * dst]);
        lds_st<f32x4>(red + (dst * 3 + (o - 1)) * 2048 + 1024 + lane * 16, acc[2 * dst + 1]);
      }
      y0 = acc[2 * q0]; y1 = acc[2 * q0 + 1];
    };
    if (wq == 0) scatter(IC<0>{}); else if (wq == 1) scatter(IC<1>{}); else if (wq == 2) scatter(IC<2>{}); else scatter(IC<3>{});
  }
  const unsigned ua = up + 480 + 1024 * wq + 32 * i + 16 * (kg >> 1) + 8 * (1 - (kg & 1));
  const u32x2 ua0 = lds_ld<u32x2>(ua), ua1 = lds_ld<u32x2>(ua + 512);
  bar();
  cx.hok = __builtin_amdgcn_readfirstlane(lds_ld<unsigned>(lb + L_OK));
  {
    const int tok = 256 * x + 64 * (s / 7) + 16 * (w >> 1) + (lane & 15);
    const unsigned ox = tok * 896 + (s % 7) * 128 + (4 * (w & 1) + (lane >> 4)) * 16, os = WS_STATS + tok * 16;
    asm volatile("buffer_load_dwordx4 %0, %2, %4, 0 offen\n\tbuffer_load_dwordx4 %1, %3, %5, 0 offen sc1"
                 : "=&v"(cx.xq), "=&v"(cx.sg) : "v"(ox), "v"(os), "s"(mk_rsrc(p.q, S_ * D_ * 2)), "s"(rws) : "memory");
  }
  if (cx.hok) h_first(p, x, s, w, lane, cx);
#pragma unroll
  for (int o = 0; o < 3; ++o) {
    y0 += lds_ld<f32x4>(red + (wq * 3 + o) * 2048 + lane * 16);
    y1 += lds_ld<f32x4>(red + (wq * 3 + o) * 2048 + 1024 + lane * 16);
  }
  y0[0] = rbf(y0[0]) + rbf(dv * bf_hi(ua0[1])); y0[1] = rbf(y0[1]) + rbf(dv * bf_lo(ua0[1])); y0[2] = rbf(y0[2]) + rbf(dv * bf_hi(ua0[0])); y0[3] = rbf(y0[3]) + rbf(dv * bf_lo(ua0[0]));
  y1[0] = rbf(y1[0]) + rbf(dv * bf_hi(ua1[1])); y1[1] = rbf(y1[1]) + rbf(dv * bf_lo(ua1[1])); y1[2] = rbf(y1[2]) + rbf(dv * bf_hi(ua1[0])); y1[3] = rbf(y1[3]) + rbf(dv * bf_lo(ua1[0]));
  const unsigned d0 = pk_bf16(y0[0], y0[1]), d1 = pk_bf16(y0[2], y0[3]), e0 = pk_bf16(y1[0], y1[1]), e1 = pk_bf16(y1[2], y1[3]);
  const auto s0 = __builtin_amdgcn_permlane16_swap(d0, e0, false, false);
  const auto s1 = __builtin_amdgcn_permlane16_swap(d1, e1, false, false);
  const u32x4 o = {s0[0], s1[0], s0[1], s1[1]};
  const unsigned oy = WS_YT + ch * TROW + 512 * (2 * wq + (kg & 1)) + 32 * i + 8 * (kg & 2);
  asm volatile("buffer_store_dwordx4 %0, %1, %2, 0 offen sc1\n\ts_nop 1" ::"v"(o), "v"(oy), "s"(rws) : "memory");
}

DI void phase_gemm2(const Params& p, unsigned lb, int x, int s, int w, int lane, Ctx& cx) {
  const int i2 = s / 7, db = s % 7, th = w >> 2, kq = w & 3, rot = g2_rot(s);
  const unsigned tag = cx.tag;
  const rsrc_t rws = mk_rsrc(p.ws, WS_END);
  const rsrc_t rq = mk_rsrc(p.q, S_ * D_ * 2), ro = mk_rsrc(p.out, S_ * D_ * 2);
  const int tk = 16 * (w >> 1) + (lane & 15), ck = 4 * (w & 1) + (lane >> 4), tok = 256 * x + 64 * i2 + tk;
  bar();
  TS(10);
  const unsigned hb = WS_H + ((8 * x + 2 * i2 + th) * 112 + 28 * kq) * 1024;
  const unsigned vh = lane * 16;
  if (!cx.hok) h_first(p, x, s, w, lane, cx);
  u32x4 (&hr)[HD] = cx.hr;
  const int hh = lane >> 5, blk = (lane >> 4) & 1, q = (lane & 15) >> 2, pp = lane & 3;
  unsigned aa[2];
#pragma unroll
  for (int dt = 0; dt < 2; ++dt) aa[dt] = lb + kq * 8192 + (4 * hh + q) * 128 + 64 * (dt ^ ((q >> 1) & 1)) + 32 * blk + 8 * pp;
  f32x16 acc[2] = {};
  bf16x8 fa[2][2];
  static_assert(HD == 4, "one group of four k-steps per loop iteration");
  auto rd1 = [&](int j, int dt) { fa[j & 1][dt] = cat(lds_tr(aa[dt] + (j & 3) * 2048), lds_tr(aa[dt] + (j & 3) * 2048 + 1024)); };
  auto step = [&](int j, bool ahead) {
    SB();
    const bf16x8 b = __builtin_bit_cast(bf16x8, hr[j]);
    acc[0] = mfma32(fa[j & 1][0], b, acc[0]);
    SB();
    if (ahead) rd1(j + 1, 0);
    SB();
    acc[1] = mfma32(fa[j & 1][1], b, acc[1]);
    SB();
    if (ahead) rd1(j + 1, 1);
    SB();
  };
  u32x4 yv; int yok;
  rd1(0, 0); rd1(0, 1);
  int st = rot + 4; st -= st >= 28 ? 28 : 0;
#pragma unroll 1
  for (int g = 0; g < 6; ++g) {
#pragma unroll
    for (int j = 0; j < 4; ++j) {
      if (j == 3) {
        const unsigned d = (g & 3) == 3 ? (unsigned)-98304 : 32768u;
        aa[0] += d; aa[1] += d;
        bar();
        TS2(24 + g);
      }
      asm volatile("s_waitcnt vmcnt(3)" : "+v"(hr[j])::"memory");
      step(j, true);
      asm volatile("buffer_load_dwordx4 %0, %1, %2, %3 offen" HPOL : "=v"(hr[j]) : "v"(vh), "s"(rws), "s"(hb + st * 1024) : "memory");
      st = st == 27 ? 0 : st + 1;
      if (j == 3 && g == 2 && threadIdx.x == 0) post_flag(p.ws + WS_FY + (x * 28 + s) * 128, tag);
      SB();
    }
  }
  yok = __builtin_amdgcn_readfirstlane(lds_ld<unsigned>(lb + 131072));
  {
    const unsigned oy = yok ? WS_YT + (64 * db + (threadIdx.x >> 3)) * TROW + (256 * x + 64 * i2) * 2 + (threadIdx.x & 7) * 16 : 0x80000000u;
    asm volatile("buffer_load_dwordx4 %0, %1, %2, 0 offen sc1" : "=&v"(yv) : "v"(oy), "s"(rws) : "memory");
  }
  asm volatile("s_waitcnt vmcnt(4)" : "+v"(hr[0])::"memory"); step(0, true);
  asm volatile("s_waitcnt vmcnt(3)" : "+v"(hr[1])::"memory"); step(1, true);
  asm volatile("s_waitcnt vmcnt(2)" : "+v"(hr[2])::"memory"); step(2, true);
  asm volatile("s_waitcnt vmcnt(1)" : "+v"(hr[3])::"memory"); step(3, false);
  u32x4 xq = cx.xq, sg = cx.sg;
  TS(11);
  wait_vm<0>();
  asm volatile("" : "+v"(xq), "+v"(sg), "+v"(yv));
  bar();
  if (!yok) yv = __builtin_amdgcn_raw_buffer_load_b128(rws, WS_YT + (64 * db + (threadIdx.x >> 3)) * TROW + (256 * x + 64 * i2) * 2 + (threadIdx.x & 7) * 16, 0, 16);
  float mean = __uint_as_float(sg[1]), rstd = __uint_as_float(sg[3]);
  if (!__all(sg[0] == tag && sg[2] == tag)) stats_wait(rws, WS_STATS + tok * 16, tag, mean, rstd);
  const unsigned pb = lb, yb = lb + 73728;
  {
    const int r = lane & 31;
#pragma unroll
    for (int dt = 0; dt < 2; ++dt)
#pragma unroll
      for (int G = 0; G < 4; ++G) {
        const f32x4 t = {acc[dt][4 * G], acc[dt][4 * G + 1], acc[dt][4 * G + 2], acc[dt][4 * G + 3]};
        lds_st<f32x4>(pb + kq * 17408 + (32 * th + r) * 272 + (32 * dt + 8 * G + 4 * hh) * 4, t);
      }
  }
  lds_st<u32x4>(yb + (threadIdx.x >> 3) * 144 + (threadIdx.x & 7) * 16, yv);
  bar();
  f32x4 o0 = {}, o1 = {};
#pragma unroll
  for (int k4 = 0; k4 < 4; ++k4) {
    o0 += lds_ld<f32x4>(pb + k4 * 17408 + tk * 272 + ck * 32);
    o1 += lds_ld<f32x4>(pb + k4 * 17408 + tk * 272 + ck * 32 + 16);
  }
  const unsigned ya = yb + (8 * ck + q) * 144 + (16 * (w >> 1) + 4 * pp) * 2;
  const s16x4 y0 = lds_tr(ya), y1 = lds_tr(ya + 4 * 144);
  u32x4 ov;
#pragma unroll
  for (int e = 0; e < 8; e += 2) {
    const f32x2 o = e < 4 ? (f32x2){o0[e & 3], o0[(e & 3) + 1]} : (f32x2){o1[e & 3], o1[(e & 3) + 1]};
    const f32x2 xv = {bf_lo(xq[e >> 1]), bf_hi(xq[e >> 1])};
    const unsigned yp = e < 4 ? __builtin_bit_cast(u32x2, y0)[(e & 3) >> 1] : __builtin_bit_cast(u32x2, y1)[(e & 3) >> 1];
    const f32x2 yy = {bf_lo(yp), bf_hi(yp)};
    const f32x2 t = o * (f32x2){-1.4426950409f, -1.4426950409f};
    const f32x2 dn = (f32x2){__builtin_amdgcn_exp2f(t[0]), __builtin_amdgcn_exp2f(t[1])} + (f32x2){1.f, 1.f};
    const f32x2 g = o * (f32x2){__builtin_amdgcn_rcpf(dn[0]), __builtin_amdgcn_rcpf(dn[1])};
    const f32x2 xn = rbf2(rbf2(xv - (f32x2){mean, mean}) * (f32x2){rstd, rstd});
    const f32x2 r2 = __builtin_elementwise_fma(rbf2(xn * yy), g, xv);
    ov[e >> 1] = pk_bf16(r2[0], r2[1]);
  }
  __builtin_amdgcn_raw_buffer_store_b128(ov, ro, tok * 896 + db * 128 + ck * 16, 0, 0);
  TS(12);
}

__global__ __launch_bounds__(768, 3) void h3_geglu_kernel(Params p) {
  __shared__ __attribute__((aligned(16))) char smem[LDS_BYTES];
  const unsigned lb = (unsigned)(size_t)(LAS char*)smem;
  const int tid = threadIdx.x, lane = tid & 63, w = __builtin_amdgcn_readfirstlane(tid >> 6);
  const int b = blockIdx.x, x = b & 7, s = b >> 3;
  const char* pc;
  asm volatile("s_getpc_b64 %0" : "=s"(pc));
  TS(0);
  if (s < 28) {
    if (w >= 8) {
      loader_main(p, lb, x, s, w - 8, lane, pc);
    } else {
      Ctx cx;
      phase_gemm1(p, lb, x, s, w, lane, cx);
      phase_conv(p, lb, x, s, w, lane, cx);
      TS(9);
      phase_gemm2(p, lb, x, s, w, lane, cx);
    }
  } else {
    const int j = b - 224;
    if (j < 28) {
      const unsigned tag = __builtin_amdgcn_readfirstlane(*(const GAS unsigned*)(p.ws + WS_EPOCH) + 1u);
      const int m = w & 1, r0 = (w >> 1) * 8 + (lane >> 3);
      const rsrc_t rm = mk_rsrc(m ? p.wu : p.wg, D_ * F_ * 2);
      u32x4 t[10];
#pragma unroll
      for (int n = 0; n < 10; ++n) t[n] = __builtin_amdgcn_raw_buffer_load_b128(rm, r0 + 48 * n < D_ ? (r0 + 48 * n) * 3584 + j * 128 + (lane & 7) * 16 : 0x80000000u, 0, 0);
      float a[8] = {};
#pragma unroll
      for (int n = 0; n < 10; ++n)
#pragma unroll
        for (int i = 0; i < 4; ++i) { a[2 * i] += bf_lo(t[n][i]); a[2 * i + 1] += bf_hi(t[n][i]); }
      lds_st<f32x4>(lb + (w * 8 + (lane >> 3)) * 256 + (lane & 7) * 32, (f32x4){a[0], a[1], a[2], a[3]});
      lds_st<f32x4>(lb + (w * 8 + (lane >> 3)) * 256 + (lane & 7) * 32 + 16, (f32x4){a[4], a[5], a[6], a[7]});
      bar();
      if (w < 2) {
        float sum = 0.f;
#pragma unroll
        for (int q = 0; q < 48; ++q) sum += lds_ld<float>(lb + (((q >> 3) * 2 + w) * 8 + (q & 7)) * 256 + lane * 4);
        const u32x2 g = {tag, __float_as_uint(sum)};
        __builtin_amdgcn_raw_buffer_store_b64(g, mk_rsrc(p.ws, WS_END), WS_CS + (w * 1792 + j * 64 + lane) * 8, 0, 16);
      }
    }
    if (WARM) {
#pragma unroll 1
      for (int i = w; i < 28; i += 12) {
        const int blk = i * 32 + j;
        if (blk < 448) touch(mk_rsrc((blk & 1) ? p.v : p.k, S_ * D_ * 2), (unsigned)((blk >> 1) * 64 + lane) * 128u, lb + L_TOUCH);
        else if (blk < 644) touch(mk_rsrc(p.wd, F_ * D_ * 2), (unsigned)((blk - 448) * 64 + lane) * 128u, lb + L_TOUCH);
        else touch(mk_rsrc(p.h, D_ * S_ * 2), (unsigned)((blk - 644) * 64 + lane) * 128u, lb + L_TOUCH);
      }
    }
    if (b == 224 && w == 11) {
      const unsigned tag = __builtin_amdgcn_readfirstlane(*(const GAS unsigned*)(p.ws + WS_EPOCH) + 1u);
#pragma unroll 1
      for (int n = 0; n < 4; ++n) poll_flags_slow(p.ws + WS_FH + n * 56 * 128, 56, tag, lane);
      if (lane == 0) post_flag(p.ws + WS_EPOCH, tag);
    }
    wait_vm<0>();
  }
  TS(15);
}

extern "C" void kernel_launch(void* const* d_in, const int* in_sizes, int n_in, void* d_out, int out_size, void* d_ws, size_t ws_size, hipStream_t stream) {
  Params p;
  p.q = (const u16*)d_in[0]; p.k = (const u16*)d_in[1]; p.v = (const u16*)d_in[2]; p.sk = (const u16*)d_in[3];
  p.h = (const u16*)d_in[4]; p.dv = (const u16*)d_in[5];
  p.wg = (const u16*)d_in[7]; p.wu = (const u16*)d_in[8]; p.wd = (const u16*)d_in[9];
  p.out = (u16*)d_out; p.ws = (char*)d_ws;
  h3_geglu_kernel<<<256, 768, 0, stream>>>(p);
}
```

```cpp
#include <hip/hip_runtime.h>
#include <hip/hip_bf16.h>
#include <stdint.h>

typedef unsigned short u16;
typedef __attribute__((ext_vector_type(8))) short bf16x8;
typedef __attribute__((ext_vector_type(4))) short s16x4;
typedef __attribute__((ext_vector_type(4))) float f32x4;
typedef __attribute__((ext_vector_type(2))) float f32x2;
typedef __attribute__((ext_vector_type(16))) float f32x16;
typedef __attribute__((ext_vector_type(4))) unsigned u32x4;
typedef __attribute__((ext_vector_type(2))) unsigned u32x2;
typedef __attribute__((ext_vector_type(2))) __bf16 bf16x2;
typedef __amdgpu_buffer_rsrc_t rsrc_t;
#define HSPOL " sc1"
#define LAS __attribute__((address_space(3)))
#define GAS __attribute__((address_space(1)))
#define DI __device__ __forceinline__

#define OPT_OWN 0
#define KVT 1
#define KVFIRST 0
#define WDLATE 1
#define PAUX 2
#define USTAUX 16
#define HSLEEP 10
#define LPRIO 0
#define WARM 1
#define UPRIO 2
#define KVPOL " nt"
#define WPRIO 0
#define XPRIO 0
#define WDT 0
#define DRA 2
#define DRR 1
#define PFN 17

constexpr int S_ = 2048, D_ = 448, F_ = 1792;
constexpr unsigned WS_EPOCH = 0, WS_FH = 8192, WS_FY = 36864, WS_STATS = 65536, WS_FU = 98304, WS_CS = 2031616;
constexpr unsigned TROW = 4224;
constexpr unsigned WS_UT = 131072, WS_YT = 2097152, WS_H = 4194304, WS_END = WS_H + 7340032;
constexpr unsigned WS_DBG = 16u << 20;
constexpr int LDS_BYTES = 163840;
constexpr unsigned SPIN_CAP = 1u << 14;

struct Params {
  const u16 *q, *k, *v, *sk, *h, *dv, *wg, *wu, *wd;
  u16* out;
  char* ws;
};

DI rsrc_t mk_rsrc(const void* p, unsigned bytes) { return __builtin_amdgcn_make_buffer_rsrc((void*)p, (short)0, (int)bytes, 0x00020000); }
DI void dma16(rsrc_t r, unsigned voff, unsigned soff, unsigned lds) {
  asm volatile("s_mov_b32 m0, %0\n\ts_nop 0\n\tbuffer_load_dwordx4 %1, %2, %3 offen lds" ::"s"(lds), "v"(voff), "s"(r), "s"(soff) : "memory");
}
DI void touch(rsrc_t r, unsigned voff, unsigned lds) {
  asm volatile("s_mov_b32 m0, %0\n\ts_nop 0\n\tbuffer_load_dword %1, %2, 0 offen lds" ::"s"(lds), "v"(voff), "s"(r) : "memory");
}
template <int NT> DI void touch_p(rsrc_t r, unsigned voff, unsigned lds) {
  if (NT) asm volatile("s_mov_b32 m0, %0\n\ts_nop 0\n\tbuffer_load_dword %1, %2, 0 offen nt lds" ::"s"(lds), "v"(voff), "s"(r) : "memory");
  else touch(r, voff, lds);
}
DI void touch_g(const char* a, unsigned lds) {
  asm volatile("s_mov_b32 m0, %0\n\ts_nop 0\n\tglobal_load_lds_dword %1, off" ::"s"(lds), "v"(a) : "memory");
}
template <unsigned LA, unsigned SA>
DI void dma16c(rsrc_t r, unsigned voff, unsigned sbase, unsigned lbase) {
  unsigned t;
  asm volatile("s_add_u32 m0, %2, %5\n\ts_add_u32 %0, %3, %6\n\tbuffer_load_dwordx4 %1, %4, %0 offen lds"
               : "=&s"(t) : "v"(voff), "s"(lbase), "s"(sbase), "s"(r), "i"(LA), "i"(SA) : "memory", "scc");
}
template <int N> DI void wait_vm() { asm volatile("s_waitcnt vmcnt(%0)" ::"i"(N) : "memory"); }
DI void bar() { asm volatile("s_waitcnt lgkmcnt(0)\n\ts_barrier" ::: "memory"); }
DI float bf_lo(unsigned x) { return __uint_as_float(x << 16); }
DI float bf_hi(unsigned x) { return __uint_as_float(x & 0xffff0000u); }
DI unsigned pk_bf16(float lo, float hi) { f32x2 t = {lo, hi}; return __builtin_bit_cast(unsigned, __builtin_convertvector(t, bf16x2)); }
DI bf16x8 lds_b128(const char* p) { return *(const LAS bf16x8*)(p); }
DI s16x4 lds_tr(unsigned a) { return __builtin_amdgcn_ds_read_tr16_b64_v4i16((LAS s16x4*)(size_t)a); }
DI bf16x8 cat(s16x4 a, s16x4 b) { return __builtin_shufflevector(a, b, 0, 1, 2, 3, 4, 5, 6, 7); }
template <class T> DI T lds_ld(unsigned a) { return *(const LAS T*)(size_t)a; }
template <class T> DI void lds_st(unsigned a, T v) { *(LAS T*)(size_t)a = v; }
DI f32x16 mfma32(bf16x8 a, bf16x8 b, f32x16 c) { return __builtin_amdgcn_mfma_f32_32x32x16_bf16(a, b, c, 0, 0, 0); }
DI f32x4 mfma16(bf16x8 a, bf16x8 b, f32x4 c) { return __builtin_amdgcn_mfma_f32_16x16x32_bf16(a, b, c, 0, 0, 0); }
DI unsigned long long rtime() { return __builtin_amdgcn_s_memrealtime(); }
DI float dpp_add(float v, const int ctrl_row_shr) { return v; }
template <int CTRL, int RM> DI float dpp_mov(float v) { return __builtin_bit_cast(float, __builtin_amdgcn_update_dpp(0, __builtin_bit_cast(int, v), CTRL, RM, 0xf, true)); }
DI float wave_sum(float v) {
  v += dpp_mov<0x111, 0xf>(v); v += dpp_mov<0x112, 0xf>(v); v += dpp_mov<0x114, 0xf>(v); v += dpp_mov<0x118, 0xf>(v);
  v += dpp_mov<0x142, 0xa>(v); v += dpp_mov<0x143, 0xc>(v);
  return __builtin_bit_cast(float, __builtin_amdgcn_readlane(__builtin_bit_cast(int, v), 63));
}
#define TS(i)
#define TSL(i)
#define TSL_DECL
#define TSL_DUMP()
#define TS2(i)

DI void poll_flags(const char* base, int n, unsigned tag, int lane) {
  const GAS unsigned* f = (const GAS unsigned*)(base + (lane < n ? lane : 0) * 128);
  for (unsigned it = 0; it < SPIN_CAP; ++it) {
    unsigned x = __hip_atomic_load(f, __ATOMIC_RELAXED, __HIP_MEMORY_SCOPE_AGENT);
    if (__all(x == tag)) break;
    __builtin_amdgcn_s_sleep(2);
  }
}
DI void poll_flags_slow(const char* base, int n, unsigned tag, int lane) {
  const GAS unsigned* f = (const GAS unsigned*)(base + (lane < n ? lane : 0) * 128);
  for (unsigned it = 0; it < SPIN_CAP; ++it) {
    unsigned x = __hip_atomic_load(f, __ATOMIC_RELAXED, __HIP_MEMORY_SCOPE_AGENT);
    if (__all(x == tag)) break;
    __builtin_amdgcn_s_sleep(32);
  }
}
DI void post_flag(char* addr, unsigned tag) { __hip_atomic_store((GAS unsigned*)addr, tag, __ATOMIC_RELAXED, __HIP_MEMORY_SCOPE_AGENT); }

DI bool flags_ok(unsigned v, unsigned tag) { return __all(v == tag); }
DI unsigned flag_off(int n, int lane) { return (lane < n ? lane : 0) * 128; }
#define SB() __builtin_amdgcn_sched_barrier(0)
template <int V> struct IC { static constexpr int value = V; };
DI void asm_pin(unsigned& v) { asm volatile("" : "+v"(v)); }
DI float rbf(float x) { return __uint_as_float(pk_bf16(x, 0.f) << 16); }

DI f32x2 rbf2(f32x2 v) { const unsigned u = pk_bf16(v[0], v[1]); return (f32x2){bf_lo(u), bf_hi(u)}; }
DI u32x4 row_stats(u32x4 d, unsigned tag) {
  float e[8];
#pragma unroll
  for (int i = 0; i < 4; ++i) { e[2 * i] = bf_lo(d[i]); e[2 * i + 1] = bf_hi(d[i]); }
  float s = ((e[0] + e[1]) + (e[2] + e[3])) + ((e[4] + e[5]) + (e[6] + e[7]));
  const float mean = wave_sum(s) * (1.f / D_);
  const float on = (threadIdx.x & 63) < 56 ? 1.f : 0.f;
  float ss = 0.f;
#pragma unroll
  for (int i = 0; i < 8; ++i) { const float t = e[i] - mean; ss = fmaf(t, t, ss); }
  const float var = wave_sum(ss * on) * (1.f / D_);
  const float den = rbf(__builtin_amdgcn_sqrtf(rbf(rbf(var) + 1e-5f)));
  u32x4 g = {tag, __float_as_uint(rbf(mean)), tag, __float_as_uint(__builtin_amdgcn_rcpf(den))};
  return g;
}
DI void stats_wait(rsrc_t rs, unsigned off, unsigned tag, float& mean, float& rstd) {
  for (unsigned it = 0;; ++it) {
    const u32x4 g = __builtin_amdgcn_raw_buffer_load_b128(rs, off, 0, 16);
    mean = __uint_as_float(g[1]); rstd = __uint_as_float(g[3]);
    if (__all(g[0] == tag && g[2] == tag) || it >= SPIN_CAP) break;
    __builtin_amdgcn_s_sleep(1);
  }
}

constexpr unsigned CV_BASE = 98304;
constexpr unsigned CV_UP = CV_BASE;
constexpr unsigned CV_HS = CV_BASE + 9216;
constexpr unsigned CV_RED = CV_HS;
constexpr unsigned L_UT = 147456, L_SK = 155648, L_ST = 159744;
constexpr unsigned L_CS = 157696, L_OK = 158720, L_CNT = 158728, L_TOUCH = 158976;

#define HPOL " sc1"
#define HDEPTH 4
constexpr int HD = HDEPTH;
struct Ctx {
  unsigned tag; float dv; u32x4 hr[HD], xq, sg;
};
DI void g2_stage(rsrc_t rw, unsigned vw, int kq, int th, int grp, int rot, unsigned lb) {
#pragma unroll
  for (int n = 0; n < 4; ++n) {
    int st = 4 * grp + n + rot; st -= st >= 28 ? 28 : 0;
    dma16(rw, vw, (unsigned)((28 * kq + st) * 16 + 8 * th) * 896u, lb + (grp & 3) * 32768 + kq * 8192 + n * 2048 + th * 1024);
  }
}
DI void g2_piece(rsrc_t rw, unsigned vw, int kq, int th, int step, int rot, unsigned lb) {
  int st = step + rot; st -= st >= 28 ? 28 : 0;
  dma16(rw, vw, (unsigned)((28 * kq + st) * 16 + 8 * th) * 896u, lb + ((step >> 2) & 3) * 32768 + kq * 8192 + (step & 3) * 2048 + th * 1024);
}
DI void h_load(const Params& p, u32x4& dst, unsigned hb, int step, int rot, unsigned vh) {
  int st = step + rot; st -= st >= 28 ? 28 : 0;
  asm volatile("buffer_load_dwordx4 %0, %1, %2, %3 offen" HPOL : "=v"(dst) : "v"(vh), "s"(mk_rsrc(p.ws, WS_END)), "s"(hb + st * 1024) : "memory");
}
DI int g2_rot(int s);
DI void h_first(const Params& p, int x, int s, int w, int lane, Ctx& cx) {
  const unsigned hb = WS_H + ((8 * x + 2 * (s / 7) + (w >> 2)) * 112 + 28 * (w & 3)) * 1024;
#pragma unroll
  for (int n = 0; n < HD; ++n) h_load(p, cx.hr[n], hb, n, g2_rot(s), lane * 16);
}
DI unsigned g2_vw(int lane, int db) { const int L3 = lane >> 3, L7 = lane & 7; return L3 * 896 + db * 128 + 16 * (L7 ^ (((L3 >> 1) & 1) << 2)); }
DI int g2_rot(int s) { const int r = 4 * (s % 7) + 7 * (s / 7); return r >= 28 ? r - 28 : r; }

DI void phase_gemm1(const Params& p, unsigned lb, int x, int s, int w, int lane, Ctx& cx) {
  const int ib = 4 * (x >> 2) + s / 7, jb = 7 * (x & 3) + s % 7;
  const rsrc_t rq = mk_rsrc(p.q, S_ * D_ * 2), rws = mk_rsrc(p.ws, WS_END);
  const int gw = jb * 8 + w;
  unsigned tag;
  {
    u32x4 sr0, sr1; unsigned etag;
    const unsigned o0 = lane < 56 ? (256 * ib + gw) * 896 + 16 * lane : 0x80000000u;
    const unsigned o1 = (lane < 56 && gw < 32) ? (256 * ib + 224 + gw) * 896 + 16 * lane : 0x80000000u;
    const unsigned oz = WS_EPOCH;
    unsigned dvb; const unsigned od = (2 * (x * 28 + s) + (w >> 2)) * 2;
    asm volatile("buffer_load_dword %2, %6, %9, 0 offen\n\tbuffer_load_dwordx4 %0, %4, %8, 0 offen\n\tbuffer_load_dwordx4 %1, %5, %8, 0 offen\n\t"
                 "buffer_load_ushort %3, %7, %10, 0 offen\n\ts_waitcnt vmcnt(0)"
                 : "=&v"(sr0), "=&v"(sr1), "=&v"(etag), "=&v"(dvb) : "v"(o0), "v"(o1), "v"(oz), "v"(od), "s"(rq), "s"(rws), "s"(mk_rsrc(p.dv, D_ * 2)) : "memory");
    cx.dv = bf_lo(dvb);
    tag = __builtin_amdgcn_readfirstlane(etag) + 1u;
    cx.tag = tag;
    const u32x4 g0 = row_stats(sr0, tag), g1 = row_stats(sr1, tag);
    const unsigned p0 = lane == 0 ? WS_STATS + (256 * ib + gw) * 16 : 0x80000000u;
    const unsigned p1 = (lane == 0 && gw < 32) ? WS_STATS + (256 * ib + 224 + gw) * 16 : 0x80000000u;
    asm volatile("buffer_store_dwordx4 %0, %2, %4, 0 offen sc1\n\tbuffer_store_dwordx4 %1, %3, %4, 0 offen sc1\n\ts_nop 1" ::"v"(g0), "v"(g1), "v"(p0), "v"(p1), "s"(rws) : "memory");
  }
  const int tw = w & 3, fh = w >> 2, r = lane & 31, hh = lane >> 5;
  unsigned xa[4], wa[2];
#pragma unroll
  for (int i = 0; i < 4; ++i) xa[i] = lb + (64 * tw + r) * 128 + 16 * ((2 * i + hh) ^ ((r >> 1) & 7));
  {
    const int blk = (lane >> 4) & 1, q = (lane & 15) >> 2, pp = lane & 3, m = pp >> 1, f = (q >> 1) | ((m ^ hh) << 1);
#pragma unroll
    for (int u = 0; u < 2; ++u) wa[u] = lb + 32768 + m * 8192 + (8 * hh + q) * 128 + 32 * ((2 * fh + u) ^ f) + 16 * blk + 8 * (pp & 1);
  }
  f32x16 acc[2][2] = {};
  TS(1);
  bar();
  TS(2);
  bf16x8 fb[2][2] = {}, fa[2][2] = {};
  auto rdA = [&](int ks, int u) { fa[ks & 1][u] = cat(lds_tr(wa[u] + (ks & 3) * 2048), lds_tr(wa[u] + (ks & 3) * 2048 + 512)); };
  auto rdB = [&](int ks) { fb[ks & 1][0] = lds_ld<bf16x8>(xa[ks & 3]); fb[ks & 1][1] = lds_ld<bf16x8>(xa[ks & 3] + 4096); };
  auto step = [&](int ks, bool ahead) {
    const int pb = ks & 1;
    SB();
    acc[0][0] = mfma32(fa[pb][0], fb[pb][0], acc[0][0]);
    SB();
    if (ahead) rdA(ks + 1, 0);
    SB();
    acc[1][0] = mfma32(fa[pb][0], fb[pb][1], acc[1][0]);
    SB();
    if (ahead) rdB(ks + 1);
    SB();
    acc[0][1] = mfma32(fa[pb][1], fb[pb][0], acc[0][1]);
    SB();
    if (ahead) rdA(ks + 1, 1);
    SB();
    acc[1][1] = mfma32(fa[pb][1], fb[pb][1], acc[1][1]);
    SB();
  };
  rdA(0, 0); rdB(0); rdA(0, 1);
  int sl = 0;
#pragma unroll 1
  for (int c = 0; c < 6; ++c) {
    step(0, true);
    step(1, true);
    step(2, true);
    {
      const unsigned d = __builtin_amdgcn_readfirstlane(sl == 2 ? -98304 : 49152);
      sl = sl == 2 ? 0 : sl + 1;
#pragma unroll
      for (int i = 0; i < 4; ++i) xa[i] += d;
      wa[0] += d; wa[1] += d;
    }
    bar();
    TS2(16 + c);
    step(3, true);
    if (c == 5 && threadIdx.x == 0) post_flag(p.ws + WS_FU + (((x * 28 + s) % 7) * 32 + (x * 28 + s) / 7) * 128, tag);
  }
  step(0, true); step(1, true); step(2, true); step(3, false);
  TS(3);
  const unsigned csb = lb + L_CS;
  bar();
  TS(4);
  float mean[2], rstd[2];
#pragma unroll
  for (int tt = 0; tt < 2; ++tt) { const f32x2 t = lds_ld<f32x2>(lb + L_ST + (64 * tw + 32 * tt + r) * 8); mean[tt] = t[0]; rstd[tt] = t[1]; }
  constexpr float C0 = -2.3022081985f, C1 = -0.1029432397f;
#pragma unroll
  for (int u = 0; u < 2; ++u) {
    f32x4 cg[2], cu[2];
#pragma unroll
    for (int b = 0; b < 2; ++b) {
      cg[b] = lds_ld<f32x4>(csb + (16 * (2 * fh + u) + 8 * b + 4 * hh) * 4);
      cu[b] = lds_ld<f32x4>(csb + 256 + (16 * (2 * fh + u) + 8 * b + 4 * hh) * 4);
    }
#pragma unroll
    for (int tt = 0; tt < 2; ++tt) {
      const float rr = rstd[tt], k0s = rr * C0, k1s = C1 * rr * rr * rr, ir2s = __builtin_amdgcn_rcpf(rr * rr);
      const f32x2 nm = {-mean[tt], -mean[tt]}, k0 = {k0s, k0s}, k1 = {k1s, k1s}, ir2 = {ir2s, ir2s};
      u32x4 o;
#pragma unroll
      for (int b = 0; b < 2; ++b)
#pragma unroll
        for (int e = 0; e < 4; e += 2) {
          const f32x2 ag = {acc[tt][u][8 * b + e], acc[tt][u][8 * b + e + 1]}, au = {acc[tt][u][8 * b + 4 + e], acc[tt][u][8 * b + 5 + e]};
          const f32x2 c2g = {cg[b][e], cg[b][e + 1]}, c2u = {cu[b][e], cu[b][e + 1]};
          const f32x2 a = __builtin_elementwise_fma(nm, c2g, ag), bb = __builtin_elementwise_fma(nm, c2u, au);
          const f32x2 z = a * __builtin_elementwise_fma(a * a, k1, k0);
          const f32x2 ex = {__builtin_amdgcn_exp2f(z[0]), __builtin_amdgcn_exp2f(z[1])};
          const f32x2 dn = __builtin_elementwise_fma(ex, ir2, ir2);
          const f32x2 rc = {__builtin_amdgcn_rcpf(dn[0]), __builtin_amdgcn_rcpf(dn[1])};
          const f32x2 hv = (a * bb) * rc;
          o[2 * b + (e >> 1)] = pk_bf16(hv[0], hv[1]);
        }
      const unsigned oh = WS_H + ((8 * ib + 2 * tw + tt) * 112 + 4 * jb + 2 * fh + u) * 1024 + lane * 16;
      asm volatile("buffer_store_dwordx4 %0, %1, %2, 0 offen" HSPOL "\n\ts_nop 1" ::"v"(o), "v"(oh), "s"(rws) : "memory");
    }
  }
  TS(5);
}

constexpr int CODE_LINES = 208;
struct WdStage {
  rsrc_t rw; unsigned vw, lb; int kq, th, rot;
  DI void group(int g) const {
#pragma unroll
    for (int n = 0; n < 4; ++n) g2_piece(rw, vw, kq, th, 4 * g + n, rot, lb);
  }
};
#define TAILARG
#define TAILPASS
DI void loader_tail(const Params& p, unsigned lb, int s, int lane, const WdStage& wd, bool poller, unsigned tag TAILARG) {
  TSL(12);
  bar();
  TSL(13);
  wd.group(3);
  TSL(14);
  bar();
  wd.group(4);
  bar();
  wd.group(5);
  TSL(22);
  wait_vm<8>();
  TSL(23);
  bar();
  wd.group(6);
  wait_vm<8>();
  TSL(30);
  bar();
  wait_vm<4>();
  bar();
  if (poller) {
    const unsigned yf = __hip_atomic_load((const GAS unsigned*)(p.ws + WS_FY + (s % 7) * 32 * 128 + flag_off(32, lane)), __ATOMIC_RELAXED, __HIP_MEMORY_SCOPE_AGENT);
    const unsigned ok = flags_ok(yf, tag) ? 1u : 0u;
    if (lane == 0) lds_st<unsigned>(lb + 131072, ok);
  }
  wait_vm<0>();
  bar();
  const int yok = __builtin_amdgcn_readfirstlane(lds_ld<unsigned>(lb + 131072));
  if (poller && !yok) poll_flags(p.ws + WS_FY + (s % 7) * 32 * 128, 32, tag, lane);
  bar();
  if (!yok) bar();
}

DI void loader_x(const Params& p, unsigned lb, int x, int s, int lw, int lane) {
  const int ib = 4 * (x >> 2) + s / 7;
  const rsrc_t rq = mk_rsrc(p.q, S_ * D_ * 2);
  const int L3 = lane >> 3, L7 = lane & 7;
  const unsigned vx0 = (64 * lw + L3) * 896 + 16 * (L7 ^ ((lane >> 4) & 3));
  const unsigned vx1 = (64 * lw + 8 + L3) * 896 + 16 * (L7 ^ (((lane >> 4) & 3) | 4));
  const unsigned sx = ib * 256 * 896, lx = lb + lw * 8192;
  auto piece = [&](auto C, auto I) {
    constexpr int c = decltype(C)::value, i = decltype(I)::value;
    dma16c<(c % 3) * 49152 + i * 1024, c * 128 + (i >> 1) * 16 * 896>(rq, (i & 1) ? vx1 : vx0, sx, lx);
  };
  auto stage = [&](auto C) {
    piece(C, IC<0>{}); piece(C, IC<1>{}); piece(C, IC<2>{}); piece(C, IC<3>{}); piece(C, IC<4>{}); piece(C, IC<5>{}); piece(C, IC<6>{}); piece(C, IC<7>{});
  };
  if (XPRIO) __builtin_amdgcn_s_setprio(XPRIO);
  const rsrc_t rws = mk_rsrc(p.ws, WS_END);
  unsigned etag;
  TSL_DECL
  TS(8);
  { const unsigned oz = WS_EPOCH; asm volatile("buffer_load_dword %0, %1, %2, 0 offen" : "=&v"(etag) : "v"(oz), "s"(rws) : "memory"); }
  stage(IC<0>{});
  stage(IC<1>{});
  wait_vm<8>();
  asm volatile("" : "+v"(etag));
  const unsigned tag = __builtin_amdgcn_readfirstlane(etag) + 1u;
  TS(1);
  bar();
  stage(IC<2>{});
  auto lstage = [&](auto C) {
    constexpr int c = decltype(C)::value;
    TSL(16 + c);
    if (c < 5) wait_vm<8>(); else wait_vm<0>();
    TSL(24 + c);
    bar();
    TSL(2 + c);
    if constexpr (c + 3 <= 6) stage(IC<c + 3>{});
    if (c == 4) {
      const unsigned os = WS_STATS + (256 * ib + 64 * lw + lane) * 16;
      float mean, rstd;
      stats_wait(rws, os, tag, mean, rstd);
      lds_st<f32x2>(lb + L_ST + (64 * lw + lane) * 8, (f32x2){mean, rstd});
    }
  };
  lstage(IC<0>{}); lstage(IC<1>{}); lstage(IC<2>{}); lstage(IC<3>{}); lstage(IC<4>{}); lstage(IC<5>{});
  bar();
  bar();
  const WdStage wd = {mk_rsrc(p.wd, F_ * D_ * 2), g2_vw(lane, s % 7), lb, lw, 0, g2_rot(s)};
  TSL(8);
  wd.group(0); wd.group(1); wd.group(2);
  TSL(9);
  bar();
  bar();
  wait_vm<0>();
  loader_tail(p, lb, s, lane, wd, false, 0u TAILPASS);
  TSL_DUMP();
}

DI void loader_w(const Params& p, unsigned lb, int x, int s, int lw, int lane) {
  const int jb = 7 * (x & 3) + s % 7;
  const rsrc_t rg = mk_rsrc(p.wg, D_ * F_ * 2), ru = mk_rsrc(p.wu, D_ * F_ * 2);
  const rsrc_t rw = mk_rsrc(p.wd, F_ * D_ * 2), rws = mk_rsrc(p.ws, WS_END);
  const int L3 = lane >> 3, L7 = lane & 7;
  const unsigned vwA = (16 * lw + L3) * 3584 + jb * 128 + 32 * ((L7 >> 1) ^ ((lane >> 4) & 1)) + 16 * (L7 & 1);
  const unsigned vwB = (16 * lw + L3) * 3584 + jb * 128 + 32 * ((L7 >> 1) ^ (((lane >> 4) & 1) | 2)) + 16 * (L7 & 1);
  const unsigned lx = lb + 32768 + lw * 2048;
  auto stage = [&](auto C) {
    constexpr int c = decltype(C)::value;
    constexpr int cm = c;
    dma16c<(c % 3) * 49152, cm * 64 * 3584>(rg, vwA, 0, lx);
    dma16c<(c % 3) * 49152 + 1024, cm * 64 * 3584 + 8 * 3584>(rg, vwB, 0, lx);
    dma16c<(c % 3) * 49152 + 8192, cm * 64 * 3584>(ru, vwB, 0, lx);
    dma16c<(c % 3) * 49152 + 8192 + 1024, cm * 64 * 3584 + 8 * 3584>(ru, vwA, 0, lx);
  };
  if (WPRIO) __builtin_amdgcn_s_setprio(WPRIO);
  unsigned etag;
  { const unsigned oz = WS_EPOCH; asm volatile("buffer_load_dword %0, %1, %2, 0 offen" : "=&v"(etag) : "v"(oz), "s"(rws) : "memory"); }
  stage(IC<0>{});
  stage(IC<1>{});
  wait_vm<4>();
  asm volatile("" : "+v"(etag));
  const unsigned tag = __builtin_amdgcn_readfirstlane(etag) + 1u;
  bar();
  const int mi = x * 28 + s, cb = mi % 7, tb = mi / 7, ucg = lane >> 3, urp = lane & 7, ut0 = 64 * tb + 16 * lw + 2 * urp;
  u32x4 kk[5], vv[2];
  stage(IC<2>{});
  {
    const rsrc_t rk = mk_rsrc(p.k, S_ * D_ * 2), rv = mk_rsrc(p.v, S_ * D_ * 2);
    unsigned ok[5];
#pragma unroll
    for (int n = 0; n < 5; ++n) ok[n] = ut0 - 3 + n >= 0 ? (unsigned)((ut0 - 3 + n) * 896 + cb * 128 + ucg * 16) : 0x80000000u;
    const unsigned ov = ut0 * 896 + cb * 128 + ucg * 16;
    asm volatile("buffer_load_dwordx4 %0, %7, %13, 0 offen" KVPOL "\n\tbuffer_load_dwordx4 %1, %8, %13, 0 offen" KVPOL "\n\tbuffer_load_dwordx4 %2, %9, %13, 0 offen" KVPOL "\n\t"
                 "buffer_load_dwordx4 %3, %10, %13, 0 offen" KVPOL "\n\tbuffer_load_dwordx4 %4, %11, %13, 0 offen" KVPOL "\n\t"
                 "buffer_load_dwordx4 %5, %12, %14, 0 offen" KVPOL "\n\tbuffer_load_dwordx4 %6, %12, %14, 0 offen offset:896" KVPOL
                 : "=&v"(kk[0]), "=&v"(kk[1]), "=&v"(kk[2]), "=&v"(kk[3]), "=&v"(kk[4]), "=&v"(vv[0]), "=&v"(vv[1])
                 : "v"(ok[0]), "v"(ok[1]), "v"(ok[2]), "v"(ok[3]), "v"(ok[4]), "v"(ov), "s"(rk), "s"(rv) : "memory");
    dma16(mk_rsrc(p.sk, D_ * 8), lane < 32 ? cb * 512 + lane * 16 : 0x80000000u, 0, lb + L_SK);
  }
  auto wd_touch = [&](int i) {
    const int sh = 2 * i + (lw & 1);
    if ((lw >> 1) == (i & 1) && sh < 7) touch(rw, (unsigned)((s * 7 + sh) * 64 + lane) * 128u, lb + L_TOUCH);
  };
  const int chl = lw >> 1, ch = 2 * mi + chl, tA = (lw & 1) * 128 + lane;
  u32x4 hA, hB; u32x2 gA, gB, cg2; unsigned uf = 0;
  auto upairs = [&](const int j0) {
    const int tl = 16 * lw + 2 * urp, e = (tl & ~7) + 6 - (tl & 7);
    const unsigned ta = lb + L_UT + 8 * ucg * 128 + (((e >> 3) ^ ucg) * 16) + 2 * (e & 7);
    u32x4 wq[2];
#pragma unroll
    for (int j = 0; j < 2; ++j) wq[j] = lds_ld<u32x4>(lb + L_SK + ucg * 64 + (j0 + j) * 16);
    f32x2 kf[2][5], vf[2][2], wt[2][4], s0[2], s1[2];
#pragma unroll
    for (int j = 0; j < 2; ++j) {
#pragma unroll
      for (int n = 0; n < 5; ++n) kf[j][n] = (f32x2){bf_lo(kk[n][j0 + j]), bf_hi(kk[n][j0 + j])};
#pragma unroll
      for (int n = 0; n < 2; ++n) vf[j][n] = (f32x2){bf_lo(vv[n][j0 + j]), bf_hi(vv[n][j0 + j])};
    }
#pragma unroll
    for (int j = 0; j < 2; ++j) {
      wt[j][0] = (f32x2){bf_lo(wq[j][0]), bf_lo(wq[j][2])}; wt[j][1] = (f32x2){bf_hi(wq[j][0]), bf_hi(wq[j][2])};
      wt[j][2] = (f32x2){bf_lo(wq[j][1]), bf_lo(wq[j][3])}; wt[j][3] = (f32x2){bf_hi(wq[j][1]), bf_hi(wq[j][3])};
    }
    SB();
#pragma unroll
    for (int j = 0; j < 2; ++j) { s0[j] = kf[j][0] * wt[j][3]; s1[j] = kf[j][1] * wt[j][3]; }
    SB();
#pragma unroll
    for (int t = 2; t >= 0; --t) {
#pragma unroll
      for (int j = 0; j < 2; ++j) { s0[j] = __builtin_elementwise_fma(kf[j][3 - t], wt[j][t], s0[j]); s1[j] = __builtin_elementwise_fma(kf[j][4 - t], wt[j][t], s1[j]); }
      SB();
    }
#pragma unroll
    for (int j = 0; j < 2; ++j) { s0[j] *= vf[j][0]; s1[j] *= vf[j][1]; }
    SB();
#pragma unroll
    for (int j = 0; j < 2; ++j) {
      lds_st<unsigned>(ta + (2 * (j0 + j)) * 128, pk_bf16(s1[j][0], s0[j][0]));
      lds_st<unsigned>(ta + (2 * (j0 + j) + 1) * 128, pk_bf16(s1[j][1], s0[j][1]));
    }
  };
  TSL_DECL
  auto lstage = [&](auto C) {
    constexpr int c = decltype(C)::value;
    TSL(16 + c);
    if (c == 0) wait_vm<12>(); else if (c < 4) wait_vm<4>(); else if (c == 4) wait_vm<6>(); else wait_vm<5>();
    TSL(24 + c);
    bar();
    TSL(2 + c);
    if (c == 3) {
#pragma unroll
      for (int n = 0; n < 2; ++n) {
        const int cl = 16 * lw + 8 * n + (lane >> 3);
        const u32x4 t = lds_ld<u32x4>(lb + L_UT + cl * 128 + (((lane & 7) ^ (cl >> 3)) * 16));
        const unsigned o = WS_UT + (64 * cb + cl) * TROW + 128 * tb + (lane & 7) * 16;
        asm volatile("buffer_store_dwordx4 %0, %1, %2, 0 offen sc1\n\ts_nop 1" ::"v"(t), "v"(o), "s"(rws) : "memory");
      }
    }
    if (c == 4) {
      const rsrc_t rh = mk_rsrc(p.h, D_ * S_ * 2);
      const unsigned oh = ch * 4096 + tA * 16;
      asm volatile("buffer_load_dwordx4 %0, %4, %5, 0 offen nt\n\tbuffer_load_dwordx2 %1, %4, %5, 0 offen offset:16 nt\n\t"
                   "buffer_load_dwordx4 %2, %4, %5, 0 offen offset:1024 nt\n\tbuffer_load_dwordx2 %3, %4, %5, 0 offen offset:1040 nt"
                   : "=&v"(hA), "=&v"(gA), "=&v"(hB), "=&v"(gB) : "v"(oh), "s"(rh) : "memory");
      const unsigned oc = lw < 2 ? WS_CS + (lw * 1792 + jb * 64 + lane) * 8 : 0x80000000u;
      asm volatile("buffer_load_dwordx2 %0, %1, %2, 0 offen sc1" : "=&v"(cg2) : "v"(oc), "s"(rws) : "memory");
    }
    if (c == 5) {
      const unsigned of = WS_FU + (mi >> 5) * 32 * 128 + flag_off(32, lane);
      asm volatile("buffer_load_dword %0, %1, %2, 0 offen sc1" : "=&v"(uf) : "v"(of), "s"(rws) : "memory");
    }
    if (c == 1) {
      asm volatile("" : "+v"(kk[0]), "+v"(kk[1]), "+v"(kk[2]), "+v"(kk[3]), "+v"(kk[4]), "+v"(vv[0]), "+v"(vv[1]));
      upairs(0);
    }
    if (c == 2) upairs(2);
    if constexpr (c + 3 <= 6) stage(IC<c + 3>{});
    if (WDT && c >= 2) wd_touch(c - 2);
  };
  lstage(IC<0>{}); lstage(IC<1>{}); lstage(IC<2>{}); lstage(IC<3>{}); lstage(IC<4>{}); lstage(IC<5>{});
  asm volatile("s_waitcnt vmcnt(1)" : "+v"(cg2)::"memory");
  if (lw < 2) {
    if (!__all(cg2[0] == tag)) {
      for (unsigned it = 0; it < SPIN_CAP; ++it) {
        cg2 = __builtin_amdgcn_raw_buffer_load_b64(rws, WS_CS + (lw * 1792 + jb * 64 + lane) * 8, 0, 16);
        if (__all(cg2[0] == tag)) break;
        __builtin_amdgcn_s_sleep(1);
      }
    }
    lds_st<unsigned>(lb + L_CS + lw * 256 + lane * 4, cg2[1]);
  }
  if (lw == 0 && lane == 0) { lds_st<unsigned>(lb + L_CNT, 0u); }
  bar();
  wait_vm<0>();
  asm volatile("" : "+v"(hA), "+v"(gA), "+v"(hB), "+v"(gB), "+v"(uf));
  if (!flags_ok(uf, tag)) poll_flags(p.ws + WS_FU + (mi >> 5) * 32 * 128, 32, tag, lane);
  u32x4 uA, uB;
  {
    const unsigned ou = WS_UT + ch * TROW + tA * 16;
    asm volatile("buffer_load_dwordx4 %0, %2, %3, 0 offen sc1\n\tbuffer_load_dwordx4 %1, %2, %3, 0 offen offset:1024 sc1" : "=&v"(uA), "=&v"(uB) : "v"(ou), "s"(rws) : "memory");
  }
  const unsigned up = lb + CV_UP + chl * 4608, hs = lb + CV_HS + chl * 16640;
  {
    auto copies = [&](const u32x4& h4, const u32x2& h2, int t4) {
      const unsigned w0 = h4[0], w1 = h4[1], w2 = h4[2], w3 = h4[3], w4 = h2[0], w5 = h2[1];
      const unsigned a0 = __builtin_amdgcn_alignbit(w1, w0, 16), a1 = __builtin_amdgcn_alignbit(w2, w1, 16), a2 = __builtin_amdgcn_alignbit(w3, w2, 16),
                     a3 = __builtin_amdgcn_alignbit(w4, w3, 16), a4 = __builtin_amdgcn_alignbit(w5, w4, 16);
      const unsigned o = hs + 32 + 16 * t4;
      lds_st<u32x4>(o, (u32x4){w0, w1, w2, w3});
      lds_st<u32x4>(o + 4160, (u32x4){a0, a1, a2, a3});
      lds_st<u32x4>(o + 8320, (u32x4){w1, w2, w3, w4});
      lds_st<u32x4>(o + 12480, (u32x4){a1, a2, a3, a4});
    };
    copies(hA, gA, tA); copies(hB, gB, tA + 64);
    if (tA == 0) {
      const unsigned w0 = hA[0], a0 = __builtin_amdgcn_alignbit(hA[1], hA[0], 16);
      const u32x4 z = {0, 0, 0, 0};
#pragma unroll
      for (int sg = 0; sg < 4; ++sg) lds_st<u32x4>(hs + sg * 4160, z);
      lds_st<u32x4>(hs + 16, z);
      lds_st<u32x4>(hs + 4160 + 16, (u32x4){0, 0, 0, w0 << 16});
      lds_st<u32x4>(hs + 8320 + 16, (u32x4){0, 0, 0, w0});
      lds_st<u32x4>(hs + 12480 + 16, (u32x4){0, 0, w0 << 16, a0});
    }
    if (tA < 30) lds_st<u32x4>(up + 16 * tA, (u32x4){0, 0, 0, 0});
  }
  wait_vm<0>();
  asm volatile("" : "+v"(uA), "+v"(uB));
  lds_st<u32x4>(up + 480 + 16 * tA, uA);
  lds_st<u32x4>(up + 480 + 16 * (tA + 64), uB);
  bar();
  const WdStage wd = {rw, g2_vw(lane, s % 7), lb, lw, 1, g2_rot(s)};
  TSL(8);
  wd.group(0); wd.group(1); wd.group(2);
  TSL(9);
  bar();
  TSL(10);
  if (lw == 0) poll_flags(p.ws + WS_FH + x * 28 * 128, 28, tag, lane);
  TSL(11);
  bar();
  wait_vm<0>();
  loader_tail(p, lb, s, lane, wd, lw == 0, tag TAILPASS);
  TSL_DUMP();
}

DI void phase_conv(const Params& p, unsigned lb, int x, int s, int w, int lane, Ctx& cx) {
  const int mi = x * 28 + s, chl = w >> 2, wq = w & 3, ch = 2 * mi + chl, t4 = wq * 64 + lane;
  const rsrc_t rws = mk_rsrc(p.ws, WS_END);
  const unsigned tag = cx.tag;
  const float dv = cx.dv;
  const unsigned up = lb + CV_UP + chl * 4608, hs = lb + CV_HS + chl * 16640;
  bar();
  TS(6);
  const int i = lane & 15, kg = lane >> 4, sg = (i + 1) & 3;
  f32x4 acc[8] = {};
  {
    const unsigned ga = up + 448 - 128 * wq + 32 * i + 16 * kg;
    unsigned f0 = hs + sg * 4160 + 128 * wq + 2 * (25 + i - 8 * kg - sg), f1 = f0 + 8;
    asm volatile("" : "+v"(f1));
    auto publish_h = [&]() {
      wait_vm<0>();
      if (lane == 0) {
        const unsigned old = __hip_atomic_fetch_add((LAS unsigned*)(size_t)(lb + L_CNT), 1u, __ATOMIC_RELAXED, __HIP_MEMORY_SCOPE_WORKGROUP);
        if (old == 7u) post_flag(p.ws + WS_FH + ((4 * (x >> 2) + s / 7) * 28 + 7 * (x & 3) + s % 7) * 128, tag);
      }
    };
    bf16x8 G[8], Fq[4];
    unsigned gb = ga;
    auto rdF = [&](int a, int dres) { Fq[a & 3] = cat(lds_ld<s16x4>(f0 + 64 * dres + 512 * (a & 7)), lds_ld<s16x4>(f1 + 64 * dres + 512 * (a & 7))); };
    rdF(0, 0); rdF(1, 0);
#pragma unroll
    for (int b = 7; b >= 0; --b) G[b] = lds_ld<bf16x8>(gb + 512 * b);
#pragma unroll 1
    for (int rr = 0; rr < 2; ++rr) {
      gb -= 64;
#pragma unroll
      for (int a = 0; a < 8; ++a) {
        if (a == DRA && rr == DRR) publish_h();
#pragma unroll
        for (int b = 7 - a; b >= 0; --b) {
          acc[a + b] = mfma16(Fq[a & 3], G[b], acc[a + b]);
          SB();
          if (b == 7 - a) {
            rdF(a + 2, a + 2 >= 8 ? 1 : 0);
            G[7 - a] = lds_ld<bf16x8>(gb + 512 * (7 - a));
          }
          SB();
        }
      }
      f0 += 64; f1 += 64;
    }
    if (DRR == 2) publish_h();
  }
  TS2(13);
  bar();
  TS(7);
  const unsigned red = lb + CV_RED + chl * 24576;
  f32x4 y0, y1;
  {
    auto scatter = [&](auto WQ) {
      constexpr int q0 = decltype(WQ)::value;
#pragma unroll
      for (int o = 1; o < 4; ++o) {
        constexpr int dummy = 0; (void)dummy;
        const int dst = (q0 + o) & 3;
        lds_st<f32x4>(red + (dst * 3 + (o - 1)) * 2048 + lane * 16, acc[2 * dst]);
        lds_st<f32x4>(red + (dst * 3 + (o - 1)) * 2048 + 1024 + lane * 16, acc[2 * dst + 1]);
      }
      y0 = acc[2 * q0]; y1 = acc[2 * q0 + 1];
    };
    if (wq == 0) scatter(IC<0>{}); else if (wq == 1) scatter(IC<1>{}); else if (wq == 2) scatter(IC<2>{}); else scatter(IC<3>{});
  }
  const unsigned ua = up + 480 + 1024 * wq + 32 * i + 16 * (kg >> 1) + 8 * (1 - (kg & 1));
  const u32x2 ua0 = lds_ld<u32x2>(ua), ua1 = lds_ld<u32x2>(ua + 512);
  bar();
  {
    const int tok = 256 * x + 64 * (s / 7) + 16 * (w >> 1) + (lane & 15);
    const unsigned ox = tok * 896 + (s % 7) * 128 + (4 * (w & 1) + (lane >> 4)) * 16, os = WS_STATS + tok * 16;
    asm volatile("buffer_load_dwordx4 %0, %2, %4, 0 offen\n\tbuffer_load_dwordx4 %1, %3, %5, 0 offen sc1"
                 : "=&v"(cx.xq), "=&v"(cx.sg) : "v"(ox), "v"(os), "s"(mk_rsrc(p.q, S_ * D_ * 2)), "s"(rws) : "memory");
  }
  h_first(p, x, s, w, lane, cx);
#pragma unroll
  for (int o = 0; o < 3; ++o) {
    y0 += lds_ld<f32x4>(red + (wq * 3 + o) * 2048 + lane * 16);
    y1 += lds_ld<f32x4>(red + (wq * 3 + o) * 2048 + 1024 + lane * 16);
  }
  y0[0] = rbf(y0[0]) + rbf(dv * bf_hi(ua0[1])); y0[1] = rbf(y0[1]) + rbf(dv * bf_lo(ua0[1])); y0[2] = rbf(y0[2]) + rbf(dv * bf_hi(ua0[0])); y0[3] = rbf(y0[3]) + rbf(dv * bf_lo(ua0[0]));
  y1[0] = rbf(y1[0]) + rbf(dv * bf_hi(ua1[1])); y1[1] = rbf(y1[1]) + rbf(dv * bf_lo(ua1[1])); y1[2] = rbf(y1[2]) + rbf(dv * bf_hi(ua1[0])); y1[3] = rbf(y1[3]) + rbf(dv * bf_lo(ua1[0]));
  const unsigned d0 = pk_bf16(y0[0], y0[1]), d1 = pk_bf16(y0[2], y0[3]), e0 = pk_bf16(y1[0], y1[1]), e1 = pk_bf16(y1[2], y1[3]);
  const auto s0 = __builtin_amdgcn_permlane16_swap(d0, e0, false, false);
  const auto s1 = __builtin_amdgcn_permlane16_swap(d1, e1, false, false);
  const u32x4 o = {s0[0], s1[0], s0[1], s1[1]};
  const unsigned oy = WS_YT + ch * TROW + 512 * (2 * wq + (kg & 1)) + 32 * i + 8 * (kg & 2);
  asm volatile("buffer_store_dwordx4 %0, %1, %2, 0 offen sc1\n\ts_nop 1" ::"v"(o), "v"(oy), "s"(rws) : "memory");
}

DI void phase_gemm2(const Params& p, unsigned lb, int x, int s, int w, int lane, Ctx& cx) {
  const int i2 = s / 7, db = s % 7, th = w >> 2, kq = w & 3, rot = g2_rot(s);
  const unsigned tag = cx.tag;
  const rsrc_t rws = mk_rsrc(p.ws, WS_END);
  const rsrc_t rq = mk_rsrc(p.q, S_ * D_ * 2), ro = mk_rsrc(p.out, S_ * D_ * 2);
  const int tk = 16 * (w >> 1) + (lane & 15), ck = 4 * (w & 1) + (lane >> 4), tok = 256 * x + 64 * i2 + tk;
  bar();
  TS(10);
  const unsigned hb = WS_H + ((8 * x + 2 * i2 + th) * 112 + 28 * kq) * 1024;
  const unsigned vh = lane * 16;
  u32x4 (&hr)[HD] = cx.hr;
  const int hh = lane >> 5, blk = (lane >> 4) & 1, q = (lane & 15) >> 2, pp = lane & 3;
  unsigned aa[2];
#pragma unroll
  for (int dt = 0; dt < 2; ++dt) aa[dt] = lb + kq * 8192 + (4 * hh + q) * 128 + 64 * (dt ^ ((q >> 1) & 1)) + 32 * blk + 8 * pp;
  f32x16 acc[2] = {};
  bf16x8 fa[2][2];
  static_assert(HD == 4, "one group of four k-steps per loop iteration");
  auto rd1 = [&](int j, int dt) { fa[j & 1][dt] = cat(lds_tr(aa[dt] + (j & 3) * 2048), lds_tr(aa[dt] + (j & 3) * 2048 + 1024)); };
  auto step = [&](int j, bool ahead) {
    SB();
    const bf16x8 b = __builtin_bit_cast(bf16x8, hr[j]);
    acc[0] = mfma32(fa[j & 1][0], b, acc[0]);
    SB();
    if (ahead) rd1(j + 1, 0);
    SB();
    acc[1] = mfma32(fa[j & 1][1], b, acc[1]);
    SB();
    if (ahead) rd1(j + 1, 1);
    SB();
  };
  u32x4 yv; int yok;
  rd1(0, 0); rd1(0, 1);
  int st = rot + 4; st -= st >= 28 ? 28 : 0;
#pragma unroll 1
  for (int g = 0; g < 6; ++g) {
#pragma unroll
    for (int j = 0; j < 4; ++j) {
      if (j == 3) {
        const unsigned d = (g & 3) == 3 ? (unsigned)-98304 : 32768u;
        aa[0] += d; aa[1] += d;
        bar();
        TS2(24 + g);
      }
      asm volatile("s_waitcnt vmcnt(3)" : "+v"(hr[j])::"memory");
      step(j, true);
      asm volatile("buffer_load_dwordx4 %0, %1, %2, %3 offen" HPOL : "=v"(hr[j]) : "v"(vh), "s"(rws), "s"(hb + st * 1024) : "memory");
      st = st == 27 ? 0 : st + 1;
      if (j == 3 && g == 2 && threadIdx.x == 0) post_flag(p.ws + WS_FY + (x * 28 + s) * 128, tag);
      SB();
    }
  }
  yok = __builtin_amdgcn_readfirstlane(lds_ld<unsigned>(lb + 131072));
  {
    const unsigned oy = yok ? WS_YT + (64 * db + (threadIdx.x >> 3)) * TROW + (256 * x + 64 * i2) * 2 + (threadIdx.x & 7) * 16 : 0x80000000u;
    asm volatile("buffer_load_dwordx4 %0, %1, %2, 0 offen sc1" : "=&v"(yv) : "v"(oy), "s"(rws) : "memory");
  }
  asm volatile("s_waitcnt vmcnt(4)" : "+v"(hr[0])::"memory"); step(0, true);
  asm volatile("s_waitcnt vmcnt(3)" : "+v"(hr[1])::"memory"); step(1, true);
  asm volatile("s_waitcnt vmcnt(2)" : "+v"(hr[2])::"memory"); step(2, true);
  asm volatile("s_waitcnt vmcnt(1)" : "+v"(hr[3])::"memory"); step(3, false);
  u32x4 xq = cx.xq, sg = cx.sg;
  TS(11);
  const unsigned pb = lb, yb = lb + 135168;
  {
    const int row = 32 * th + (lane & 31);
#pragma unroll
    for (int dt = 0; dt < 2; ++dt)
#pragma unroll
      for (int G = 0; G < 4; ++G) {
        const f32x4 t = {acc[dt][4 * G], acc[dt][4 * G + 1], acc[dt][4 * G + 2], acc[dt][4 * G + 3]};
        lds_st<f32x4>(pb + kq * 16384 + row * 256 + (((8 * dt + 2 * G + hh) ^ (row & 15)) * 16), t);
      }
  }
  wait_vm<0>();
  asm volatile("" : "+v"(xq), "+v"(sg), "+v"(yv));
  if (yok) lds_st<u32x4>(yb + (threadIdx.x >> 3) * 144 + (threadIdx.x & 7) * 16, yv);
  float mean = __uint_as_float(sg[1]), rstd = __uint_as_float(sg[3]);
  if (!__all(sg[0] == tag && sg[2] == tag)) stats_wait(rws, WS_STATS + tok * 16, tag, mean, rstd);
  bar();
  if (!yok) {
    yv = __builtin_amdgcn_raw_buffer_load_b128(rws, WS_YT + (64 * db + (threadIdx.x >> 3)) * TROW + (256 * x + 64 * i2) * 2 + (threadIdx.x & 7) * 16, 0, 16);
    lds_st<u32x4>(yb + (threadIdx.x >> 3) * 144 + (threadIdx.x & 7) * 16, yv);
    bar();
  }
  f32x4 o0 = {}, o1 = {};
#pragma unroll
  for (int k4 = 0; k4 < 4; ++k4) {
    o0 += lds_ld<f32x4>(pb + k4 * 16384 + tk * 256 + (((2 * ck) ^ (tk & 15)) * 16));
    o1 += lds_ld<f32x4>(pb + k4 * 16384 + tk * 256 + (((2 * ck + 1) ^ (tk & 15)) * 16));
  }
  const unsigned ya = yb + (8 * ck + q) * 144 + (16 * (w >> 1) + 4 * pp) * 2;
  const s16x4 y0 = lds_tr(ya), y1 = lds_tr(ya + 4 * 144);
  u32x4 ov;
#pragma unroll
  for (int e = 0; e < 8; e += 2) {
    const f32x2 o = e < 4 ? (f32x2){o0[e & 3], o0[(e & 3) + 1]} : (f32x2){o1[e & 3], o1[(e & 3) + 1]};
    const f32x2 xv = {bf_lo(xq[e >> 1]), bf_hi(xq[e >> 1])};
    const unsigned yp = e < 4 ? __builtin_bit_cast(u32x2, y0)[(e & 3) >> 1] : __builtin_bit_cast(u32x2, y1)[(e & 3) >> 1];
    const f32x2 yy = {bf_lo(yp), bf_hi(yp)};
    const f32x2 t = o * (f32x2){-1.4426950409f, -1.4426950409f};
    const f32x2 dn = (f32x2){__builtin_amdgcn_exp2f(t[0]), __builtin_amdgcn_exp2f(t[1])} + (f32x2){1.f, 1.f};
    const f32x2 g = o * (f32x2){__builtin_amdgcn_rcpf(dn[0]), __builtin_amdgcn_rcpf(dn[1])};
    const f32x2 xn = rbf2(rbf2(xv - (f32x2){mean, mean}) * (f32x2){rstd, rstd});
    const f32x2 r2 = __builtin_elementwise_fma(rbf2(xn * yy), g, xv);
    ov[e >> 1] = pk_bf16(r2[0], r2[1]);
  }
  __builtin_amdgcn_raw_buffer_store_b128(ov, ro, tok * 896 + db * 128 + ck * 16, 0, 0);
  TS(12);
}

__global__ __launch_bounds__(1024, 4) void h3_geglu_kernel(Params p) {
  __shared__ __attribute__((aligned(16))) char smem[LDS_BYTES];
  const unsigned lb = (unsigned)(size_t)(LAS char*)smem;
  const int tid = threadIdx.x, lane = tid & 63, w = __builtin_amdgcn_readfirstlane(tid >> 6);
  const int b = blockIdx.x, x = b & 7, s = b >> 3;
  TS(0);
  if (s < 28) {
    if (w >= 12) {
      loader_w(p, lb, x, s, w - 12, lane);
    } else if (w >= 8) {
      loader_x(p, lb, x, s, w - 8, lane);
    } else {
      Ctx cx;
      phase_gemm1(p, lb, x, s, w, lane, cx);
      phase_conv(p, lb, x, s, w, lane, cx);
      TS(9);
      phase_gemm2(p, lb, x, s, w, lane, cx);
    }
  } else {
    const int j = b - 224;
    if (WARM && w < 14) {
      const int q = j >> 3;
      touch(mk_rsrc((w & 1) ? p.v : p.k, S_ * D_ * 2), (unsigned)((256 * x + 64 * q) * 896 + ((w >> 1) * 64 + lane) * 128), lb + 65536);
    }
    if (j < 28) {
      const unsigned tag = __builtin_amdgcn_readfirstlane(*(const GAS unsigned*)(p.ws + WS_EPOCH) + 1u);
      const int m = w & 1, r0 = (w >> 1) * 8 + (lane >> 3);
      const rsrc_t rm = mk_rsrc(m ? p.wu : p.wg, D_ * F_ * 2);
      u32x4 t[7];
#pragma unroll
      for (int n = 0; n < 7; ++n) t[n] = __builtin_amdgcn_raw_buffer_load_b128(rm, (r0 + 64 * n) * 3584 + j * 128 + (lane & 7) * 16, 0, 0);
      float a[8] = {};
#pragma unroll
      for (int n = 0; n < 7; ++n)
#pragma unroll
        for (int i = 0; i < 4; ++i) { a[2 * i] += bf_lo(t[n][i]); a[2 * i + 1] += bf_hi(t[n][i]); }
      lds_st<f32x4>(lb + (w * 8 + (lane >> 3)) * 256 + (lane & 7) * 32, (f32x4){a[0], a[1], a[2], a[3]});
      lds_st<f32x4>(lb + (w * 8 + (lane >> 3)) * 256 + (lane & 7) * 32 + 16, (f32x4){a[4], a[5], a[6], a[7]});
      bar();
      if (w < 2) {
        float sum = 0.f;
#pragma unroll
        for (int q = 0; q < 64; ++q) sum += lds_ld<float>(lb + (((q >> 3) * 2 + w) * 8 + (q & 7)) * 256 + lane * 4);
        const u32x2 g = {tag, __float_as_uint(sum)};
        __builtin_amdgcn_raw_buffer_store_b64(g, mk_rsrc(p.ws, WS_END), WS_CS + (w * 1792 + j * 64 + lane) * 8, 0, 16);
      }
    }
    if (WARM && w < 14) {
      const int blk = w * 32 + j;
      if (blk < 196) touch(mk_rsrc(p.wd, F_ * D_ * 2), (unsigned)(blk * 64 + lane) * 128u, lb + 65536);
      else touch(mk_rsrc(p.h, D_ * S_ * 2), (unsigned)((blk - 196) * 64 + lane) * 128u, lb + 65536);
    }
    if (b == 224 && w == 15) {
      const unsigned tag = __builtin_amdgcn_readfirstlane(*(const GAS unsigned*)(p.ws + WS_EPOCH) + 1u);
#pragma unroll 1
      for (int n = 0; n < 4; ++n) poll_flags_slow(p.ws + WS_FH + n * 56 * 128, 56, tag, lane);
      if (lane == 0) post_flag(p.ws + WS_EPOCH, tag);
    }
    wait_vm<0>();
  }
  TS(15);
}

extern "C" void kernel_launch(void* const* d_in, const int* in_sizes, int n_in, void* d_out, int out_size, void* d_ws, size_t ws_size, hipStream_t stream) {
  Params p;
  p.q = (const u16*)d_in[0]; p.k = (const u16*)d_in[1]; p.v = (const u16*)d_in[2]; p.sk = (const u16*)d_in[3];
  p.h = (const u16*)d_in[4]; p.dv = (const u16*)d_in[5];
  p.wg = (const u16*)d_in[7]; p.wu = (const u16*)d_in[8]; p.wd = (const u16*)d_in[9];
  p.out = (u16*)d_out; p.ws = (char*)d_ws;
  h3_geglu_kernel<<<256, 1024, 0, stream>>>(p);
}
```

```cpp
#include <hip/hip_runtime.h>
#include <hip/hip_bf16.h>
#include <stdint.h>

typedef unsigned short u16;
typedef __attribute__((ext_vector_type(8))) short bf16x8;
typedef __attribute__((ext_vector_type(4))) short s16x4;
typedef __attribute__((ext_vector_type(4))) float f32x4;
typedef __attribute__((ext_vector_type(2))) float f32x2;
typedef __attribute__((ext_vector_type(16))) float f32x16;
typedef __attribute__((ext_vector_type(4))) unsigned u32x4;
typedef __attribute__((ext_vector_type(2))) unsigned u32x2;
typedef __attribute__((ext_vector_type(2))) __bf16 bf16x2;
typedef __amdgpu_buffer_rsrc_t rsrc_t;
#define HSPOL " sc1"
#define LAS __attribute__((address_space(3)))
#define GAS __attribute__((address_space(1)))
#define DI __device__ __forceinline__

#define OPT_OWN 0
#define KVT 1
#define KVFIRST 0
#define WDLATE 1
#define PAUX 2
#define USTAUX 16
#define HSLEEP 10
#define LPRIO 0
#define WARM 1
#define UPRIO 2
#define KVPOL " nt"
#define WPRIO 0
#define XPRIO 0
#define WDT 0
#define DRA 2
#define DRR 1
#define TLBW 0
#define OUTAUX 0
#define PREW 0
#define PFN 17

constexpr int S_ = 2048, D_ = 448, F_ = 1792;
constexpr unsigned WS_EPOCH = 0, WS_FH = 8192, WS_FY = 36864, WS_STATS = 65536, WS_FU = 98304, WS_CS = 2031616;
constexpr unsigned TROW = 4224;
constexpr unsigned WS_UT = 131072, WS_YT = 2097152, WS_H = 4194304, WS_END = WS_H + 7340032;
constexpr unsigned WS_DBG = 16u << 20;
constexpr int LDS_BYTES = 163840;
constexpr unsigned SPIN_CAP = 1u << 14;

struct Params {
  const u16 *q, *k, *v, *sk, *h, *dv, *wg, *wu, *wd;
  u16* out;
  char* ws;
};

DI rsrc_t mk_rsrc(const void* p, unsigned bytes) { return __builtin_amdgcn_make_buffer_rsrc((void*)p, (short)0, (int)bytes, 0x00020000); }
DI void dma16(rsrc_t r, unsigned voff, unsigned soff, unsigned lds) {
  asm volatile("s_mov_b32 m0, %0\n\ts_nop 0\n\tbuffer_load_dwordx4 %1, %2, %3 offen lds" ::"s"(lds), "v"(voff), "s"(r), "s"(soff) : "memory");
}
DI void touch(rsrc_t r, unsigned voff, unsigned lds) {
  asm volatile("s_mov_b32 m0, %0\n\ts_nop 0\n\tbuffer_load_dword %1, %2, 0 offen lds" ::"s"(lds), "v"(voff), "s"(r) : "memory");
}
template <int NT> DI void touch_p(rsrc_t r, unsigned voff, unsigned lds) {
  if (NT) asm volatile("s_mov_b32 m0, %0\n\ts_nop 0\n\tbuffer_load_dword %1, %2, 0 offen nt lds" ::"s"(lds), "v"(voff), "s"(r) : "memory");
  else touch(r, voff, lds);
}
DI void touch_g(const char* a, unsigned lds) {
  asm volatile("s_mov_b32 m0, %0\n\ts_nop 0\n\tglobal_load_lds_dword %1, off" ::"s"(lds), "v"(a) : "memory");
}
template <unsigned LA, unsigned SA>
DI void dma16c(rsrc_t r, unsigned voff, unsigned sbase, unsigned lbase) {
  unsigned t;
  asm volatile("s_add_u32 m0, %2, %5\n\ts_add_u32 %0, %3, %6\n\tbuffer_load_dwordx4 %1, %4, %0 offen lds"
               : "=&s"(t) : "v"(voff), "s"(lbase), "s"(sbase), "s"(r), "i"(LA), "i"(SA) : "memory", "scc");
}
template <int N> DI void wait_vm() { asm volatile("s_waitcnt vmcnt(%0)" ::"i"(N) : "memory"); }
DI void bar() { asm volatile("s_waitcnt lgkmcnt(0)\n\ts_barrier" ::: "memory"); }
DI float bf_lo(unsigned x) { return __uint_as_float(x << 16); }
DI float bf_hi(unsigned x) { return __uint_as_float(x & 0xffff0000u); }
DI unsigned pk_bf16(float lo, float hi) { f32x2 t = {lo, hi}; return __builtin_bit_cast(unsigned, __builtin_convertvector(t, bf16x2)); }
DI bf16x8 lds_b128(const char* p) { return *(const LAS bf16x8*)(p); }
DI s16x4 lds_tr(unsigned a) { return __builtin_amdgcn_ds_read_tr16_b64_v4i16((LAS s16x4*)(size_t)a); }
DI bf16x8 cat(s16x4 a, s16x4 b) { return __builtin_shufflevector(a, b, 0, 1, 2, 3, 4, 5, 6, 7); }
template <class T> DI T lds_ld(unsigned a) { return *(const LAS T*)(size_t)a; }
template <class T> DI void lds_st(unsigned a, T v) { *(LAS T*)(size_t)a = v; }
DI f32x16 mfma32(bf16x8 a, bf16x8 b, f32x16 c) { return __builtin_amdgcn_mfma_f32_32x32x16_bf16(a, b, c, 0, 0, 0); }
DI f32x4 mfma16(bf16x8 a, bf16x8 b, f32x4 c) { return __builtin_amdgcn_mfma_f32_16x16x32_bf16(a, b, c, 0, 0, 0); }
DI unsigned long long rtime() { return __builtin_amdgcn_s_memrealtime(); }
DI float dpp_add(float v, const int ctrl_row_shr) { return v; }
template <int CTRL, int RM> DI float dpp_mov(float v) { return __builtin_bit_cast(float, __builtin_amdgcn_update_dpp(0, __builtin_bit_cast(int, v), CTRL, RM, 0xf, true)); }
DI float wave_sum(float v) {
  v += dpp_mov<0x111, 0xf>(v); v += dpp_mov<0x112, 0xf>(v); v += dpp_mov<0x114, 0xf>(v); v += dpp_mov<0x118, 0xf>(v);
  v += dpp_mov<0x142, 0xa>(v); v += dpp_mov<0x143, 0xc>(v);
  return __builtin_bit_cast(float, __builtin_amdgcn_readlane(__builtin_bit_cast(int, v), 63));
}
#define TS(i)
#define TSL(i)
#define TSL_DECL
#define TSL_DUMP()
#define TS2(i)

DI void poll_flags(const char* base, int n, unsigned tag, int lane) {
  const GAS unsigned* f = (const GAS unsigned*)(base + (lane < n ? lane : 0) * 128);
  for (unsigned it = 0; it < SPIN_CAP; ++it) {
    unsigned x = __hip_atomic_load(f, __ATOMIC_RELAXED, __HIP_MEMORY_SCOPE_AGENT);
    if (__all(x == tag)) break;
    __builtin_amdgcn_s_sleep(2);
  }
}
DI void poll_flags_slow(const char* base, int n, unsigned tag, int lane) {
  const GAS unsigned* f = (const GAS unsigned*)(base + (lane < n ? lane : 0) * 128);
  for (unsigned it = 0; it < SPIN_CAP; ++it) {
    unsigned x = __hip_atomic_load(f, __ATOMIC_RELAXED, __HIP_MEMORY_SCOPE_AGENT);
    if (__all(x == tag)) break;
    __builtin_amdgcn_s_sleep(32);
  }
}
DI void post_flag(char* addr, unsigned tag) { __hip_atomic_store((GAS unsigned*)addr, tag, __ATOMIC_RELAXED, __HIP_MEMORY_SCOPE_AGENT); }

DI bool flags_ok(unsigned v, unsigned tag) { return __all(v == tag); }
DI unsigned flag_off(int n, int lane) { return (lane < n ? lane : 0) * 128; }
#define SB() __builtin_amdgcn_sched_barrier(0)
template <int V> struct IC { static constexpr int value = V; };
DI void asm_pin(unsigned& v) { asm volatile("" : "+v"(v)); }
DI float rbf(float x) { return __uint_as_float(pk_bf16(x, 0.f) << 16); }

DI f32x2 rbf2(f32x2 v) { const unsigned u = pk_bf16(v[0], v[1]); return (f32x2){bf_lo(u), bf_hi(u)}; }
DI u32x4 row_stats(u32x4 d, unsigned tag) {
  float e[8];
#pragma unroll
  for (int i = 0; i < 4; ++i) { e[2 * i] = bf_lo(d[i]); e[2 * i + 1] = bf_hi(d[i]); }
  float s = ((e[0] + e[1]) + (e[2] + e[3])) + ((e[4] + e[5]) + (e[6] + e[7]));
  const float mean = wave_sum(s) * (1.f / D_);
  const float on = (threadIdx.x & 63) < 56 ? 1.f : 0.f;
  float ss = 0.f;
#pragma unroll
  for (int i = 0; i < 8; ++i) { const float t = e[i] - mean; ss = fmaf(t, t, ss); }
  const float var = wave_sum(ss * on) * (1.f / D_);
  const float den = rbf(__builtin_amdgcn_sqrtf(rbf(rbf(var) + 1e-5f)));
  u32x4 g = {tag, __float_as_uint(rbf(mean)), tag, __float_as_uint(__builtin_amdgcn_rcpf(den))};
  return g;
}
DI void stats_wait(rsrc_t rs, unsigned off, unsigned tag, float& mean, float& rstd) {
  for (unsigned it = 0;; ++it) {
    const u32x4 g = __builtin_amdgcn_raw_buffer_load_b128(rs, off, 0, 16);
    mean = __uint_as_float(g[1]); rstd = __uint_as_float(g[3]);
    if (__all(g[0] == tag && g[2] == tag) || it >= SPIN_CAP) break;
    __builtin_amdgcn_s_sleep(1);
  }
}

constexpr unsigned CV_BASE = 98304;
constexpr unsigned CV_UP = CV_BASE;
constexpr unsigned CV_HS = CV_BASE + 9216;
constexpr unsigned CV_RED = CV_HS;
constexpr unsigned L_UT = 147456, L_SK = 155648, L_ST = 159744;
constexpr unsigned L_CS = 157696, L_OK = 158720, L_CNT = 158728, L_TOUCH = 158976;

#define HPOL " sc1"
#define HDEPTH 4
constexpr int HD = HDEPTH;
struct Ctx {
  unsigned tag; float dv; u32x4 hr[HD], xq, sg;
};
DI void g2_stage(rsrc_t rw, unsigned vw, int kq, int th, int grp, int rot, unsigned lb) {
#pragma unroll
  for (int n = 0; n < 4; ++n) {
    int st = 4 * grp + n + rot; st -= st >= 28 ? 28 : 0;
    dma16(rw, vw, (unsigned)((28 * kq + st) * 16 + 8 * th) * 896u, lb + (grp & 3) * 32768 + kq * 8192 + n * 2048 + th * 1024);
  }
}
DI void g2_piece(rsrc_t rw, unsigned vw, int kq, int th, int step, int rot, unsigned lb) {
  int st = step + rot; st -= st >= 28 ? 28 : 0;
  dma16(rw, vw, (unsigned)((28 * kq + st) * 16 + 8 * th) * 896u, lb + ((step >> 2) & 3) * 32768 + kq * 8192 + (step & 3) * 2048 + th * 1024);
}
DI void h_load(const Params& p, u32x4& dst, unsigned hb, int step, int rot, unsigned vh) {
  int st = step + rot; st -= st >= 28 ? 28 : 0;
  asm volatile("buffer_load_dwordx4 %0, %1, %2, %3 offen" HPOL : "=v"(dst) : "v"(vh), "s"(mk_rsrc(p.ws, WS_END)), "s"(hb + st * 1024) : "memory");
}
DI int g2_rot(int s);
DI void h_first(const Params& p, int x, int s, int w, int lane, Ctx& cx) {
  const unsigned hb = WS_H + ((8 * x + 2 * (s / 7) + (w >> 2)) * 112 + 28 * (w & 3)) * 1024;
#pragma unroll
  for (int n = 0; n < HD; ++n) h_load(p, cx.hr[n], hb, n, g2_rot(s), lane * 16);
}
DI unsigned g2_vw(int lane, int db) { const int L3 = lane >> 3, L7 = lane & 7; return L3 * 896 + db * 128 + 16 * (L7 ^ (((L3 >> 1) & 1) << 2)); }
DI int g2_rot(int s) { const int r = 4 * (s % 7) + 7 * (s / 7); return r >= 28 ? r - 28 : r; }

DI void phase_gemm1(const Params& p, unsigned lb, int x, int s, int w, int lane, Ctx& cx) {
  const int ib = 4 * (x >> 2) + s / 7, jb = 7 * (x & 3) + s % 7;
  const rsrc_t rq = mk_rsrc(p.q, S_ * D_ * 2), rws = mk_rsrc(p.ws, WS_END);
  const int gw = jb * 8 + w;
  unsigned tag;
  {
    u32x4 sr0, sr1; unsigned etag;
    const unsigned o0 = lane < 56 ? (256 * ib + gw) * 896 + 16 * lane : 0x80000000u;
    const unsigned o1 = (lane < 56 && gw < 32) ? (256 * ib + 224 + gw) * 896 + 16 * lane : 0x80000000u;
    const unsigned oz = WS_EPOCH;
    unsigned dvb; const unsigned od = (2 * (x * 28 + s) + (w >> 2)) * 2;
    asm volatile("buffer_load_dword %2, %6, %9, 0 offen\n\tbuffer_load_dwordx4 %0, %4, %8, 0 offen\n\tbuffer_load_dwordx4 %1, %5, %8, 0 offen\n\t"
                 "buffer_load_ushort %3, %7, %10, 0 offen\n\ts_waitcnt vmcnt(0)"
                 : "=&v"(sr0), "=&v"(sr1), "=&v"(etag), "=&v"(dvb) : "v"(o0), "v"(o1), "v"(oz), "v"(od), "s"(rq), "s"(rws), "s"(mk_rsrc(p.dv, D_ * 2)) : "memory");
    cx.dv = bf_lo(dvb);
    tag = __builtin_amdgcn_readfirstlane(etag) + 1u;
    cx.tag = tag;
    const u32x4 g0 = row_stats(sr0, tag), g1 = row_stats(sr1, tag);
    const unsigned p0 = lane == 0 ? WS_STATS + (256 * ib + gw) * 16 : 0x80000000u;
    const unsigned p1 = (lane == 0 && gw < 32) ? WS_STATS + (256 * ib + 224 + gw) * 16 : 0x80000000u;
    asm volatile("buffer_store_dwordx4 %0, %2, %4, 0 offen sc1\n\tbuffer_store_dwordx4 %1, %3, %4, 0 offen sc1\n\ts_nop 1" ::"v"(g0), "v"(g1), "v"(p0), "v"(p1), "s"(rws) : "memory");
  }
  const int tw = w & 3, fh = w >> 2, r = lane & 31, hh = lane >> 5;
  unsigned xa[4], wa[2];
#pragma unroll
  for (int i = 0; i < 4; ++i) xa[i] = lb + (64 * tw + r) * 128 + 16 * ((2 * i + hh) ^ ((r >> 1) & 7));
  {
    const int blk = (lane >> 4) & 1, q = (lane & 15) >> 2, pp = lane & 3, m = pp >> 1, f = (q >> 1) | ((m ^ hh) << 1);
#pragma unroll
    for (int u = 0; u < 2; ++u) wa[u] = lb + 32768 + m * 8192 + (8 * hh + q) * 128 + 32 * ((2 * fh + u) ^ f) + 16 * blk + 8 * (pp & 1);
  }
  f32x16 acc[2][2] = {};
  if (PREW) {
    f32x16 z = {};
    const bf16x8 o = {0x3c00, 0x3c10, 0x3c20, 0x3c30, 0x3c40, 0x3c50, 0x3c60, 0x3c70};
#pragma unroll 1
    for (int i = 0; i < PREW; ++i) { z = mfma32(o, o, z); z = mfma32(o, o, z); z = mfma32(o, o, z); z = mfma32(o, o, z); }
    asm volatile("" ::"v"(z));
  }
  TS(1);
  bar();
  TS(2);
  bf16x8 fb[2][2] = {}, fa[2][2] = {};
  auto rdA = [&](int ks, int u) { fa[ks & 1][u] = cat(lds_tr(wa[u] + (ks & 3) * 2048), lds_tr(wa[u] + (ks & 3) * 2048 + 512)); };
  auto rdB = [&](int ks) { fb[ks & 1][0] = lds_ld<bf16x8>(xa[ks & 3]); fb[ks & 1][1] = lds_ld<bf16x8>(xa[ks & 3] + 4096); };
  auto step = [&](int ks, bool ahead) {
    const int pb = ks & 1;
    SB();
    acc[0][0] = mfma32(fa[pb][0], fb[pb][0], acc[0][0]);
    SB();
    if (ahead) rdA(ks + 1, 0);
    SB();
    acc[1][0] = mfma32(fa[pb][0], fb[pb][1], acc[1][0]);
    SB();
    if (ahead) rdB(ks + 1);
    SB();
    acc[0][1] = mfma32(fa[pb][1], fb[pb][0], acc[0][1]);
    SB();
    if (ahead) rdA(ks + 1, 1);
    SB();
    acc[1][1] = mfma32(fa[pb][1], fb[pb][1], acc[1][1]);
    SB();
  };
  rdA(0, 0); rdB(0); rdA(0, 1);
  int sl = 0;
#pragma unroll 1
  for (int c = 0; c < 6; ++c) {
    step(0, true);
    step(1, true);
    step(2, true);
    {
      const unsigned d = __builtin_amdgcn_readfirstlane(sl == 2 ? -98304 : 49152);
      sl = sl == 2 ? 0 : sl + 1;
#pragma unroll
      for (int i = 0; i < 4; ++i) xa[i] += d;
      wa[0] += d; wa[1] += d;
    }
    bar();
    TS2(16 + c);
    step(3, true);
    if (c == 5 && threadIdx.x == 0) post_flag(p.ws + WS_FU + (((x * 28 + s) % 7) * 32 + (x * 28 + s) / 7) * 128, tag);
  }
  step(0, true); step(1, true); step(2, true); step(3, false);
  TS(3);
  const unsigned csb = lb + L_CS;
  bar();
  TS(4);
  float mean[2], rstd[2];
#pragma unroll
  for (int tt = 0; tt < 2; ++tt) { const f32x2 t = lds_ld<f32x2>(lb + L_ST + (64 * tw + 32 * tt + r) * 8); mean[tt] = t[0]; rstd[tt] = t[1]; }
  constexpr float C0 = -2.3022081985f, C1 = -0.1029432397f;
#pragma unroll
  for (int u = 0; u < 2; ++u) {
    f32x4 cg[2], cu[2];
#pragma unroll
    for (int b = 0; b < 2; ++b) {
      cg[b] = lds_ld<f32x4>(csb + (16 * (2 * fh + u) + 8 * b + 4 * hh) * 4);
      cu[b] = lds_ld<f32x4>(csb + 256 + (16 * (2 * fh + u) + 8 * b + 4 * hh) * 4);
    }
#pragma unroll
    for (int tt = 0; tt < 2; ++tt) {
      const float rr = rstd[tt], k0s = rr * C0, k1s = C1 * rr * rr * rr, ir2s = __builtin_amdgcn_rcpf(rr * rr);
      const f32x2 nm = {-mean[tt], -mean[tt]}, k0 = {k0s, k0s}, k1 = {k1s, k1s}, ir2 = {ir2s, ir2s};
      u32x4 o;
#pragma unroll
      for (int b = 0; b < 2; ++b)
#pragma unroll
        for (int e = 0; e < 4; e += 2) {
          const f32x2 ag = {acc[tt][u][8 * b + e], acc[tt][u][8 * b + e + 1]}, au = {acc[tt][u][8 * b + 4 + e], acc[tt][u][8 * b + 5 + e]};
          const f32x2 c2g = {cg[b][e], cg[b][e + 1]}, c2u = {cu[b][e], cu[b][e + 1]};
          const f32x2 a = __builtin_elementwise_fma(nm, c2g, ag), bb = __builtin_elementwise_fma(nm, c2u, au);
          const f32x2 z = a * __builtin_elementwise_fma(a * a, k1, k0);
          const f32x2 ex = {__builtin_amdgcn_exp2f(z[0]), __builtin_amdgcn_exp2f(z[1])};
          const f32x2 dn = __builtin_elementwise_fma(ex, ir2, ir2);
          const f32x2 rc = {__builtin_amdgcn_rcpf(dn[0]), __builtin_amdgcn_rcpf(dn[1])};
          const f32x2 hv = (a * bb) * rc;
          o[2 * b + (e >> 1)] = pk_bf16(hv[0], hv[1]);
        }
      const unsigned oh = WS_H + ((8 * ib + 2 * tw + tt) * 112 + 4 * jb + 2 * fh + u) * 1024 + lane * 16;
      asm volatile("buffer_store_dwordx4 %0, %1, %2, 0 offen" HSPOL "\n\ts_nop 1" ::"v"(o), "v"(oh), "s"(rws) : "memory");
    }
  }
  TS(5);
}

constexpr int CODE_LINES = 208;
struct WdStage {
  rsrc_t rw; unsigned vw, lb; int kq, th, rot;
  DI void group(int g) const {
#pragma unroll
    for (int n = 0; n < 4; ++n) g2_piece(rw, vw, kq, th, 4 * g + n, rot, lb);
  }
};
#define TAILARG
#define TAILPASS
DI void loader_tail(const Params& p, unsigned lb, int s, int lane, const WdStage& wd, bool poller, unsigned tag TAILARG) {
  TSL(12);
  bar();
  TSL(13);
  wd.group(3);
  TSL(14);
  bar();
  wd.group(4);
  bar();
  wd.group(5);
  TSL(22);
  wait_vm<8>();
  TSL(23);
  bar();
  wd.group(6);
  wait_vm<8>();
  TSL(30);
  bar();
  wait_vm<4>();
  bar();
  if (poller) {
    const unsigned yf = __hip_atomic_load((const GAS unsigned*)(p.ws + WS_FY + (s % 7) * 32 * 128 + flag_off(32, lane)), __ATOMIC_RELAXED, __HIP_MEMORY_SCOPE_AGENT);
    const unsigned ok = flags_ok(yf, tag) ? 1u : 0u;
    if (lane == 0) lds_st<unsigned>(lb + 131072, ok);
  }
  wait_vm<0>();
  bar();
  const int yok = __builtin_amdgcn_readfirstlane(lds_ld<unsigned>(lb + 131072));
  if (poller && !yok) poll_flags(p.ws + WS_FY + (s % 7) * 32 * 128, 32, tag, lane);
  bar();
  if (!yok) bar();
}

DI void loader_x(const Params& p, unsigned lb, int x, int s, int lw, int lane) {
  const int ib = 4 * (x >> 2) + s / 7;
  const rsrc_t rq = mk_rsrc(p.q, S_ * D_ * 2);
  const int L3 = lane >> 3, L7 = lane & 7;
  const unsigned vx0 = (64 * lw + L3) * 896 + 16 * (L7 ^ ((lane >> 4) & 3));
  const unsigned vx1 = (64 * lw + 8 + L3) * 896 + 16 * (L7 ^ (((lane >> 4) & 3) | 4));
  const unsigned sx = ib * 256 * 896, lx = lb + lw * 8192;
  auto piece = [&](auto C, auto I) {
    constexpr int c = decltype(C)::value, i = decltype(I)::value;
    dma16c<(c % 3) * 49152 + i * 1024, c * 128 + (i >> 1) * 16 * 896>(rq, (i & 1) ? vx1 : vx0, sx, lx);
  };
  auto stage = [&](auto C) {
    piece(C, IC<0>{}); piece(C, IC<1>{}); piece(C, IC<2>{}); piece(C, IC<3>{}); piece(C, IC<4>{}); piece(C, IC<5>{}); piece(C, IC<6>{}); piece(C, IC<7>{});
  };
  if (XPRIO) __builtin_amdgcn_s_setprio(XPRIO);
  const rsrc_t rws = mk_rsrc(p.ws, WS_END);
  unsigned etag;
  TSL_DECL
  TS(8);
  { const unsigned oz = WS_EPOCH; asm volatile("buffer_load_dword %0, %1, %2, 0 offen" : "=&v"(etag) : "v"(oz), "s"(rws) : "memory"); }
  stage(IC<0>{});
  stage(IC<1>{});
  wait_vm<8>();
  asm volatile("" : "+v"(etag));
  const unsigned tag = __builtin_amdgcn_readfirstlane(etag) + 1u;
  TS(1);
  bar();
  stage(IC<2>{});
  auto lstage = [&](auto C) {
    constexpr int c = decltype(C)::value;
    TSL(16 + c);
    if (c < 5) wait_vm<8>(); else wait_vm<0>();
    TSL(24 + c);
    bar();
    TSL(2 + c);
    if constexpr (c + 3 <= 6) stage(IC<c + 3>{});
    if (c == 4) {
      const unsigned os = WS_STATS + (256 * ib + 64 * lw + lane) * 16;
      float mean, rstd;
      stats_wait(rws, os, tag, mean, rstd);
      lds_st<f32x2>(lb + L_ST + (64 * lw + lane) * 8, (f32x2){mean, rstd});
    }
  };
  lstage(IC<0>{}); lstage(IC<1>{}); lstage(IC<2>{}); lstage(IC<3>{}); lstage(IC<4>{}); lstage(IC<5>{});
  bar();
  bar();
  const WdStage wd = {mk_rsrc(p.wd, F_ * D_ * 2), g2_vw(lane, s % 7), lb, lw, 0, g2_rot(s)};
  TSL(8);
  wd.group(0); wd.group(1); wd.group(2);
  TSL(9);
  bar();
  bar();
  wait_vm<0>();
  loader_tail(p, lb, s, lane, wd, false, 0u TAILPASS);
  TSL_DUMP();
}

DI void loader_w(const Params& p, unsigned lb, int x, int s, int lw, int lane) {
  const int jb = 7 * (x & 3) + s % 7;
  const rsrc_t rg = mk_rsrc(p.wg, D_ * F_ * 2), ru = mk_rsrc(p.wu, D_ * F_ * 2);
  const rsrc_t rw = mk_rsrc(p.wd, F_ * D_ * 2), rws = mk_rsrc(p.ws, WS_END);
  const int L3 = lane >> 3, L7 = lane & 7;
  const unsigned vwA = (16 * lw + L3) * 3584 + jb * 128 + 32 * ((L7 >> 1) ^ ((lane >> 4) & 1)) + 16 * (L7 & 1);
  const unsigned vwB = (16 * lw + L3) * 3584 + jb * 128 + 32 * ((L7 >> 1) ^ (((lane >> 4) & 1) | 2)) + 16 * (L7 & 1);
  const unsigned lx = lb + 32768 + lw * 2048;
  auto stage = [&](auto C) {
    constexpr int c = decltype(C)::value;
    constexpr int cm = c;
    dma16c<(c % 3) * 49152, cm * 64 * 3584>(rg, vwA, 0, lx);
    dma16c<(c % 3) * 49152 + 1024, cm * 64 * 3584 + 8 * 3584>(rg, vwB, 0, lx);
    dma16c<(c % 3) * 49152 + 8192, cm * 64 * 3584>(ru, vwB, 0, lx);
    dma16c<(c % 3) * 49152 + 8192 + 1024, cm * 64 * 3584 + 8 * 3584>(ru, vwA, 0, lx);
  };
  if (WPRIO) __builtin_amdgcn_s_setprio(WPRIO);
  unsigned etag;
  { const unsigned oz = WS_EPOCH; asm volatile("buffer_load_dword %0, %1, %2, 0 offen" : "=&v"(etag) : "v"(oz), "s"(rws) : "memory"); }
  stage(IC<0>{});
  stage(IC<1>{});
  wait_vm<4>();
  asm volatile("" : "+v"(etag));
  const unsigned tag = __builtin_amdgcn_readfirstlane(etag) + 1u;
  bar();
  const int mi = x * 28 + s, cb = mi % 7, tb = mi / 7, ucg = lane >> 3, urp = lane & 7, ut0 = 64 * tb + 16 * lw + 2 * urp;
  u32x4 kk[5], vv[2];
  stage(IC<2>{});
  {
    const rsrc_t rk = mk_rsrc(p.k, S_ * D_ * 2), rv = mk_rsrc(p.v, S_ * D_ * 2);
    unsigned ok[5];
#pragma unroll
    for (int n = 0; n < 5; ++n) ok[n] = ut0 - 3 + n >= 0 ? (unsigned)((ut0 - 3 + n) * 896 + cb * 128 + ucg * 16) : 0x80000000u;
    const unsigned ov = ut0 * 896 + cb * 128 + ucg * 16;
    asm volatile("buffer_load_dwordx4 %0, %7, %13, 0 offen" KVPOL "\n\tbuffer_load_dwordx4 %1, %8, %13, 0 offen" KVPOL "\n\tbuffer_load_dwordx4 %2, %9, %13, 0 offen" KVPOL "\n\t"
                 "buffer_load_dwordx4 %3, %10, %13, 0 offen" KVPOL "\n\tbuffer_load_dwordx4 %4, %11, %13, 0 offen" KVPOL "\n\t"
                 "buffer_load_dwordx4 %5, %12, %14, 0 offen" KVPOL "\n\tbuffer_load_dwordx4 %6, %12, %14, 0 offen offset:896" KVPOL
                 : "=&v"(kk[0]), "=&v"(kk[1]), "=&v"(kk[2]), "=&v"(kk[3]), "=&v"(kk[4]), "=&v"(vv[0]), "=&v"(vv[1])
                 : "v"(ok[0]), "v"(ok[1]), "v"(ok[2]), "v"(ok[3]), "v"(ok[4]), "v"(ov), "s"(rk), "s"(rv) : "memory");
    dma16(mk_rsrc(p.sk, D_ * 8), lane < 32 ? cb * 512 + lane * 16 : 0x80000000u, 0, lb + L_SK);
  }
  auto wd_touch = [&](int i) {
    const int sh = 2 * i + (lw & 1);
    if ((lw >> 1) == (i & 1) && sh < 7) touch(rw, (unsigned)((s * 7 + sh) * 64 + lane) * 128u, lb + L_TOUCH);
  };
  const int chl = lw >> 1, ch = 2 * mi + chl, tA = (lw & 1) * 128 + lane;
  u32x4 hA, hB; u32x2 gA, gB, cg2; unsigned uf = 0;
  auto upairs = [&](const int j0) {
    const int tl = 16 * lw + 2 * urp, e = (tl & ~7) + 6 - (tl & 7);
    const unsigned ta = lb + L_UT + 8 * ucg * 128 + (((e >> 3) ^ ucg) * 16) + 2 * (e & 7);
    u32x4 wq[2];
#pragma unroll
    for (int j = 0; j < 2; ++j) wq[j] = lds_ld<u32x4>(lb + L_SK + ucg * 64 + (j0 + j) * 16);
    f32x2 kf[2][5], vf[2][2], wt[2][4], s0[2], s1[2];
#pragma unroll
    for (int j = 0; j < 2; ++j) {
#pragma unroll
      for (int n = 0; n < 5; ++n) kf[j][n] = (f32x2){bf_lo(kk[n][j0 + j]), bf_hi(kk[n][j0 + j])};
#pragma unroll
      for (int n = 0; n < 2; ++n) vf[j][n] = (f32x2){bf_lo(vv[n][j0 + j]), bf_hi(vv[n][j0 + j])};
    }
#pragma unroll
    for (int j = 0; j < 2; ++j) {
      wt[j][0] = (f32x2){bf_lo(wq[j][0]), bf_lo(wq[j][2])}; wt[j][1] = (f32x2){bf_hi(wq[j][0]), bf_hi(wq[j][2])};
      wt[j][2] = (f32x2){bf_lo(wq[j][1]), bf_lo(wq[j][3])}; wt[j][3] = (f32x2){bf_hi(wq[j][1]), bf_hi(wq[j][3])};
    }
    SB();
#pragma unroll
    for (int j = 0; j < 2; ++j) { s0[j] = kf[j][0] * wt[j][3]; s1[j] = kf[j][1] * wt[j][3]; }
    SB();
#pragma unroll
    for (int t = 2; t >= 0; --t) {
#pragma unroll
      for (int j = 0; j < 2; ++j) { s0[j] = __builtin_elementwise_fma(kf[j][3 - t], wt[j][t], s0[j]); s1[j] = __builtin_elementwise_fma(kf[j][4 - t], wt[j][t], s1[j]); }
      SB();
    }
#pragma unroll
    for (int j = 0; j < 2; ++j) { s0[j] *= vf[j][0]; s1[j] *= vf[j][1]; }
    SB();
#pragma unroll
    for (int j = 0; j < 2; ++j) {
      lds_st<unsigned>(ta + (2 * (j0 + j)) * 128, pk_bf16(s1[j][0], s0[j][0]));
      lds_st<unsigned>(ta + (2 * (j0 + j) + 1) * 128, pk_bf16(s1[j][1], s0[j][1]));
    }
  };
  TSL_DECL
  auto lstage = [&](auto C) {
    constexpr int c = decltype(C)::value;
    TSL(16 + c);
    if (c == 0) wait_vm<12>(); else if (c < 4) wait_vm<4>(); else if (c == 4) wait_vm<6>(); else wait_vm<5>();
    TSL(24 + c);
    bar();
    TSL(2 + c);
    if (c == 3) {
#pragma unroll
      for (int n = 0; n < 2; ++n) {
        const int cl = 16 * lw + 8 * n + (lane >> 3);
        const u32x4 t = lds_ld<u32x4>(lb + L_UT + cl * 128 + (((lane & 7) ^ (cl >> 3)) * 16));
        const unsigned o = WS_UT + (64 * cb + cl) * TROW + 128 * tb + (lane & 7) * 16;
        asm volatile("buffer_store_dwordx4 %0, %1, %2, 0 offen sc1\n\ts_nop 1" ::"v"(t), "v"(o), "s"(rws) : "memory");
      }
    }
    if (c == 4) {
      const rsrc_t rh = mk_rsrc(p.h, D_ * S_ * 2);
      const unsigned oh = ch * 4096 + tA * 16;
      asm volatile("buffer_load_dwordx4 %0, %4, %5, 0 offen nt\n\tbuffer_load_dwordx2 %1, %4, %5, 0 offen offset:16 nt\n\t"
                   "buffer_load_dwordx4 %2, %4, %5, 0 offen offset:1024 nt\n\tbuffer_load_dwordx2 %3, %4, %5, 0 offen offset:1040 nt"
                   : "=&v"(hA), "=&v"(gA), "=&v"(hB), "=&v"(gB) : "v"(oh), "s"(rh) : "memory");
      const unsigned oc = lw < 2 ? WS_CS + (lw * 1792 + jb * 64 + lane) * 8 : 0x80000000u;
      asm volatile("buffer_load_dwordx2 %0, %1, %2, 0 offen sc1" : "=&v"(cg2) : "v"(oc), "s"(rws) : "memory");
    }
    if (c == 5) {
      const unsigned of = WS_FU + (mi >> 5) * 32 * 128 + flag_off(32, lane);
      asm volatile("buffer_load_dword %0, %1, %2, 0 offen sc1" : "=&v"(uf) : "v"(of), "s"(rws) : "memory");
    }
    if (c == 1) {
      asm volatile("" : "+v"(kk[0]), "+v"(kk[1]), "+v"(kk[2]), "+v"(kk[3]), "+v"(kk[4]), "+v"(vv[0]), "+v"(vv[1]));
      upairs(0);
    }
    if (c == 2) upairs(2);
    if constexpr (c + 3 <= 6) stage(IC<c + 3>{});
    if (WDT && c >= 2) wd_touch(c - 2);
  };
  lstage(IC<0>{}); lstage(IC<1>{}); lstage(IC<2>{}); lstage(IC<3>{}); lstage(IC<4>{}); lstage(IC<5>{});
  asm volatile("s_waitcnt vmcnt(1)" : "+v"(cg2)::"memory");
  if (lw < 2) {
    if (!__all(cg2[0] == tag)) {
      for (unsigned it = 0; it < SPIN_CAP; ++it) {
        cg2 = __builtin_amdgcn_raw_buffer_load_b64(rws, WS_CS + (lw * 1792 + jb * 64 + lane) * 8, 0, 16);
        if (__all(cg2[0] == tag)) break;
        __builtin_amdgcn_s_sleep(1);
      }
    }
    lds_st<unsigned>(lb + L_CS + lw * 256 + lane * 4, cg2[1]);
  }
  if (lw == 0 && lane == 0) { lds_st<unsigned>(lb + L_CNT, 0u); }
  bar();
  wait_vm<0>();
  asm volatile("" : "+v"(hA), "+v"(gA), "+v"(hB), "+v"(gB), "+v"(uf));
  if (!flags_ok(uf, tag)) poll_flags(p.ws + WS_FU + (mi >> 5) * 32 * 128, 32, tag, lane);
  u32x4 uA, uB;
  {
    const unsigned ou = WS_UT + ch * TROW + tA * 16;
    asm volatile("buffer_load_dwordx4 %0, %2, %3, 0 offen sc1\n\tbuffer_load_dwordx4 %1, %2, %3, 0 offen offset:1024 sc1" : "=&v"(uA), "=&v"(uB) : "v"(ou), "s"(rws) : "memory");
  }
  const unsigned up = lb + CV_UP + chl * 4608, hs = lb + CV_HS + chl * 16640;
  {
    auto copies = [&](const u32x4& h4, const u32x2& h2, int t4) {
      const unsigned w0 = h4[0], w1 = h4[1], w2 = h4[2], w3 = h4[3], w4 = h2[0], w5 = h2[1];
      const unsigned a0 = __builtin_amdgcn_alignbit(w1, w0, 16), a1 = __builtin_amdgcn_alignbit(w2, w1, 16), a2 = __builtin_amdgcn_alignbit(w3, w2, 16),
                     a3 = __builtin_amdgcn_alignbit(w4, w3, 16), a4 = __builtin_amdgcn_alignbit(w5, w4, 16);
      const unsigned o = hs + 32 + 16 * t4;
      lds_st<u32x4>(o, (u32x4){w0, w1, w2, w3});
      lds_st<u32x4>(o + 4160, (u32x4){a0, a1, a2, a3});
      lds_st<u32x4>(o + 8320, (u32x4){w1, w2, w3, w4});
      lds_st<u32x4>(o + 12480, (u32x4){a1, a2, a3, a4});
    };
    copies(hA, gA, tA); copies(hB, gB, tA + 64);
    if (tA == 0) {
      const unsigned w0 = hA[0], a0 = __builtin_amdgcn_alignbit(hA[1], hA[0], 16);
      const u32x4 z = {0, 0, 0, 0};
#pragma unroll
      for (int sg = 0; sg < 4; ++sg) lds_st<u32x4>(hs + sg * 4160, z);
      lds_st<u32x4>(hs + 16, z);
      lds_st<u32x4>(hs + 4160 + 16, (u32x4){0, 0, 0, w0 << 16});
      lds_st<u32x4>(hs + 8320 + 16, (u32x4){0, 0, 0, w0});
      lds_st<u32x4>(hs + 12480 + 16, (u32x4){0, 0, w0 << 16, a0});
    }
    if (tA < 30) lds_st<u32x4>(up + 16 * tA, (u32x4){0, 0, 0, 0});
  }
  wait_vm<0>();
  asm volatile("" : "+v"(uA), "+v"(uB));
  lds_st<u32x4>(up + 480 + 16 * tA, uA);
  lds_st<u32x4>(up + 480 + 16 * (tA + 64), uB);
  bar();
  const WdStage wd = {rw, g2_vw(lane, s % 7), lb, lw, 1, g2_rot(s)};
  TSL(8);
  wd.group(0); wd.group(1); wd.group(2);
  TSL(9);
  bar();
  TSL(10);
  if (lw == 0) poll_flags(p.ws + WS_FH + x * 28 * 128, 28, tag, lane);
  TSL(11);
  bar();
  wait_vm<0>();
  loader_tail(p, lb, s, lane, wd, lw == 0, tag TAILPASS);
  TSL_DUMP();
}

DI void phase_conv(const Params& p, unsigned lb, int x, int s, int w, int lane, Ctx& cx) {
  const int mi = x * 28 + s, chl = w >> 2, wq = w & 3, ch = 2 * mi + chl, t4 = wq * 64 + lane;
  const rsrc_t rws = mk_rsrc(p.ws, WS_END);
  const unsigned tag = cx.tag;
  const float dv = cx.dv;
  const unsigned up = lb + CV_UP + chl * 4608, hs = lb + CV_HS + chl * 16640;
  bar();
  TS(6);
  const int i = lane & 15, kg = lane >> 4, sg = (i + 1) & 3;
  f32x4 acc[8] = {};
  {
    const unsigned ga = up + 448 - 128 * wq + 32 * i + 16 * kg;
    unsigned f0 = hs + sg * 4160 + 128 * wq + 2 * (25 + i - 8 * kg - sg), f1 = f0 + 8;
    asm volatile("" : "+v"(f1));
    auto publish_h = [&]() {
      wait_vm<0>();
      if (lane == 0) {
        const unsigned old = __hip_atomic_fetch_add((LAS unsigned*)(size_t)(lb + L_CNT), 1u, __ATOMIC_RELAXED, __HIP_MEMORY_SCOPE_WORKGROUP);
        if (old == 7u) post_flag(p.ws + WS_FH + ((4 * (x >> 2) + s / 7) * 28 + 7 * (x & 3) + s % 7) * 128, tag);
      }
    };
    bf16x8 G[8], Fq[4];
    unsigned gb = ga;
    auto rdF = [&](int a, int dres) { Fq[a & 3] = cat(lds_ld<s16x4>(f0 + 64 * dres + 512 * (a & 7)), lds_ld<s16x4>(f1 + 64 * dres + 512 * (a & 7))); };
    rdF(0, 0); rdF(1, 0);
#pragma unroll
    for (int b = 7; b >= 0; --b) G[b] = lds_ld<bf16x8>(gb + 512 * b);
#pragma unroll 1
    for (int rr = 0; rr < 2; ++rr) {
      gb -= 64;
#pragma unroll
      for (int a = 0; a < 8; ++a) {
        if (a == DRA && rr == DRR) publish_h();
#pragma unroll
        for (int b = 7 - a; b >= 0; --b) {
          acc[a + b] = mfma16(Fq[a & 3], G[b], acc[a + b]);
          SB();
          if (b == 7 - a) {
            rdF(a + 2, a + 2 >= 8 ? 1 : 0);
            G[7 - a] = lds_ld<bf16x8>(gb + 512 * (7 - a));
          }
          SB();
        }
      }
      f0 += 64; f1 += 64;
    }
    if (DRR == 2) publish_h();
  }
  TS2(13);
  bar();
  TS(7);
  const unsigned red = lb + CV_RED + chl * 24576;
  f32x4 y0, y1;
  {
    auto scatter = [&](auto WQ) {
      constexpr int q0 = decltype(WQ)::value;
#pragma unroll
      for (int o = 1; o < 4; ++o) {
        constexpr int dummy = 0; (void)dummy;
        const int dst = (q0 + o) & 3;
        lds_st<f32x4>(red + (dst * 3 + (o - 1)) * 2048 + lane * 16, acc[2 * dst]);
        lds_st<f32x4>(red + (dst * 3 + (o - 1)) * 2048 + 1024 + lane * 16, acc[2 * dst + 1]);
      }
      y0 = acc[2 * q0]; y1 = acc[2 * q0 + 1];
    };
    if (wq == 0) scatter(IC<0>{}); else if (wq == 1) scatter(IC<1>{}); else if (wq == 2) scatter(IC<2>{}); else scatter(IC<3>{});
  }
  const unsigned ua = up + 480 + 1024 * wq + 32 * i + 16 * (kg >> 1) + 8 * (1 - (kg & 1));
  const u32x2 ua0 = lds_ld<u32x2>(ua), ua1 = lds_ld<u32x2>(ua + 512);
  bar();
  {
    const int tok = 256 * x + 64 * (s / 7) + 16 * (w >> 1) + (lane & 15);
    const unsigned ox = tok * 896 + (s % 7) * 128 + (4 * (w & 1) + (lane >> 4)) * 16, os = WS_STATS + tok * 16;
    asm volatile("buffer_load_dwordx4 %0, %2, %4, 0 offen\n\tbuffer_load_dwordx4 %1, %3, %5, 0 offen sc1"
                 : "=&v"(cx.xq), "=&v"(cx.sg) : "v"(ox), "v"(os), "s"(mk_rsrc(p.q, S_ * D_ * 2)), "s"(rws) : "memory");
  }
  h_first(p, x, s, w, lane, cx);
#pragma unroll
  for (int o = 0; o < 3; ++o) {
    y0 += lds_ld<f32x4>(red + (wq * 3 + o) * 2048 + lane * 16);
    y1 += lds_ld<f32x4>(red + (wq * 3 + o) * 2048 + 1024 + lane * 16);
  }
  y0[0] = rbf(y0[0]) + rbf(dv * bf_hi(ua0[1])); y0[1] = rbf(y0[1]) + rbf(dv * bf_lo(ua0[1])); y0[2] = rbf(y0[2]) + rbf(dv * bf_hi(ua0[0])); y0[3] = rbf(y0[3]) + rbf(dv * bf_lo(ua0[0]));
  y1[0] = rbf(y1[0]) + rbf(dv * bf_hi(ua1[1])); y1[1] = rbf(y1[1]) + rbf(dv * bf_lo(ua1[1])); y1[2] = rbf(y1[2]) + rbf(dv * bf_hi(ua1[0])); y1[3] = rbf(y1[3]) + rbf(dv * bf_lo(ua1[0]));
  const unsigned d0 = pk_bf16(y0[0], y0[1]), d1 = pk_bf16(y0[2], y0[3]), e0 = pk_bf16(y1[0], y1[1]), e1 = pk_bf16(y1[2], y1[3]);
  const auto s0 = __builtin_amdgcn_permlane16_swap(d0, e0, false, false);
  const auto s1 = __builtin_amdgcn_permlane16_swap(d1, e1, false, false);
  const u32x4 o = {s0[0], s1[0], s0[1], s1[1]};
  const unsigned oy = WS_YT + ch * TROW + 512 * (2 * wq + (kg & 1)) + 32 * i + 8 * (kg & 2);
  asm volatile("buffer_store_dwordx4 %0, %1, %2, 0 offen sc1\n\ts_nop 1" ::"v"(o), "v"(oy), "s"(rws) : "memory");
}

DI void phase_gemm2(const Params& p, unsigned lb, int x, int s, int w, int lane, Ctx& cx) {
  const int i2 = s / 7, db = s % 7, th = w >> 2, kq = w & 3, rot = g2_rot(s);
  const unsigned tag = cx.tag;
  const rsrc_t rws = mk_rsrc(p.ws, WS_END);
  const rsrc_t rq = mk_rsrc(p.q, S_ * D_ * 2), ro = mk_rsrc(p.out, S_ * D_ * 2);
  const int tk = 16 * (w >> 1) + (lane & 15), ck = 4 * (w & 1) + (lane >> 4), tok = 256 * x + 64 * i2 + tk;
  bar();
  TS(10);
  const unsigned hb = WS_H + ((8 * x + 2 * i2 + th) * 112 + 28 * kq) * 1024;
  const unsigned vh = lane * 16;
  u32x4 (&hr)[HD] = cx.hr;
  const int hh = lane >> 5, blk = (lane >> 4) & 1, q = (lane & 15) >> 2, pp = lane & 3;
  unsigned aa[2];
#pragma unroll
  for (int dt = 0; dt < 2; ++dt) aa[dt] = lb + kq * 8192 + (4 * hh + q) * 128 + 64 * (dt ^ ((q >> 1) & 1)) + 32 * blk + 8 * pp;
  f32x16 acc[2] = {};
  bf16x8 fa[2][2];
  static_assert(HD == 4, "one group of four k-steps per loop iteration");
  auto rd1 = [&](int j, int dt) { fa[j & 1][dt] = cat(lds_tr(aa[dt] + (j & 3) * 2048), lds_tr(aa[dt] + (j & 3) * 2048 + 1024)); };
  auto step = [&](int j, bool ahead) {
    SB();
    const bf16x8 b = __builtin_bit_cast(bf16x8, hr[j]);
    acc[0] = mfma32(fa[j & 1][0], b, acc[0]);
    SB();
    if (ahead) rd1(j + 1, 0);
    SB();
    acc[1] = mfma32(fa[j & 1][1], b, acc[1]);
    SB();
    if (ahead) rd1(j + 1, 1);
    SB();
  };
  u32x4 yv; int yok;
  rd1(0, 0); rd1(0, 1);
  int st = rot + 4; st -= st >= 28 ? 28 : 0;
#pragma unroll 1
  for (int g = 0; g < 6; ++g) {
#pragma unroll
    for (int j = 0; j < 4; ++j) {
      if (j == 3) {
        const unsigned d = (g & 3) == 3 ? (unsigned)-98304 : 32768u;
        aa[0] += d; aa[1] += d;
        bar();
        TS2(24 + g);
      }
      asm volatile("s_waitcnt vmcnt(3)" : "+v"(hr[j])::"memory");
      step(j, true);
      asm volatile("buffer_load_dwordx4 %0, %1, %2, %3 offen" HPOL : "=v"(hr[j]) : "v"(vh), "s"(rws), "s"(hb + st * 1024) : "memory");
      st = st == 27 ? 0 : st + 1;
      if (j == 3 && g == 2 && threadIdx.x == 0) post_flag(p.ws + WS_FY + (x * 28 + s) * 128, tag);
      SB();
    }
  }
  yok = __builtin_amdgcn_readfirstlane(lds_ld<unsigned>(lb + 131072));
  {
    const unsigned oy = yok ? WS_YT + (64 * db + (threadIdx.x >> 3)) * TROW + (256 * x + 64 * i2) * 2 + (threadIdx.x & 7) * 16 : 0x80000000u;
    asm volatile("buffer_load_dwordx4 %0, %1, %2, 0 offen sc1" : "=&v"(yv) : "v"(oy), "s"(rws) : "memory");
  }
  asm volatile("s_waitcnt vmcnt(4)" : "+v"(hr[0])::"memory"); step(0, true);
  asm volatile("s_waitcnt vmcnt(3)" : "+v"(hr[1])::"memory"); step(1, true);
  asm volatile("s_waitcnt vmcnt(2)" : "+v"(hr[2])::"memory"); step(2, true);
  asm volatile("s_waitcnt vmcnt(1)" : "+v"(hr[3])::"memory"); step(3, false);
  u32x4 xq = cx.xq, sg = cx.sg;
  TS(11);
  const unsigned pb = lb, yb = lb + 135168;
  {
    const int row = 32 * th + (lane & 31);
#pragma unroll
    for (int dt = 0; dt < 2; ++dt)
#pragma unroll
      for (int G = 0; G < 4; ++G) {
        const f32x4 t = {acc[dt][4 * G], acc[dt][4 * G + 1], acc[dt][4 * G + 2], acc[dt][4 * G + 3]};
        lds_st<f32x4>(pb + kq * 16384 + row * 256 + (((8 * dt + 2 * G + hh) ^ (row & 15)) * 16), t);
      }
  }
  wait_vm<0>();
  asm volatile("" : "+v"(xq), "+v"(sg), "+v"(yv));
  if (yok) lds_st<u32x4>(yb + (threadIdx.x >> 3) * 144 + (threadIdx.x & 7) * 16, yv);
  float mean = __uint_as_float(sg[1]), rstd = __uint_as_float(sg[3]);
  if (!__all(sg[0] == tag && sg[2] == tag)) stats_wait(rws, WS_STATS + tok * 16, tag, mean, rstd);
  bar();
  if (!yok) {
    yv = __builtin_amdgcn_raw_buffer_load_b128(rws, WS_YT + (64 * db + (threadIdx.x >> 3)) * TROW + (256 * x + 64 * i2) * 2 + (threadIdx.x & 7) * 16, 0, 16);
    lds_st<u32x4>(yb + (threadIdx.x >> 3) * 144 + (threadIdx.x & 7) * 16, yv);
    bar();
  }
  f32x4 o0 = {}, o1 = {};
#pragma unroll
  for (int k4 = 0; k4 < 4; ++k4) {
    o0 += lds_ld<f32x4>(pb + k4 * 16384 + tk * 256 + (((2 * ck) ^ (tk & 15)) * 16));
    o1 += lds_ld<f32x4>(pb + k4 * 16384 + tk * 256 + (((2 * ck + 1) ^ (tk & 15)) * 16));
  }
  const unsigned ya = yb + (8 * ck + q) * 144 + (16 * (w >> 1) + 4 * pp) * 2;
  const s16x4 y0 = lds_tr(ya), y1 = lds_tr(ya + 4 * 144);
  u32x4 ov;
#pragma unroll
  for (int e = 0; e < 8; e += 2) {
    const f32x2 o = e < 4 ? (f32x2){o0[e & 3], o0[(e & 3) + 1]} : (f32x2){o1[e & 3], o1[(e & 3) + 1]};
    const f32x2 xv = {bf_lo(xq[e >> 1]), bf_hi(xq[e >> 1])};
    const unsigned yp = e < 4 ? __builtin_bit_cast(u32x2, y0)[(e & 3) >> 1] : __builtin_bit_cast(u32x2, y1)[(e & 3) >> 1];
    const f32x2 yy = {bf_lo(yp), bf_hi(yp)};
    const f32x2 t = o * (f32x2){-1.4426950409f, -1.4426950409f};
    const f32x2 dn = (f32x2){__builtin_amdgcn_exp2f(t[0]), __builtin_amdgcn_exp2f(t[1])} + (f32x2){1.f, 1.f};
    const f32x2 g = o * (f32x2){__builtin_amdgcn_rcpf(dn[0]), __builtin_amdgcn_rcpf(dn[1])};
    const f32x2 xn = rbf2(rbf2(xv - (f32x2){mean, mean}) * (f32x2){rstd, rstd});
    const f32x2 r2 = __builtin_elementwise_fma(rbf2(xn * yy), g, xv);
    ov[e >> 1] = pk_bf16(r2[0], r2[1]);
  }
  __builtin_amdgcn_raw_buffer_store_b128(ov, ro, tok * 896 + db * 128 + ck * 16, 0, OUTAUX);
  TS(12);
}

__global__ __launch_bounds__(1024, 4) void h3_geglu_kernel(Params p) {
  __shared__ __attribute__((aligned(16))) char smem[LDS_BYTES];
  const unsigned lb = (unsigned)(size_t)(LAS char*)smem;
  const int tid = threadIdx.x, lane = tid & 63, w = __builtin_amdgcn_readfirstlane(tid >> 6);
  const int b = blockIdx.x, x = b & 7, s = b >> 3;
  TS(0);
  if (s < 28) {
    if (w >= 12) {
      loader_w(p, lb, x, s, w - 12, lane);
    } else if (w >= 8) {
      loader_x(p, lb, x, s, w - 8, lane);
    } else {
      Ctx cx;
      phase_gemm1(p, lb, x, s, w, lane, cx);
      phase_conv(p, lb, x, s, w, lane, cx);
      TS(9);
      phase_gemm2(p, lb, x, s, w, lane, cx);
    }
  } else {
    const int j = b - 224;
    if (TLBW && (j >> 3) == 0 && w >= 12) {
      if (w < 15) touch(mk_rsrc(p.ws, WS_END), (unsigned)((w - 12) * 64 + lane) * 65536u, lb + 65536);
      else touch(mk_rsrc(p.out, S_ * D_ * 2), (unsigned)lane * 65536u, lb + 65536);
    }
    if (WARM && w < 14) {
      const int q = j >> 3;
      touch(mk_rsrc((w & 1) ? p.v : p.k, S_ * D_ * 2), (unsigned)((256 * x + 64 * q) * 896 + ((w >> 1) * 64 + lane) * 128), lb + 65536);
    }
    if (j < 28) {
      const unsigned tag = __builtin_amdgcn_readfirstlane(*(const GAS unsigned*)(p.ws + WS_EPOCH) + 1u);
      const int m = w & 1, r0 = (w >> 1) * 8 + (lane >> 3);
      const rsrc_t rm = mk_rsrc(m ? p.wu : p.wg, D_ * F_ * 2);
      u32x4 t[7];
#pragma unroll
      for (int n = 0; n < 7; ++n) t[n] = __builtin_amdgcn_raw_buffer_load_b128(rm, (r0 + 64 * n) * 3584 + j * 128 + (lane & 7) * 16, 0, 0);
      float a[8] = {};
#pragma unroll
      for (int n = 0; n < 7; ++n)
#pragma unroll
        for (int i = 0; i < 4; ++i) { a[2 * i] += bf_lo(t[n][i]); a[2 * i + 1] += bf_hi(t[n][i]); }
      lds_st<f32x4>(lb + (w * 8 + (lane >> 3)) * 256 + (lane & 7) * 32, (f32x4){a[0], a[1], a[2], a[3]});
      lds_st<f32x4>(lb + (w * 8 + (lane >> 3)) * 256 + (lane & 7) * 32 + 16, (f32x4){a[4], a[5], a[6], a[7]});
      bar();
      if (w < 2) {
        float sum = 0.f;
#pragma unroll
        for (int q = 0; q < 64; ++q) sum += lds_ld<float>(lb + (((q >> 3) * 2 + w) * 8 + (q & 7)) * 256 + lane * 4);
        const u32x2 g = {tag, __float_as_uint(sum)};
        __builtin_amdgcn_raw_buffer_store_b64(g, mk_rsrc(p.ws, WS_END), WS_CS + (w * 1792 + j * 64 + lane) * 8, 0, 16);
      }
    }
    if (WARM && w < 14) {
      const int blk = w * 32 + j;
      if (blk < 196) touch(mk_rsrc(p.wd, F_ * D_ * 2), (unsigned)(blk * 64 + lane) * 128u, lb + 65536);
      else touch(mk_rsrc(p.h, D_ * S_ * 2), (unsigned)((blk - 196) * 64 + lane) * 128u, lb + 65536);
    }
    if (b == 224 && w == 15) {
      const unsigned tag = __builtin_amdgcn_readfirstlane(*(const GAS unsigned*)(p.ws + WS_EPOCH) + 1u);
#pragma unroll 1
      for (int n = 0; n < 4; ++n) poll_flags_slow(p.ws + WS_FH + n * 56 * 128, 56, tag, lane);
      if (lane == 0) post_flag(p.ws + WS_EPOCH, tag);
    }
    wait_vm<0>();
  }
  TS(15);
}

extern "C" void kernel_launch(void* const* d_in, const int* in_sizes, int n_in, void* d_out, int out_size, void* d_ws, size_t ws_size, hipStream_t stream) {
  Params p;
  p.q = (const u16*)d_in[0]; p.k = (const u16*)d_in[1]; p.v = (const u16*)d_in[2]; p.sk = (const u16*)d_in[3];
  p.h = (const u16*)d_in[4]; p.dv = (const u16*)d_in[5];
  p.wg = (const u16*)d_in[7]; p.wu = (const u16*)d_in[8]; p.wd = (const u16*)d_in[9];
  p.out = (u16*)d_out; p.ws = (char*)d_ws;
  h3_geglu_kernel<<<256, 1024, 0, stream>>>(p);
}
```

```cpp
#include <hip/hip_runtime.h>
#include <hip/hip_bf16.h>
#include <stdint.h>

typedef unsigned short u16;
typedef __attribute__((ext_vector_type(8))) short bf16x8;
typedef __attribute__((ext_vector_type(4))) short s16x4;
typedef __attribute__((ext_vector_type(4))) float f32x4;
typedef __attribute__((ext_vector_type(2))) float f32x2;
typedef __attribute__((ext_vector_type(16))) float f32x16;
typedef __attribute__((ext_vector_type(4))) unsigned u32x4;
typedef __attribute__((ext_vector_type(2))) unsigned u32x2;
typedef __attribute__((ext_vector_type(2))) __bf16 bf16x2;
typedef __amdgpu_buffer_rsrc_t rsrc_t;
#define HSPOL " sc1"
#define LAS __attribute__((address_space(3)))
#define GAS __attribute__((address_space(1)))
#define DI __device__ __forceinline__

#define OPT_OWN 0
#define KVT 1
#define KVFIRST 0
#define WDLATE 1
#define PAUX 2
#define USTAUX 16
#define HSLEEP 10
#define LPRIO 0
#define WARM 1
#define UPRIO 2
#define KVPOL " nt"
#define WPRIO 0
#define XPRIO 0
#define WDT 0
#define DRA 2
#define DRR 1
#define TLBW 0
#define OUTAUX 0
#define PREW 0
#define CODEW 1
constexpr int CODE_LINES = 224;
#define PFN 17

constexpr int S_ = 2048, D_ = 448, F_ = 1792;
constexpr unsigned WS_EPOCH = 0, WS_FH = 8192, WS_FY = 36864, WS_STATS = 65536, WS_FU = 98304, WS_CS = 2031616;
constexpr unsigned TROW = 4224;
constexpr unsigned WS_UT = 131072, WS_YT = 2097152, WS_H = 4194304, WS_END = WS_H + 7340032;
constexpr unsigned WS_DBG = 16u << 20;
constexpr int LDS_BYTES = 163840;
constexpr unsigned SPIN_CAP = 1u << 14;

struct Params {
  const u16 *q, *k, *v, *sk, *h, *dv, *wg, *wu, *wd;
  u16* out;
  char* ws;
};

DI rsrc_t mk_rsrc(const void* p, unsigned bytes) { return __builtin_amdgcn_make_buffer_rsrc((void*)p, (short)0, (int)bytes, 0x00020000); }
DI void dma16(rsrc_t r, unsigned voff, unsigned soff, unsigned lds) {
  asm volatile("s_mov_b32 m0, %0\n\ts_nop 0\n\tbuffer_load_dwordx4 %1, %2, %3 offen lds" ::"s"(lds), "v"(voff), "s"(r), "s"(soff) : "memory");
}
DI void touch(rsrc_t r, unsigned voff, unsigned lds) {
  asm volatile("s_mov_b32 m0, %0\n\ts_nop 0\n\tbuffer_load_dword %1, %2, 0 offen lds" ::"s"(lds), "v"(voff), "s"(r) : "memory");
}
template <int NT> DI void touch_p(rsrc_t r, unsigned voff, unsigned lds) {
  if (NT) asm volatile("s_mov_b32 m0, %0\n\ts_nop 0\n\tbuffer_load_dword %1, %2, 0 offen nt lds" ::"s"(lds), "v"(voff), "s"(r) : "memory");
  else touch(r, voff, lds);
}
DI void touch_g(const char* a, unsigned lds) {
  asm volatile("s_mov_b32 m0, %0\n\ts_nop 0\n\tglobal_load_lds_dword %1, off" ::"s"(lds), "v"(a) : "memory");
}
template <unsigned LA, unsigned SA>
DI void dma16c(rsrc_t r, unsigned voff, unsigned sbase, unsigned lbase) {
  unsigned t;
  asm volatile("s_add_u32 m0, %2, %5\n\ts_add_u32 %0, %3, %6\n\tbuffer_load_dwordx4 %1, %4, %0 offen lds"
               : "=&s"(t) : "v"(voff), "s"(lbase), "s"(sbase), "s"(r), "i"(LA), "i"(SA) : "memory", "scc");
}
template <int N> DI void wait_vm() { asm volatile("s_waitcnt vmcnt(%0)" ::"i"(N) : "memory"); }
DI void bar() { asm volatile("s_waitcnt lgkmcnt(0)\n\ts_barrier" ::: "memory"); }
DI float bf_lo(unsigned x) { return __uint_as_float(x << 16); }
DI float bf_hi(unsigned x) { return __uint_as_float(x & 0xffff0000u); }
DI unsigned pk_bf16(float lo, float hi) { f32x2 t = {lo, hi}; return __builtin_bit_cast(unsigned, __builtin_convertvector(t, bf16x2)); }
DI bf16x8 lds_b128(const char* p) { return *(const LAS bf16x8*)(p); }
DI s16x4 lds_tr(unsigned a) { return __builtin_amdgcn_ds_read_tr16_b64_v4i16((LAS s16x4*)(size_t)a); }
DI bf16x8 cat(s16x4 a, s16x4 b) { return __builtin_shufflevector(a, b, 0, 1, 2, 3, 4, 5, 6, 7); }
template <class T> DI T lds_ld(unsigned a) { return *(const LAS T*)(size_t)a; }
template <class T> DI void lds_st(unsigned a, T v) { *(LAS T*)(size_t)a = v; }
DI f32x16 mfma32(bf16x8 a, bf16x8 b, f32x16 c) { return __builtin_amdgcn_mfma_f32_32x32x16_bf16(a, b, c, 0, 0, 0); }
DI f32x4 mfma16(bf16x8 a, bf16x8 b, f32x4 c) { return __builtin_amdgcn_mfma_f32_16x16x32_bf16(a, b, c, 0, 0, 0); }
DI unsigned long long rtime() { return __builtin_amdgcn_s_memrealtime(); }
DI float dpp_add(float v, const int ctrl_row_shr) { return v; }
template <int CTRL, int RM> DI float dpp_mov(float v) { return __builtin_bit_cast(float, __builtin_amdgcn_update_dpp(0, __builtin_bit_cast(int, v), CTRL, RM, 0xf, true)); }
DI float wave_sum(float v) {
  v += dpp_mov<0x111, 0xf>(v); v += dpp_mov<0x112, 0xf>(v); v += dpp_mov<0x114, 0xf>(v); v += dpp_mov<0x118, 0xf>(v);
  v += dpp_mov<0x142, 0xa>(v); v += dpp_mov<0x143, 0xc>(v);
  return __builtin_bit_cast(float, __builtin_amdgcn_readlane(__builtin_bit_cast(int, v), 63));
}
#define TS(i)
#define TSL(i)
#define TSL_DECL
#define TSL_DUMP()
#define TS2(i)

DI void poll_flags(const char* base, int n, unsigned tag, int lane) {
  const GAS unsigned* f = (const GAS unsigned*)(base + (lane < n ? lane : 0) * 128);
  for (unsigned it = 0; it < SPIN_CAP; ++it) {
    unsigned x = __hip_atomic_load(f, __ATOMIC_RELAXED, __HIP_MEMORY_SCOPE_AGENT);
    if (__all(x == tag)) break;
    __builtin_amdgcn_s_sleep(2);
  }
}
DI void poll_flags_slow(const char* base, int n, unsigned tag, int lane) {
  const GAS unsigned* f = (const GAS unsigned*)(base + (lane < n ? lane : 0) * 128);
  for (unsigned it = 0; it < SPIN_CAP; ++it) {
    unsigned x = __hip_atomic_load(f, __ATOMIC_RELAXED, __HIP_MEMORY_SCOPE_AGENT);
    if (__all(x == tag)) break;
    __builtin_amdgcn_s_sleep(32);
  }
}
DI void post_flag(char* addr, unsigned tag) { __hip_atomic_store((GAS unsigned*)addr, tag, __ATOMIC_RELAXED, __HIP_MEMORY_SCOPE_AGENT); }

DI bool flags_ok(unsigned v, unsigned tag) { return __all(v == tag); }
DI unsigned flag_off(int n, int lane) { return (lane < n ? lane : 0) * 128; }
#define SB() __builtin_amdgcn_sched_barrier(0)
template <int V> struct IC { static constexpr int value = V; };
DI void asm_pin(unsigned& v) { asm volatile("" : "+v"(v)); }
DI float rbf(float x) { return __uint_as_float(pk_bf16(x, 0.f) << 16); }

DI f32x2 rbf2(f32x2 v) { const unsigned u = pk_bf16(v[0], v[1]); return (f32x2){bf_lo(u), bf_hi(u)}; }
DI u32x4 row_stats(u32x4 d, unsigned tag) {
  float e[8];
#pragma unroll
  for (int i = 0; i < 4; ++i) { e[2 * i] = bf_lo(d[i]); e[2 * i + 1] = bf_hi(d[i]); }
  float s = ((e[0] + e[1]) + (e[2] + e[3])) + ((e[4] + e[5]) + (e[6] + e[7]));
  const float mean = wave_sum(s) * (1.f / D_);
  const float on = (threadIdx.x & 63) < 56 ? 1.f : 0.f;
  float ss = 0.f;
#pragma unroll
  for (int i = 0; i < 8; ++i) { const float t = e[i] - mean; ss = fmaf(t, t, ss); }
  const float var = wave_sum(ss * on) * (1.f / D_);
  const float den = rbf(__builtin_amdgcn_sqrtf(rbf(rbf(var) + 1e-5f)));
  u32x4 g = {tag, __float_as_uint(rbf(mean)), tag, __float_as_uint(__builtin_amdgcn_rcpf(den))};
  return g;
}
DI void stats_wait(rsrc_t rs, unsigned off, unsigned tag, float& mean, float& rstd) {
  for (unsigned it = 0;; ++it) {
    const u32x4 g = __builtin_amdgcn_raw_buffer_load_b128(rs, off, 0, 16);
    mean = __uint_as_float(g[1]); rstd = __uint_as_float(g[3]);
    if (__all(g[0] == tag && g[2] == tag) || it >= SPIN_CAP) break;
    __builtin_amdgcn_s_sleep(1);
  }
}

constexpr unsigned CV_BASE = 98304;
constexpr unsigned CV_UP = CV_BASE;
constexpr unsigned CV_HS = CV_BASE + 9216;
constexpr unsigned CV_RED = CV_HS;
constexpr unsigned L_UT = 147456, L_SK = 155648, L_ST = 159744;
constexpr unsigned L_CS = 157696, L_OK = 158720, L_CNT = 158728, L_TOUCH = 158976;

#define HPOL " sc1"
#define HDEPTH 4
constexpr int HD = HDEPTH;
struct Ctx {
  unsigned tag; float dv; u32x4 hr[HD], xq, sg;
};
DI void g2_stage(rsrc_t rw, unsigned vw, int kq, int th, int grp, int rot, unsigned lb) {
#pragma unroll
  for (int n = 0; n < 4; ++n) {
    int st = 4 * grp + n + rot; st -= st >= 28 ? 28 : 0;
    dma16(rw, vw, (unsigned)((28 * kq + st) * 16 + 8 * th) * 896u, lb + (grp & 3) * 32768 + kq * 8192 + n * 2048 + th * 1024);
  }
}
DI void g2_piece(rsrc_t rw, unsigned vw, int kq, int th, int step, int rot, unsigned lb) {
  int st = step + rot; st -= st >= 28 ? 28 : 0;
  dma16(rw, vw, (unsigned)((28 * kq + st) * 16 + 8 * th) * 896u, lb + ((step >> 2) & 3) * 32768 + kq * 8192 + (step & 3) * 2048 + th * 1024);
}
DI void h_load(const Params& p, u32x4& dst, unsigned hb, int step, int rot, unsigned vh) {
  int st = step + rot; st -= st >= 28 ? 28 : 0;
  asm volatile("buffer_load_dwordx4 %0, %1, %2, %3 offen" HPOL : "=v"(dst) : "v"(vh), "s"(mk_rsrc(p.ws, WS_END)), "s"(hb + st * 1024) : "memory");
}
DI int g2_rot(int s);
DI void h_first(const Params& p, int x, int s, int w, int lane, Ctx& cx) {
  const unsigned hb = WS_H + ((8 * x + 2 * (s / 7) + (w >> 2)) * 112 + 28 * (w & 3)) * 1024;
#pragma unroll
  for (int n = 0; n < HD; ++n) h_load(p, cx.hr[n], hb, n, g2_rot(s), lane * 16);
}
DI unsigned g2_vw(int lane, int db) { const int L3 = lane >> 3, L7 = lane & 7; return L3 * 896 + db * 128 + 16 * (L7 ^ (((L3 >> 1) & 1) << 2)); }
DI int g2_rot(int s) { const int r = 4 * (s % 7) + 7 * (s / 7); return r >= 28 ? r - 28 : r; }

DI void phase_gemm1(const Params& p, unsigned lb, int x, int s, int w, int lane, Ctx& cx) {
  const int ib = 4 * (x >> 2) + s / 7, jb = 7 * (x & 3) + s % 7;
  const rsrc_t rq = mk_rsrc(p.q, S_ * D_ * 2), rws = mk_rsrc(p.ws, WS_END);
  const int gw = jb * 8 + w;
  unsigned tag;
  {
    u32x4 sr0, sr1; unsigned etag;
    const unsigned o0 = lane < 56 ? (256 * ib + gw) * 896 + 16 * lane : 0x80000000u;
    const unsigned o1 = (lane < 56 && gw < 32) ? (256 * ib + 224 + gw) * 896 + 16 * lane : 0x80000000u;
    const unsigned oz = WS_EPOCH;
    unsigned dvb; const unsigned od = (2 * (x * 28 + s) + (w >> 2)) * 2;
    asm volatile("buffer_load_dword %2, %6, %9, 0 offen\n\tbuffer_load_dwordx4 %0, %4, %8, 0 offen\n\tbuffer_load_dwordx4 %1, %5, %8, 0 offen\n\t"
                 "buffer_load_ushort %3, %7, %10, 0 offen\n\ts_waitcnt vmcnt(0)"
                 : "=&v"(sr0), "=&v"(sr1), "=&v"(etag), "=&v"(dvb) : "v"(o0), "v"(o1), "v"(oz), "v"(od), "s"(rq), "s"(rws), "s"(mk_rsrc(p.dv, D_ * 2)) : "memory");
    cx.dv = bf_lo(dvb);
    tag = __builtin_amdgcn_readfirstlane(etag) + 1u;
    cx.tag = tag;
    const u32x4 g0 = row_stats(sr0, tag), g1 = row_stats(sr1, tag);
    const unsigned p0 = lane == 0 ? WS_STATS + (256 * ib + gw) * 16 : 0x80000000u;
    const unsigned p1 = (lane == 0 && gw < 32) ? WS_STATS + (256 * ib + 224 + gw) * 16 : 0x80000000u;
    asm volatile("buffer_store_dwordx4 %0, %2, %4, 0 offen sc1\n\tbuffer_store_dwordx4 %1, %3, %4, 0 offen sc1\n\ts_nop 1" ::"v"(g0), "v"(g1), "v"(p0), "v"(p1), "s"(rws) : "memory");
  }
  const int tw = w & 3, fh = w >> 2, r = lane & 31, hh = lane >> 5;
  unsigned xa[4], wa[2];
#pragma unroll
  for (int i = 0; i < 4; ++i) xa[i] = lb + (64 * tw + r) * 128 + 16 * ((2 * i + hh) ^ ((r >> 1) & 7));
  {
    const int blk = (lane >> 4) & 1, q = (lane & 15) >> 2, pp = lane & 3, m = pp >> 1, f = (q >> 1) | ((m ^ hh) << 1);
#pragma unroll
    for (int u = 0; u < 2; ++u) wa[u] = lb + 32768 + m * 8192 + (8 * hh + q) * 128 + 32 * ((2 * fh + u) ^ f) + 16 * blk + 8 * (pp & 1);
  }
  f32x16 acc[2][2] = {};
  if (PREW) {
    f32x16 z = {};
    const bf16x8 o = {0x3c00, 0x3c10, 0x3c20, 0x3c30, 0x3c40, 0x3c50, 0x3c60, 0x3c70};
#pragma unroll 1
    for (int i = 0; i < PREW; ++i) { z = mfma32(o, o, z); z = mfma32(o, o, z); z = mfma32(o, o, z); z = mfma32(o, o, z); }
    asm volatile("" ::"v"(z));
  }
  TS(1);
  bar();
  TS(2);
  bf16x8 fb[2][2] = {}, fa[2][2] = {};
  auto rdA = [&](int ks, int u) { fa[ks & 1][u] = cat(lds_tr(wa[u] + (ks & 3) * 2048), lds_tr(wa[u] + (ks & 3) * 2048 + 512)); };
  auto rdB = [&](int ks) { fb[ks & 1][0] = lds_ld<bf16x8>(xa[ks & 3]); fb[ks & 1][1] = lds_ld<bf16x8>(xa[ks & 3] + 4096); };
  auto step = [&](int ks, bool ahead) {
    const int pb = ks & 1;
    SB();
    acc[0][0] = mfma32(fa[pb][0], fb[pb][0], acc[0][0]);
    SB();
    if (ahead) rdA(ks + 1, 0);
    SB();
    acc[1][0] = mfma32(fa[pb][0], fb[pb][1], acc[1][0]);
    SB();
    if (ahead) rdB(ks + 1);
    SB();
    acc[0][1] = mfma32(fa[pb][1], fb[pb][0], acc[0][1]);
    SB();
    if (ahead) rdA(ks + 1, 1);
    SB();
    acc[1][1] = mfma32(fa[pb][1], fb[pb][1], acc[1][1]);
    SB();
  };
  rdA(0, 0); rdB(0); rdA(0, 1);
  int sl = 0;
#pragma unroll 1
  for (int c = 0; c < 6; ++c) {
    step(0, true);
    step(1, true);
    step(2, true);
    {
      const unsigned d = __builtin_amdgcn_readfirstlane(sl == 2 ? -98304 : 49152);
      sl = sl == 2 ? 0 : sl + 1;
#pragma unroll
      for (int i = 0; i < 4; ++i) xa[i] += d;
      wa[0] += d; wa[1] += d;
    }
    bar();
    TS2(16 + c);
    step(3, true);
    if (c == 5 && threadIdx.x == 0) post_flag(p.ws + WS_FU + (((x * 28 + s) % 7) * 32 + (x * 28 + s) / 7) * 128, tag);
  }
  step(0, true); step(1, true); step(2, true); step(3, false);
  TS(3);
  const unsigned csb = lb + L_CS;
  bar();
  TS(4);
  float mean[2], rstd[2];
#pragma unroll
  for (int tt = 0; tt < 2; ++tt) { const f32x2 t = lds_ld<f32x2>(lb + L_ST + (64 * tw + 32 * tt + r) * 8); mean[tt] = t[0]; rstd[tt] = t[1]; }
  constexpr float C0 = -2.3022081985f, C1 = -0.1029432397f;
#pragma unroll
  for (int u = 0; u < 2; ++u) {
    f32x4 cg[2], cu[2];
#pragma unroll
    for (int b = 0; b < 2; ++b) {
      cg[b] = lds_ld<f32x4>(csb + (16 * (2 * fh + u) + 8 * b + 4 * hh) * 4);
      cu[b] = lds_ld<f32x4>(csb + 256 + (16 * (2 * fh + u) + 8 * b + 4 * hh) * 4);
    }
#pragma unroll
    for (int tt = 0; tt < 2; ++tt) {
      const float rr = rstd[tt], k0s = rr * C0, k1s = C1 * rr * rr * rr, ir2s = __builtin_amdgcn_rcpf(rr * rr);
      const f32x2 nm = {-mean[tt], -mean[tt]}, k0 = {k0s, k0s}, k1 = {k1s, k1s}, ir2 = {ir2s, ir2s};
      u32x4 o;
#pragma unroll
      for (int b = 0; b < 2; ++b)
#pragma unroll
        for (int e = 0; e < 4; e += 2) {
          const f32x2 ag = {acc[tt][u][8 * b + e], acc[tt][u][8 * b + e + 1]}, au = {acc[tt][u][8 * b + 4 + e], acc[tt][u][8 * b + 5 + e]};
          const f32x2 c2g = {cg[b][e], cg[b][e + 1]}, c2u = {cu[b][e], cu[b][e + 1]};
          const f32x2 a = __builtin_elementwise_fma(nm, c2g, ag), bb = __builtin_elementwise_fma(nm, c2u, au);
          const f32x2 z = a * __builtin_elementwise_fma(a * a, k1, k0);
          const f32x2 ex = {__builtin_amdgcn_exp2f(z[0]), __builtin_amdgcn_exp2f(z[1])};
          const f32x2 dn = __builtin_elementwise_fma(ex, ir2, ir2);
          const f32x2 rc = {__builtin_amdgcn_rcpf(dn[0]), __builtin_amdgcn_rcpf(dn[1])};
          const f32x2 hv = (a * bb) * rc;
          o[2 * b + (e >> 1)] = pk_bf16(hv[0], hv[1]);
        }
      const unsigned oh = WS_H + ((8 * ib + 2 * tw + tt) * 112 + 4 * jb + 2 * fh + u) * 1024 + lane * 16;
      asm volatile("buffer_store_dwordx4 %0, %1, %2, 0 offen" HSPOL "\n\ts_nop 1" ::"v"(o), "v"(oh), "s"(rws) : "memory");
    }
  }
  TS(5);
}

struct WdStage {
  rsrc_t rw; unsigned vw, lb; int kq, th, rot;
  DI void group(int g) const {
#pragma unroll
    for (int n = 0; n < 4; ++n) g2_piece(rw, vw, kq, th, 4 * g + n, rot, lb);
  }
};
#define TAILARG
#define TAILPASS
DI void loader_tail(const Params& p, unsigned lb, int s, int lane, const WdStage& wd, bool poller, unsigned tag TAILARG) {
  TSL(12);
  bar();
  TSL(13);
  wd.group(3);
  TSL(14);
  bar();
  wd.group(4);
  bar();
  wd.group(5);
  TSL(22);
  wait_vm<8>();
  TSL(23);
  bar();
  wd.group(6);
  wait_vm<8>();
  TSL(30);
  bar();
  wait_vm<4>();
  bar();
  if (poller) {
    const unsigned yf = __hip_atomic_load((const GAS unsigned*)(p.ws + WS_FY + (s % 7) * 32 * 128 + flag_off(32, lane)), __ATOMIC_RELAXED, __HIP_MEMORY_SCOPE_AGENT);
    const unsigned ok = flags_ok(yf, tag) ? 1u : 0u;
    if (lane == 0) lds_st<unsigned>(lb + 131072, ok);
  }
  wait_vm<0>();
  bar();
  const int yok = __builtin_amdgcn_readfirstlane(lds_ld<unsigned>(lb + 131072));
  if (poller && !yok) poll_flags(p.ws + WS_FY + (s % 7) * 32 * 128, 32, tag, lane);
  bar();
  if (!yok) bar();
}

DI void loader_x(const Params& p, unsigned lb, int x, int s, int lw, int lane) {
  const int ib = 4 * (x >> 2) + s / 7;
  const rsrc_t rq = mk_rsrc(p.q, S_ * D_ * 2);
  const int L3 = lane >> 3, L7 = lane & 7;
  const unsigned vx0 = (64 * lw + L3) * 896 + 16 * (L7 ^ ((lane >> 4) & 3));
  const unsigned vx1 = (64 * lw + 8 + L3) * 896 + 16 * (L7 ^ (((lane >> 4) & 3) | 4));
  const unsigned sx = ib * 256 * 896, lx = lb + lw * 8192;
  auto piece = [&](auto C, auto I) {
    constexpr int c = decltype(C)::value, i = decltype(I)::value;
    dma16c<(c % 3) * 49152 + i * 1024, c * 128 + (i >> 1) * 16 * 896>(rq, (i & 1) ? vx1 : vx0, sx, lx);
  };
  auto stage = [&](auto C) {
    piece(C, IC<0>{}); piece(C, IC<1>{}); piece(C, IC<2>{}); piece(C, IC<3>{}); piece(C, IC<4>{}); piece(C, IC<5>{}); piece(C, IC<6>{}); piece(C, IC<7>{});
  };
  if (XPRIO) __builtin_amdgcn_s_setprio(XPRIO);
  const rsrc_t rws = mk_rsrc(p.ws, WS_END);
  unsigned etag;
  TSL_DECL
  TS(8);
  { const unsigned oz = WS_EPOCH; asm volatile("buffer_load_dword %0, %1, %2, 0 offen" : "=&v"(etag) : "v"(oz), "s"(rws) : "memory"); }
  stage(IC<0>{});
  stage(IC<1>{});
  wait_vm<8>();
  asm volatile("" : "+v"(etag));
  const unsigned tag = __builtin_amdgcn_readfirstlane(etag) + 1u;
  TS(1);
  bar();
  stage(IC<2>{});
  auto lstage = [&](auto C) {
    constexpr int c = decltype(C)::value;
    TSL(16 + c);
    if (c < 5) wait_vm<8>(); else wait_vm<0>();
    TSL(24 + c);
    bar();
    TSL(2 + c);
    if constexpr (c + 3 <= 6) stage(IC<c + 3>{});
    if (c == 4) {
      const unsigned os = WS_STATS + (256 * ib + 64 * lw + lane) * 16;
      float mean, rstd;
      stats_wait(rws, os, tag, mean, rstd);
      lds_st<f32x2>(lb + L_ST + (64 * lw + lane) * 8, (f32x2){mean, rstd});
    }
  };
  lstage(IC<0>{}); lstage(IC<1>{}); lstage(IC<2>{}); lstage(IC<3>{}); lstage(IC<4>{}); lstage(IC<5>{});
  bar();
  bar();
  const WdStage wd = {mk_rsrc(p.wd, F_ * D_ * 2), g2_vw(lane, s % 7), lb, lw, 0, g2_rot(s)};
  TSL(8);
  wd.group(0); wd.group(1); wd.group(2);
  TSL(9);
  bar();
  bar();
  wait_vm<0>();
  loader_tail(p, lb, s, lane, wd, false, 0u TAILPASS);
  TSL_DUMP();
}

DI void loader_w(const Params& p, unsigned lb, int x, int s, int lw, int lane) {
  const int jb = 7 * (x & 3) + s % 7;
  const rsrc_t rg = mk_rsrc(p.wg, D_ * F_ * 2), ru = mk_rsrc(p.wu, D_ * F_ * 2);
  const rsrc_t rw = mk_rsrc(p.wd, F_ * D_ * 2), rws = mk_rsrc(p.ws, WS_END);
  const int L3 = lane >> 3, L7 = lane & 7;
  const unsigned vwA = (16 * lw + L3) * 3584 + jb * 128 + 32 * ((L7 >> 1) ^ ((lane >> 4) & 1)) + 16 * (L7 & 1);
  const unsigned vwB = (16 * lw + L3) * 3584 + jb * 128 + 32 * ((L7 >> 1) ^ (((lane >> 4) & 1) | 2)) + 16 * (L7 & 1);
  const unsigned lx = lb + 32768 + lw * 2048;
  auto stage = [&](auto C) {
    constexpr int c = decltype(C)::value;
    constexpr int cm = c;
    dma16c<(c % 3) * 49152, cm * 64 * 3584>(rg, vwA, 0, lx);
    dma16c<(c % 3) * 49152 + 1024, cm * 64 * 3584 + 8 * 3584>(rg, vwB, 0, lx);
    dma16c<(c % 3) * 49152 + 8192, cm * 64 * 3584>(ru, vwB, 0, lx);
    dma16c<(c % 3) * 49152 + 8192 + 1024, cm * 64 * 3584 + 8 * 3584>(ru, vwA, 0, lx);
  };
  if (WPRIO) __builtin_amdgcn_s_setprio(WPRIO);
  unsigned etag;
  { const unsigned oz = WS_EPOCH; asm volatile("buffer_load_dword %0, %1, %2, 0 offen" : "=&v"(etag) : "v"(oz), "s"(rws) : "memory"); }
  stage(IC<0>{});
  stage(IC<1>{});
  wait_vm<4>();
  asm volatile("" : "+v"(etag));
  const unsigned tag = __builtin_amdgcn_readfirstlane(etag) + 1u;
  bar();
  const int mi = x * 28 + s, cb = mi % 7, tb = mi / 7, ucg = lane >> 3, urp = lane & 7, ut0 = 64 * tb + 16 * lw + 2 * urp;
  u32x4 kk[5], vv[2];
  stage(IC<2>{});
  {
    const rsrc_t rk = mk_rsrc(p.k, S_ * D_ * 2), rv = mk_rsrc(p.v, S_ * D_ * 2);
    unsigned ok[5];
#pragma unroll
    for (int n = 0; n < 5; ++n) ok[n] = ut0 - 3 + n >= 0 ? (unsigned)((ut0 - 3 + n) * 896 + cb * 128 + ucg * 16) : 0x80000000u;
    const unsigned ov = ut0 * 896 + cb * 128 + ucg * 16;
    asm volatile("buffer_load_dwordx4 %0, %7, %13, 0 offen" KVPOL "\n\tbuffer_load_dwordx4 %1, %8, %13, 0 offen" KVPOL "\n\tbuffer_load_dwordx4 %2, %9, %13, 0 offen" KVPOL "\n\t"
                 "buffer_load_dwordx4 %3, %10, %13, 0 offen" KVPOL "\n\tbuffer_load_dwordx4 %4, %11, %13, 0 offen" KVPOL "\n\t"
                 "buffer_load_dwordx4 %5, %12, %14, 0 offen" KVPOL "\n\tbuffer_load_dwordx4 %6, %12, %14, 0 offen offset:896" KVPOL
                 : "=&v"(kk[0]), "=&v"(kk[1]), "=&v"(kk[2]), "=&v"(kk[3]), "=&v"(kk[4]), "=&v"(vv[0]), "=&v"(vv[1])
                 : "v"(ok[0]), "v"(ok[1]), "v"(ok[2]), "v"(ok[3]), "v"(ok[4]), "v"(ov), "s"(rk), "s"(rv) : "memory");
    dma16(mk_rsrc(p.sk, D_ * 8), lane < 32 ? cb * 512 + lane * 16 : 0x80000000u, 0, lb + L_SK);
  }
  auto wd_touch = [&](int i) {
    const int sh = 2 * i + (lw & 1);
    if ((lw >> 1) == (i & 1) && sh < 7) touch(rw, (unsigned)((s * 7 + sh) * 64 + lane) * 128u, lb + L_TOUCH);
  };
  const int chl = lw >> 1, ch = 2 * mi + chl, tA = (lw & 1) * 128 + lane;
  u32x4 hA, hB; u32x2 gA, gB, cg2; unsigned uf = 0;
  auto upairs = [&](const int j0) {
    const int tl = 16 * lw + 2 * urp, e = (tl & ~7) + 6 - (tl & 7);
    const unsigned ta = lb + L_UT + 8 * ucg * 128 + (((e >> 3) ^ ucg) * 16) + 2 * (e & 7);
    u32x4 wq[2];
#pragma unroll
    for (int j = 0; j < 2; ++j) wq[j] = lds_ld<u32x4>(lb + L_SK + ucg * 64 + (j0 + j) * 16);
    f32x2 kf[2][5], vf[2][2], wt[2][4], s0[2], s1[2];
#pragma unroll
    for (int j = 0; j < 2; ++j) {
#pragma unroll
      for (int n = 0; n < 5; ++n) kf[j][n] = (f32x2){bf_lo(kk[n][j0 + j]), bf_hi(kk[n][j0 + j])};
#pragma unroll
      for (int n = 0; n < 2; ++n) vf[j][n] = (f32x2){bf_lo(vv[n][j0 + j]), bf_hi(vv[n][j0 + j])};
    }
#pragma unroll
    for (int j = 0; j < 2; ++j) {
      wt[j][0] = (f32x2){bf_lo(wq[j][0]), bf_lo(wq[j][2])}; wt[j][1] = (f32x2){bf_hi(wq[j][0]), bf_hi(wq[j][2])};
      wt[j][2] = (f32x2){bf_lo(wq[j][1]), bf_lo(wq[j][3])}; wt[j][3] = (f32x2){bf_hi(wq[j][1]), bf_hi(wq[j][3])};
    }
    SB();
#pragma unroll
    for (int j = 0; j < 2; ++j) { s0[j] = kf[j][0] * wt[j][3]; s1[j] = kf[j][1] * wt[j][3]; }
    SB();
#pragma unroll
    for (int t = 2; t >= 0; --t) {
#pragma unroll
      for (int j = 0; j < 2; ++j) { s0[j] = __builtin_elementwise_fma(kf[j][3 - t], wt[j][t], s0[j]); s1[j] = __builtin_elementwise_fma(kf[j][4 - t], wt[j][t], s1[j]); }
      SB();
    }
#pragma unroll
    for (int j = 0; j < 2; ++j) { s0[j] *= vf[j][0]; s1[j] *= vf[j][1]; }
    SB();
#pragma unroll
    for (int j = 0; j < 2; ++j) {
      lds_st<unsigned>(ta + (2 * (j0 + j)) * 128, pk_bf16(s1[j][0], s0[j][0]));
      lds_st<unsigned>(ta + (2 * (j0 + j) + 1) * 128, pk_bf16(s1[j][1], s0[j][1]));
    }
  };
  TSL_DECL
  auto lstage = [&](auto C) {
    constexpr int c = decltype(C)::value;
    TSL(16 + c);
    if (c == 0) wait_vm<12>(); else if (c < 4) wait_vm<4>(); else if (c == 4) wait_vm<6>(); else wait_vm<5>();
    TSL(24 + c);
    bar();
    TSL(2 + c);
    if (c == 3) {
#pragma unroll
      for (int n = 0; n < 2; ++n) {
        const int cl = 16 * lw + 8 * n + (lane >> 3);
        const u32x4 t = lds_ld<u32x4>(lb + L_UT + cl * 128 + (((lane & 7) ^ (cl >> 3)) * 16));
        const unsigned o = WS_UT + (64 * cb + cl) * TROW + 128 * tb + (lane & 7) * 16;
        asm volatile("buffer_store_dwordx4 %0, %1, %2, 0 offen sc1\n\ts_nop 1" ::"v"(t), "v"(o), "s"(rws) : "memory");
      }
    }
    if (c == 4) {
      const rsrc_t rh = mk_rsrc(p.h, D_ * S_ * 2);
      const unsigned oh = ch * 4096 + tA * 16;
      asm volatile("buffer_load_dwordx4 %0, %4, %5, 0 offen nt\n\tbuffer_load_dwordx2 %1, %4, %5, 0 offen offset:16 nt\n\t"
                   "buffer_load_dwordx4 %2, %4, %5, 0 offen offset:1024 nt\n\tbuffer_load_dwordx2 %3, %4, %5, 0 offen offset:1040 nt"
                   : "=&v"(hA), "=&v"(gA), "=&v"(hB), "=&v"(gB) : "v"(oh), "s"(rh) : "memory");
      const unsigned oc = lw < 2 ? WS_CS + (lw * 1792 + jb * 64 + lane) * 8 : 0x80000000u;
      asm volatile("buffer_load_dwordx2 %0, %1, %2, 0 offen sc1" : "=&v"(cg2) : "v"(oc), "s"(rws) : "memory");
    }
    if (c == 5) {
      const unsigned of = WS_FU + (mi >> 5) * 32 * 128 + flag_off(32, lane);
      asm volatile("buffer_load_dword %0, %1, %2, 0 offen sc1" : "=&v"(uf) : "v"(of), "s"(rws) : "memory");
    }
    if (c == 1) {
      asm volatile("" : "+v"(kk[0]), "+v"(kk[1]), "+v"(kk[2]), "+v"(kk[3]), "+v"(kk[4]), "+v"(vv[0]), "+v"(vv[1]));
      upairs(0);
    }
    if (c == 2) upairs(2);
    if constexpr (c + 3 <= 6) stage(IC<c + 3>{});
    if (WDT && c >= 2) wd_touch(c - 2);
  };
  lstage(IC<0>{}); lstage(IC<1>{}); lstage(IC<2>{}); lstage(IC<3>{}); lstage(IC<4>{}); lstage(IC<5>{});
  asm volatile("s_waitcnt vmcnt(1)" : "+v"(cg2)::"memory");
  if (lw < 2) {
    if (!__all(cg2[0] == tag)) {
      for (unsigned it = 0; it < SPIN_CAP; ++it) {
        cg2 = __builtin_amdgcn_raw_buffer_load_b64(rws, WS_CS + (lw * 1792 + jb * 64 + lane) * 8, 0, 16);
        if (__all(cg2[0] == tag)) break;
        __builtin_amdgcn_s_sleep(1);
      }
    }
    lds_st<unsigned>(lb + L_CS + lw * 256 + lane * 4, cg2[1]);
  }
  if (lw == 0 && lane == 0) { lds_st<unsigned>(lb + L_CNT, 0u); }
  bar();
  wait_vm<0>();
  asm volatile("" : "+v"(hA), "+v"(gA), "+v"(hB), "+v"(gB), "+v"(uf));
  if (!flags_ok(uf, tag)) poll_flags(p.ws + WS_FU + (mi >> 5) * 32 * 128, 32, tag, lane);
  u32x4 uA, uB;
  {
    const unsigned ou = WS_UT + ch * TROW + tA * 16;
    asm volatile("buffer_load_dwordx4 %0, %2, %3, 0 offen sc1\n\tbuffer_load_dwordx4 %1, %2, %3, 0 offen offset:1024 sc1" : "=&v"(uA), "=&v"(uB) : "v"(ou), "s"(rws) : "memory");
  }
  const unsigned up = lb + CV_UP + chl * 4608, hs = lb + CV_HS + chl * 16640;
  {
    auto copies = [&](const u32x4& h4, const u32x2& h2, int t4) {
      const unsigned w0 = h4[0], w1 = h4[1], w2 = h4[2], w3 = h4[3], w4 = h2[0], w5 = h2[1];
      const unsigned a0 = __builtin_amdgcn_alignbit(w1, w0, 16), a1 = __builtin_amdgcn_alignbit(w2, w1, 16), a2 = __builtin_amdgcn_alignbit(w3, w2, 16),
                     a3 = __builtin_amdgcn_alignbit(w4, w3, 16), a4 = __builtin_amdgcn_alignbit(w5, w4, 16);
      const unsigned o = hs + 32 + 16 * t4;
      lds_st<u32x4>(o, (u32x4){w0, w1, w2, w3});
      lds_st<u32x4>(o + 4160, (u32x4){a0, a1, a2, a3});
      lds_st<u32x4>(o + 8320, (u32x4){w1, w2, w3, w4});
      lds_st<u32x4>(o + 12480, (u32x4){a1, a2, a3, a4});
    };
    copies(hA, gA, tA); copies(hB, gB, tA + 64);
    if (tA == 0) {
      const unsigned w0 = hA[0], a0 = __builtin_amdgcn_alignbit(hA[1], hA[0], 16);
      const u32x4 z = {0, 0, 0, 0};
#pragma unroll
      for (int sg = 0; sg < 4; ++sg) lds_st<u32x4>(hs + sg * 4160, z);
      lds_st<u32x4>(hs + 16, z);
      lds_st<u32x4>(hs + 4160 + 16, (u32x4){0, 0, 0, w0 << 16});
      lds_st<u32x4>(hs + 8320 + 16, (u32x4){0, 0, 0, w0});
      lds_st<u32x4>(hs + 12480 + 16, (u32x4){0, 0, w0 << 16, a0});
    }
    if (tA < 30) lds_st<u32x4>(up + 16 * tA, (u32x4){0, 0, 0, 0});
  }
  wait_vm<0>();
  asm volatile("" : "+v"(uA), "+v"(uB));
  lds_st<u32x4>(up + 480 + 16 * tA, uA);
  lds_st<u32x4>(up + 480 + 16 * (tA + 64), uB);
  bar();
  const WdStage wd = {rw, g2_vw(lane, s % 7), lb, lw, 1, g2_rot(s)};
  TSL(8);
  wd.group(0); wd.group(1); wd.group(2);
  TSL(9);
  bar();
  TSL(10);
  if (lw == 0) poll_flags(p.ws + WS_FH + x * 28 * 128, 28, tag, lane);
  TSL(11);
  bar();
  wait_vm<0>();
  loader_tail(p, lb, s, lane, wd, lw == 0, tag TAILPASS);
  TSL_DUMP();
}

DI void phase_conv(const Params& p, unsigned lb, int x, int s, int w, int lane, Ctx& cx) {
  const int mi = x * 28 + s, chl = w >> 2, wq = w & 3, ch = 2 * mi + chl, t4 = wq * 64 + lane;
  const rsrc_t rws = mk_rsrc(p.ws, WS_END);
  const unsigned tag = cx.tag;
  const float dv = cx.dv;
  const unsigned up = lb + CV_UP + chl * 4608, hs = lb + CV_HS + chl * 16640;
  bar();
  TS(6);
  const int i = lane & 15, kg = lane >> 4, sg = (i + 1) & 3;
  f32x4 acc[8] = {};
  {
    const unsigned ga = up + 448 - 128 * wq + 32 * i + 16 * kg;
    unsigned f0 = hs + sg * 4160 + 128 * wq + 2 * (25 + i - 8 * kg - sg), f1 = f0 + 8;
    asm volatile("" : "+v"(f1));
    auto publish_h = [&]() {
      wait_vm<0>();
      if (lane == 0) {
        const unsigned old = __hip_atomic_fetch_add((LAS unsigned*)(size_t)(lb + L_CNT), 1u, __ATOMIC_RELAXED, __HIP_MEMORY_SCOPE_WORKGROUP);
        if (old == 7u) post_flag(p.ws + WS_FH + ((4 * (x >> 2) + s / 7) * 28 + 7 * (x & 3) + s % 7) * 128, tag);
      }
    };
    bf16x8 G[8], Fq[4];
    unsigned gb = ga;
    auto rdF = [&](int a, int dres) { Fq[a & 3] = cat(lds_ld<s16x4>(f0 + 64 * dres + 512 * (a & 7)), lds_ld<s16x4>(f1 + 64 * dres + 512 * (a & 7))); };
    rdF(0, 0); rdF(1, 0);
#pragma unroll
    for (int b = 7; b >= 0; --b) G[b] = lds_ld<bf16x8>(gb + 512 * b);
#pragma unroll 1
    for (int rr = 0; rr < 2; ++rr) {
      gb -= 64;
#pragma unroll
      for (int a = 0; a < 8; ++a) {
        if (a == DRA && rr == DRR) publish_h();
#pragma unroll
        for (int b = 7 - a; b >= 0; --b) {
          acc[a + b] = mfma16(Fq[a & 3], G[b], acc[a + b]);
          SB();
          if (b == 7 - a) {
            rdF(a + 2, a + 2 >= 8 ? 1 : 0);
            G[7 - a] = lds_ld<bf16x8>(gb + 512 * (7 - a));
          }
          SB();
        }
      }
      f0 += 64; f1 += 64;
    }
    if (DRR == 2) publish_h();
  }
  TS2(13);
  bar();
  TS(7);
  const unsigned red = lb + CV_RED + chl * 24576;
  f32x4 y0, y1;
  {
    auto scatter = [&](auto WQ) {
      constexpr int q0 = decltype(WQ)::value;
#pragma unroll
      for (int o = 1; o < 4; ++o) {
        constexpr int dummy = 0; (void)dummy;
        const int dst = (q0 + o) & 3;
        lds_st<f32x4>(red + (dst * 3 + (o - 1)) * 2048 + lane * 16, acc[2 * dst]);
        lds_st<f32x4>(red + (dst * 3 + (o - 1)) * 2048 + 1024 + lane * 16, acc[2 * dst + 1]);
      }
      y0 = acc[2 * q0]; y1 = acc[2 * q0 + 1];
    };
    if (wq == 0) scatter(IC<0>{}); else if (wq == 1) scatter(IC<1>{}); else if (wq == 2) scatter(IC<2>{}); else scatter(IC<3>{});
  }
  const unsigned ua = up + 480 + 1024 * wq + 32 * i + 16 * (kg >> 1) + 8 * (1 - (kg & 1));
  const u32x2 ua0 = lds_ld<u32x2>(ua), ua1 = lds_ld<u32x2>(ua + 512);
  bar();
  {
    const int tok = 256 * x + 64 * (s / 7) + 16 * (w >> 1) + (lane & 15);
    const unsigned ox = tok * 896 + (s % 7) * 128 + (4 * (w & 1) + (lane >> 4)) * 16, os = WS_STATS + tok * 16;
    asm volatile("buffer_load_dwordx4 %0, %2, %4, 0 offen\n\tbuffer_load_dwordx4 %1, %3, %5, 0 offen sc1"
                 : "=&v"(cx.xq), "=&v"(cx.sg) : "v"(ox), "v"(os), "s"(mk_rsrc(p.q, S_ * D_ * 2)), "s"(rws) : "memory");
  }
  h_first(p, x, s, w, lane, cx);
#pragma unroll
  for (int o = 0; o < 3; ++o) {
    y0 += lds_ld<f32x4>(red + (wq * 3 + o) * 2048 + lane * 16);
    y1 += lds_ld<f32x4>(red + (wq * 3 + o) * 2048 + 1024 + lane * 16);
  }
  y0[0] = rbf(y0[0]) + rbf(dv * bf_hi(ua0[1])); y0[1] = rbf(y0[1]) + rbf(dv * bf_lo(ua0[1])); y0[2] = rbf(y0[2]) + rbf(dv * bf_hi(ua0[0])); y0[3] = rbf(y0[3]) + rbf(dv * bf_lo(ua0[0]));
  y1[0] = rbf(y1[0]) + rbf(dv * bf_hi(ua1[1])); y1[1] = rbf(y1[1]) + rbf(dv * bf_lo(ua1[1])); y1[2] = rbf(y1[2]) + rbf(dv * bf_hi(ua1[0])); y1[3] = rbf(y1[3]) + rbf(dv * bf_lo(ua1[0]));
  const unsigned d0 = pk_bf16(y0[0], y0[1]), d1 = pk_bf16(y0[2], y0[3]), e0 = pk_bf16(y1[0], y1[1]), e1 = pk_bf16(y1[2], y1[3]);
  const auto s0 = __builtin_amdgcn_permlane16_swap(d0, e0, false, false);
  const auto s1 = __builtin_amdgcn_permlane16_swap(d1, e1, false, false);
  const u32x4 o = {s0[0], s1[0], s0[1], s1[1]};
  const unsigned oy = WS_YT + ch * TROW + 512 * (2 * wq + (kg & 1)) + 32 * i + 8 * (kg & 2);
  asm volatile("buffer_store_dwordx4 %0, %1, %2, 0 offen sc1\n\ts_nop 1" ::"v"(o), "v"(oy), "s"(rws) : "memory");
}

DI void phase_gemm2(const Params& p, unsigned lb, int x, int s, int w, int lane, Ctx& cx) {
  const int i2 = s / 7, db = s % 7, th = w >> 2, kq = w & 3, rot = g2_rot(s);
  const unsigned tag = cx.tag;
  const rsrc_t rws = mk_rsrc(p.ws, WS_END);
  const rsrc_t rq = mk_rsrc(p.q, S_ * D_ * 2), ro = mk_rsrc(p.out, S_ * D_ * 2);
  const int tk = 16 * (w >> 1) + (lane & 15), ck = 4 * (w & 1) + (lane >> 4), tok = 256 * x + 64 * i2 + tk;
  bar();
  TS(10);
  const unsigned hb = WS_H + ((8 * x + 2 * i2 + th) * 112 + 28 * kq) * 1024;
  const unsigned vh = lane * 16;
  u32x4 (&hr)[HD] = cx.hr;
  const int hh = lane >> 5, blk = (lane >> 4) & 1, q = (lane & 15) >> 2, pp = lane & 3;
  unsigned aa[2];
#pragma unroll
  for (int dt = 0; dt < 2; ++dt) aa[dt] = lb + kq * 8192 + (4 * hh + q) * 128 + 64 * (dt ^ ((q >> 1) & 1)) + 32 * blk + 8 * pp;
  f32x16 acc[2] = {};
  bf16x8 fa[2][2];
  static_assert(HD == 4, "one group of four k-steps per loop iteration");
  auto rd1 = [&](int j, int dt) { fa[j & 1][dt] = cat(lds_tr(aa[dt] + (j & 3) * 2048), lds_tr(aa[dt] + (j & 3) * 2048 + 1024)); };
  auto step = [&](int j, bool ahead) {
    SB();
    const bf16x8 b = __builtin_bit_cast(bf16x8, hr[j]);
    acc[0] = mfma32(fa[j & 1][0], b, acc[0]);
    SB();
    if (ahead) rd1(j + 1, 0);
    SB();
    acc[1] = mfma32(fa[j & 1][1], b, acc[1]);
    SB();
    if (ahead) rd1(j + 1, 1);
    SB();
  };
  u32x4 yv; int yok;
  rd1(0, 0); rd1(0, 1);
  int st = rot + 4; st -= st >= 28 ? 28 : 0;
#pragma unroll 1
  for (int g = 0; g < 6; ++g) {
#pragma unroll
    for (int j = 0; j < 4; ++j) {
      if (j == 3) {
        const unsigned d = (g & 3) == 3 ? (unsigned)-98304 : 32768u;
        aa[0] += d; aa[1] += d;
        bar();
        TS2(24 + g);
      }
      asm volatile("s_waitcnt vmcnt(3)" : "+v"(hr[j])::"memory");
      step(j, true);
      asm volatile("buffer_load_dwordx4 %0, %1, %2, %3 offen" HPOL : "=v"(hr[j]) : "v"(vh), "s"(rws), "s"(hb + st * 1024) : "memory");
      st = st == 27 ? 0 : st + 1;
      if (j == 3 && g == 2 && threadIdx.x == 0) post_flag(p.ws + WS_FY + (x * 28 + s) * 128, tag);
      SB();
    }
  }
  yok = __builtin_amdgcn_readfirstlane(lds_ld<unsigned>(lb + 131072));
  {
    const unsigned oy = yok ? WS_YT + (64 * db + (threadIdx.x >> 3)) * TROW + (256 * x + 64 * i2) * 2 + (threadIdx.x & 7) * 16 : 0x80000000u;
    asm volatile("buffer_load_dwordx4 %0, %1, %2, 0 offen sc1" : "=&v"(yv) : "v"(oy), "s"(rws) : "memory");
  }
  asm volatile("s_waitcnt vmcnt(4)" : "+v"(hr[0])::"memory"); step(0, true);
  asm volatile("s_waitcnt vmcnt(3)" : "+v"(hr[1])::"memory"); step(1, true);
  asm volatile("s_waitcnt vmcnt(2)" : "+v"(hr[2])::"memory"); step(2, true);
  asm volatile("s_waitcnt vmcnt(1)" : "+v"(hr[3])::"memory"); step(3, false);
  u32x4 xq = cx.xq, sg = cx.sg;
  TS(11);
  const unsigned pb = lb, yb = lb + 135168;
  {
    const int row = 32 * th + (lane & 31);
#pragma unroll
    for (int dt = 0; dt < 2; ++dt)
#pragma unroll
      for (int G = 0; G < 4; ++G) {
        const f32x4 t = {acc[dt][4 * G], acc[dt][4 * G + 1], acc[dt][4 * G + 2], acc[dt][4 * G + 3]};
        lds_st<f32x4>(pb + kq * 16384 + row * 256 + (((8 * dt + 2 * G + hh) ^ (row & 15)) * 16), t);
      }
  }
  wait_vm<0>();
  asm volatile("" : "+v"(xq), "+v"(sg), "+v"(yv));
  if (yok) lds_st<u32x4>(yb + (threadIdx.x >> 3) * 144 + (threadIdx.x & 7) * 16, yv);
  float mean = __uint_as_float(sg[1]), rstd = __uint_as_float(sg[3]);
  if (!__all(sg[0] == tag && sg[2] == tag)) stats_wait(rws, WS_STATS + tok * 16, tag, mean, rstd);
  bar();
  if (!yok) {
    yv = __builtin_amdgcn_raw_buffer_load_b128(rws, WS_YT + (64 * db + (threadIdx.x >> 3)) * TROW + (256 * x + 64 * i2) * 2 + (threadIdx.x & 7) * 16, 0, 16);
    lds_st<u32x4>(yb + (threadIdx.x >> 3) * 144 + (threadIdx.x & 7) * 16, yv);
    bar();
  }
  f32x4 o0 = {}, o1 = {};
#pragma unroll
  for (int k4 = 0; k4 < 4; ++k4) {
    o0 += lds_ld<f32x4>(pb + k4 * 16384 + tk * 256 + (((2 * ck) ^ (tk & 15)) * 16));
    o1 += lds_ld<f32x4>(pb + k4 * 16384 + tk * 256 + (((2 * ck + 1) ^ (tk & 15)) * 16));
  }
  const unsigned ya = yb + (8 * ck + q) * 144 + (16 * (w >> 1) + 4 * pp) * 2;
  const s16x4 y0 = lds_tr(ya), y1 = lds_tr(ya + 4 * 144);
  u32x4 ov;
#pragma unroll
  for (int e = 0; e < 8; e += 2) {
    const f32x2 o = e < 4 ? (f32x2){o0[e & 3], o0[(e & 3) + 1]} : (f32x2){o1[e & 3], o1[(e & 3) + 1]};
    const f32x2 xv = {bf_lo(xq[e >> 1]), bf_hi(xq[e >> 1])};
    const unsigned yp = e < 4 ? __builtin_bit_cast(u32x2, y0)[(e & 3) >> 1] : __builtin_bit_cast(u32x2, y1)[(e & 3) >> 1];
    const f32x2 yy = {bf_lo(yp), bf_hi(yp)};
    const f32x2 t = o * (f32x2){-1.4426950409f, -1.4426950409f};
    const f32x2 dn = (f32x2){__builtin_amdgcn_exp2f(t[0]), __builtin_amdgcn_exp2f(t[1])} + (f32x2){1.f, 1.f};
    const f32x2 g = o * (f32x2){__builtin_amdgcn_rcpf(dn[0]), __builtin_amdgcn_rcpf(dn[1])};
    const f32x2 xn = rbf2(rbf2(xv - (f32x2){mean, mean}) * (f32x2){rstd, rstd});
    const f32x2 r2 = __builtin_elementwise_fma(rbf2(xn * yy), g, xv);
    ov[e >> 1] = pk_bf16(r2[0], r2[1]);
  }
  __builtin_amdgcn_raw_buffer_store_b128(ov, ro, tok * 896 + db * 128 + ck * 16, 0, OUTAUX);
  TS(12);
}

__global__ __launch_bounds__(1024, 4) void h3_geglu_kernel(Params p) {
  __shared__ __attribute__((aligned(16))) char smem[LDS_BYTES];
  const unsigned lb = (unsigned)(size_t)(LAS char*)smem;
  const int tid = threadIdx.x, lane = tid & 63, w = __builtin_amdgcn_readfirstlane(tid >> 6);
  const int b = blockIdx.x, x = b & 7, s = b >> 3;
  const char* pc;
  asm volatile("s_getpc_b64 %0" : "=s"(pc));
  TS(0);
  if (s < 28) {
    if (w >= 12) {
      loader_w(p, lb, x, s, w - 12, lane);
    } else if (w >= 8) {
      loader_x(p, lb, x, s, w - 8, lane);
    } else {
      Ctx cx;
      phase_gemm1(p, lb, x, s, w, lane, cx);
      phase_conv(p, lb, x, s, w, lane, cx);
      TS(9);
      phase_gemm2(p, lb, x, s, w, lane, cx);
    }
  } else {
    const int j = b - 224;
    if (CODEW && (j >> 3) == 0 && w < 4) {
      const int ln = w * 64 + lane;
      touch_g(pc + (ln < CODE_LINES ? ln : 0) * 128, lb + 65536);
    }
    if (TLBW && (j >> 3) == 0 && w >= 12) {
      if (w < 15) touch(mk_rsrc(p.ws, WS_END), (unsigned)((w - 12) * 64 + lane) * 65536u, lb + 65536);
      else touch(mk_rsrc(p.out, S_ * D_ * 2), (unsigned)lane * 65536u, lb + 65536);
    }
    if (WARM && w < 14) {
      const int q = j >> 3;
      touch(mk_rsrc((w & 1) ? p.v : p.k, S_ * D_ * 2), (unsigned)((256 * x + 64 * q) * 896 + ((w >> 1) * 64 + lane) * 128), lb + 65536);
    }
    if (j < 28) {
      const unsigned tag = __builtin_amdgcn_readfirstlane(*(const GAS unsigned*)(p.ws + WS_EPOCH) + 1u);
      const int m = w & 1, r0 = (w >> 1) * 8 + (lane >> 3);
      const rsrc_t rm = mk_rsrc(m ? p.wu : p.wg, D_ * F_ * 2);
      u32x4 t[7];
#pragma unroll
      for (int n = 0; n < 7; ++n) t[n] = __builtin_amdgcn_raw_buffer_load_b128(rm, (r0 + 64 * n) * 3584 + j * 128 + (lane & 7) * 16, 0, 0);
      float a[8] = {};
#pragma unroll
      for (int n = 0; n < 7; ++n)
#pragma unroll
        for (int i = 0; i < 4; ++i) { a[2 * i] += bf_lo(t[n][i]); a[2 * i + 1] += bf_hi(t[n][i]); }
      lds_st<f32x4>(lb + (w * 8 + (lane >> 3)) * 256 + (lane & 7) * 32, (f32x4){a[0], a[1], a[2], a[3]});
      lds_st<f32x4>(lb + (w * 8 + (lane >> 3)) * 256 + (lane & 7) * 32 + 16, (f32x4){a[4], a[5], a[6], a[7]});
      bar();
      if (w < 2) {
        float sum = 0.f;
#pragma unroll
        for (int q = 0; q < 64; ++q) sum += lds_ld<float>(lb + (((q >> 3) * 2 + w) * 8 + (q & 7)) * 256 + lane * 4);
        const u32x2 g = {tag, __float_as_uint(sum)};
        __builtin_amdgcn_raw_buffer_store_b64(g, mk_rsrc(p.ws, WS_END), WS_CS + (w * 1792 + j * 64 + lane) * 8, 0, 16);
      }
    }
    if (WARM && w < 14) {
      const int blk = w * 32 + j;
      if (blk < 196) touch(mk_rsrc(p.wd, F_ * D_ * 2), (unsigned)(blk * 64 + lane) * 128u, lb + 65536);
      else touch(mk_rsrc(p.h, D_ * S_ * 2), (unsigned)((blk - 196) * 64 + lane) * 128u, lb + 65536);
    }
    if (b == 224 && w == 15) {
      const unsigned tag = __builtin_amdgcn_readfirstlane(*(const GAS unsigned*)(p.ws + WS_EPOCH) + 1u);
#pragma unroll 1
      for (int n = 0; n < 4; ++n) poll_flags_slow(p.ws + WS_FH + n * 56 * 128, 56, tag, lane);
      if (lane == 0) post_flag(p.ws + WS_EPOCH, tag);
    }
    wait_vm<0>();
  }
  TS(15);
}

extern "C" void kernel_launch(void* const* d_in, const int* in_sizes, int n_in, void* d_out, int out_size, void* d_ws, size_t ws_size, hipStream_t stream) {
  Params p;
  p.q = (const u16*)d_in[0]; p.k = (const u16*)d_in[1]; p.v = (const u16*)d_in[2]; p.sk = (const u16*)d_in[3];
  p.h = (const u16*)d_in[4]; p.dv = (const u16*)d_in[5];
  p.wg = (const u16*)d_in[7]; p.wu = (const u16*)d_in[8]; p.wd = (const u16*)d_in[9];
  p.out = (u16*)d_out; p.ws = (char*)d_ws;
  h3_geglu_kernel<<<256, 1024, 0, stream>>>(p);
}
```

```cpp
#include <hip/hip_runtime.h>
#include <hip/hip_bf16.h>
#include <stdint.h>

typedef unsigned short u16;
typedef __attribute__((ext_vector_type(8))) short bf16x8;
typedef __attribute__((ext_vector_type(4))) short s16x4;
typedef __attribute__((ext_vector_type(4))) float f32x4;
typedef __attribute__((ext_vector_type(2))) float f32x2;
typedef __attribute__((ext_vector_type(16))) float f32x16;
typedef __attribute__((ext_vector_type(4))) unsigned u32x4;
typedef __attribute__((ext_vector_type(2))) unsigned u32x2;
typedef __attribute__((ext_vector_type(2))) __bf16 bf16x2;
typedef __amdgpu_buffer_rsrc_t rsrc_t;
#define HSPOL " sc1"
#define LAS __attribute__((address_space(3)))
#define GAS __attribute__((address_space(1)))
#define DI __device__ __forceinline__

#define OPT_OWN 0
#define KVT 1
#define KVFIRST 0
#define WDLATE 1
#define PAUX 2
#define USTAUX 16
#define HSLEEP 10
#define LPRIO 0
#define WARM 1
#define UPRIO 2
#define KVPOL " nt"
#define WPRIO 0
#define XPRIO 0
#define WDT 0
#define DRA 2
#define DRR 1
#define TLBW 0
#define OUTAUX 0
#define PREW 0
#define CODEW 1
constexpr int CODE_LINES = 224;
#define PFN 17

constexpr int S_ = 2048, D_ = 448, F_ = 1792;
constexpr unsigned WS_EPOCH = 0, WS_FH = 8192, WS_FY = 36864, WS_STATS = 65536, WS_FU = 98304, WS_CS = 2031616;
constexpr unsigned TROW = 4224;
constexpr unsigned WS_UT = 131072, WS_YT = 2097152, WS_H = 4194304, WS_END = WS_H + 7340032;
constexpr unsigned WS_DBG = 16u << 20;
constexpr int LDS_BYTES = 163840;
constexpr unsigned SPIN_CAP = 1u << 14;

struct Params {
  const u16 *q, *k, *v, *sk, *h, *dv, *wg, *wu, *wd;
  u16* out;
  char* ws;
};

DI rsrc_t mk_rsrc(const void* p, unsigned bytes) { return __builtin_amdgcn_make_buffer_rsrc((void*)p, (short)0, (int)bytes, 0x00020000); }
DI void dma16(rsrc_t r, unsigned voff, unsigned soff, unsigned lds) {
  asm volatile("s_mov_b32 m0, %0\n\ts_nop 0\n\tbuffer_load_dwordx4 %1, %2, %3 offen lds" ::"s"(lds), "v"(voff), "s"(r), "s"(soff) : "memory");
}
DI void touch(rsrc_t r, unsigned voff, unsigned lds) {
  asm volatile("s_mov_b32 m0, %0\n\ts_nop 0\n\tbuffer_load_dword %1, %2, 0 offen lds" ::"s"(lds), "v"(voff), "s"(r) : "memory");
}
template <int NT> DI void touch_p(rsrc_t r, unsigned voff, unsigned lds) {
  if (NT) asm volatile("s_mov_b32 m0, %0\n\ts_nop 0\n\tbuffer_load_dword %1, %2, 0 offen nt lds" ::"s"(lds), "v"(voff), "s"(r) : "memory");
  else touch(r, voff, lds);
}
DI void touch_g(const char* a, unsigned lds) {
  asm volatile("s_mov_b32 m0, %0\n\ts_nop 0\n\tglobal_load_lds_dword %1, off" ::"s"(lds), "v"(a) : "memory");
}
template <unsigned LA, unsigned SA>
DI void dma16c(rsrc_t r, unsigned voff, unsigned sbase, unsigned lbase) {
  unsigned t;
  asm volatile("s_add_u32 m0, %2, %5\n\ts_add_u32 %0, %3, %6\n\tbuffer_load_dwordx4 %1, %4, %0 offen lds"
               : "=&s"(t) : "v"(voff), "s"(lbase), "s"(sbase), "s"(r), "i"(LA), "i"(SA) : "memory", "scc");
}
template <int N> DI void wait_vm() { asm volatile("s_waitcnt vmcnt(%0)" ::"i"(N) : "memory"); }
DI void bar() { asm volatile("s_waitcnt lgkmcnt(0)\n\ts_barrier" ::: "memory"); }
DI float bf_lo(unsigned x) { return __uint_as_float(x << 16); }
DI float bf_hi(unsigned x) { return __uint_as_float(x & 0xffff0000u); }
DI unsigned pk_bf16(float lo, float hi) { f32x2 t = {lo, hi}; return __builtin_bit_cast(unsigned, __builtin_convertvector(t, bf16x2)); }
DI bf16x8 lds_b128(const char* p) { return *(const LAS bf16x8*)(p); }
DI s16x4 lds_tr(unsigned a) { return __builtin_amdgcn_ds_read_tr16_b64_v4i16((LAS s16x4*)(size_t)a); }
DI bf16x8 cat(s16x4 a, s16x4 b) { return __builtin_shufflevector(a, b, 0, 1, 2, 3, 4, 5, 6, 7); }
template <class T> DI T lds_ld(unsigned a) { return *(const LAS T*)(size_t)a; }
template <class T> DI void lds_st(unsigned a, T v) { *(LAS T*)(size_t)a = v; }
DI f32x16 mfma32(bf16x8 a, bf16x8 b, f32x16 c) { return __builtin_amdgcn_mfma_f32_32x32x16_bf16(a, b, c, 0, 0, 0); }
DI f32x4 mfma16(bf16x8 a, bf16x8 b, f32x4 c) { return __builtin_amdgcn_mfma_f32_16x16x32_bf16(a, b, c, 0, 0, 0); }
DI unsigned long long rtime() { return __builtin_amdgcn_s_memrealtime(); }
DI float dpp_add(float v, const int ctrl_row_shr) { return v; }
template <int CTRL, int RM> DI float dpp_mov(float v) { return __builtin_bit_cast(float, __builtin_amdgcn_update_dpp(0, __builtin_bit_cast(int, v), CTRL, RM, 0xf, true)); }
DI float wave_sum(float v) {
  v += dpp_mov<0x111, 0xf>(v); v += dpp_mov<0x112, 0xf>(v); v += dpp_mov<0x114, 0xf>(v); v += dpp_mov<0x118, 0xf>(v);
  v += dpp_mov<0x142, 0xa>(v); v += dpp_mov<0x143, 0xc>(v);
  return __builtin_bit_cast(float, __builtin_amdgcn_readlane(__builtin_bit_cast(int, v), 63));
}
#define TS(i)
#define TSL(i)
#define TSL_DECL
#define TSL_DUMP()
#define TS2(i)

DI void poll_flags(const char* base, int n, unsigned tag, int lane) {
  const GAS unsigned* f = (const GAS unsigned*)(base + (lane < n ? lane : 0) * 128);
  for (unsigned it = 0; it < SPIN_CAP; ++it) {
    unsigned x = __hip_atomic_load(f, __ATOMIC_RELAXED, __HIP_MEMORY_SCOPE_AGENT);
    if (__all(x == tag)) break;
    __builtin_amdgcn_s_sleep(2);
  }
}
DI void poll_flags_slow(const char* base, int n, unsigned tag, int lane) {
  const GAS unsigned* f = (const GAS unsigned*)(base + (lane < n ? lane : 0) * 128);
  for (unsigned it = 0; it < SPIN_CAP; ++it) {
    unsigned x = __hip_atomic_load(f, __ATOMIC_RELAXED, __HIP_MEMORY_SCOPE_AGENT);
    if (__all(x == tag)) break;
    __builtin_amdgcn_s_sleep(32);
  }
}
DI void post_flag(char* addr, unsigned tag) { __hip_atomic_store((GAS unsigned*)addr, tag, __ATOMIC_RELAXED, __HIP_MEMORY_SCOPE_AGENT); }

DI bool flags_ok(unsigned v, unsigned tag) { return __all(v == tag); }
DI unsigned flag_off(int n, int lane) { return (lane < n ? lane : 0) * 128; }
#define SB() __builtin_amdgcn_sched_barrier(0)
template <int V> struct IC { static constexpr int value = V; };
DI void asm_pin(unsigned& v) { asm volatile("" : "+v"(v)); }
DI float rbf(float x) { return __uint_as_float(pk_bf16(x, 0.f) << 16); }

DI f32x2 rbf2(f32x2 v) { const unsigned u = pk_bf16(v[0], v[1]); return (f32x2){bf_lo(u), bf_hi(u)}; }
DI u32x4 row_stats(u32x4 d, unsigned tag) {
  float e[8];
#pragma unroll
  for (int i = 0; i < 4; ++i) { e[2 * i] = bf_lo(d[i]); e[2 * i + 1] = bf_hi(d[i]); }
  float s = ((e[0] + e[1]) + (e[2] + e[3])) + ((e[4] + e[5]) + (e[6] + e[7]));
  const float mean = wave_sum(s) * (1.f / D_);
  const float on = (threadIdx.x & 63) < 56 ? 1.f : 0.f;
  float ss = 0.f;
#pragma unroll
  for (int i = 0; i < 8; ++i) { const float t = e[i] - mean; ss = fmaf(t, t, ss); }
  const float var = wave_sum(ss * on) * (1.f / D_);
  const float den = rbf(__builtin_amdgcn_sqrtf(rbf(rbf(var) + 1e-5f)));
  u32x4 g = {tag, __float_as_uint(rbf(mean)), tag, __float_as_uint(__builtin_amdgcn_rcpf(den))};
  return g;
}
DI void stats_wait(rsrc_t rs, unsigned off, unsigned tag, float& mean, float& rstd) {
  for (unsigned it = 0;; ++it) {
    const u32x4 g = __builtin_amdgcn_raw_buffer_load_b128(rs, off, 0, 16);
    mean = __uint_as_float(g[1]); rstd = __uint_as_float(g[3]);
    if (__all(g[0] == tag && g[2] == tag) || it >= SPIN_CAP) break;
    __builtin_amdgcn_s_sleep(1);
  }
}

constexpr unsigned CV_BASE = 98304;
constexpr unsigned CV_UP = CV_BASE;
constexpr unsigned CV_HS = CV_BASE + 9216;
constexpr unsigned CV_RED = CV_HS;
constexpr unsigned L_UT = 147456, L_SK = 155648, L_ST = 159744;
constexpr unsigned L_CS = 157696, L_OK = 158720, L_CNT = 158728, L_TOUCH = 158976;

#define HPOL " sc1"
#define HDEPTH 4
constexpr int HD = HDEPTH;
#define PADP 0
#define PADT 0
#define PAD(pt) do { if (PADP == (pt)) { while (rtime() - cx.t0 < (unsigned long long)PADT) __builtin_amdgcn_s_sleep(1); } } while (0)
struct Ctx {
  unsigned long long t0; unsigned tag; float dv; u32x4 hr[HD], xq, sg;
};
DI void g2_stage(rsrc_t rw, unsigned vw, int kq, int th, int grp, int rot, unsigned lb) {
#pragma unroll
  for (int n = 0; n < 4; ++n) {
    int st = 4 * grp + n + rot; st -= st >= 28 ? 28 : 0;
    dma16(rw, vw, (unsigned)((28 * kq + st) * 16 + 8 * th) * 896u, lb + (grp & 3) * 32768 + kq * 8192 + n * 2048 + th * 1024);
  }
}
DI void g2_piece(rsrc_t rw, unsigned vw, int kq, int th, int step, int rot, unsigned lb) {
  int st = step + rot; st -= st >= 28 ? 28 : 0;
  dma16(rw, vw, (unsigned)((28 * kq + st) * 16 + 8 * th) * 896u, lb + ((step >> 2) & 3) * 32768 + kq * 8192 + (step & 3) * 2048 + th * 1024);
}
DI void h_load(const Params& p, u32x4& dst, unsigned hb, int step, int rot, unsigned vh) {
  int st = step + rot; st -= st >= 28 ? 28 : 0;
  asm volatile("buffer_load_dwordx4 %0, %1, %2, %3 offen" HPOL : "=v"(dst) : "v"(vh), "s"(mk_rsrc(p.ws, WS_END)), "s"(hb + st * 1024) : "memory");
}
DI int g2_rot(int s);
DI void h_first(const Params& p, int x, int s, int w, int lane, Ctx& cx) {
  const unsigned hb = WS_H + ((8 * x + 2 * (s / 7) + (w >> 2)) * 112 + 28 * (w & 3)) * 1024;
#pragma unroll
  for (int n = 0; n < HD; ++n) h_load(p, cx.hr[n], hb, n, g2_rot(s), lane * 16);
}
DI unsigned g2_vw(int lane, int db) { const int L3 = lane >> 3, L7 = lane & 7; return L3 * 896 + db * 128 + 16 * (L7 ^ (((L3 >> 1) & 1) << 2)); }
DI int g2_rot(int s) { const int r = 4 * (s % 7) + 7 * (s / 7); return r >= 28 ? r - 28 : r; }

DI void phase_gemm1(const Params& p, unsigned lb, int x, int s, int w, int lane, Ctx& cx) {
  const int ib = 4 * (x >> 2) + s / 7, jb = 7 * (x & 3) + s % 7;
  const rsrc_t rq = mk_rsrc(p.q, S_ * D_ * 2), rws = mk_rsrc(p.ws, WS_END);
  const int gw = jb * 8 + w;
  unsigned tag;
  {
    u32x4 sr0, sr1; unsigned etag;
    const unsigned o0 = lane < 56 ? (256 * ib + gw) * 896 + 16 * lane : 0x80000000u;
    const unsigned o1 = (lane < 56 && gw < 32) ? (256 * ib + 224 + gw) * 896 + 16 * lane : 0x80000000u;
    const unsigned oz = WS_EPOCH;
    unsigned dvb; const unsigned od = (2 * (x * 28 + s) + (w >> 2)) * 2;
    asm volatile("buffer_load_dword %2, %6, %9, 0 offen\n\tbuffer_load_dwordx4 %0, %4, %8, 0 offen\n\tbuffer_load_dwordx4 %1, %5, %8, 0 offen\n\t"
                 "buffer_load_ushort %3, %7, %10, 0 offen\n\ts_waitcnt vmcnt(0)"
                 : "=&v"(sr0), "=&v"(sr1), "=&v"(etag), "=&v"(dvb) : "v"(o0), "v"(o1), "v"(oz), "v"(od), "s"(rq), "s"(rws), "s"(mk_rsrc(p.dv, D_ * 2)) : "memory");
    cx.dv = bf_lo(dvb);
    tag = __builtin_amdgcn_readfirstlane(etag) + 1u;
    cx.tag = tag;
    const u32x4 g0 = row_stats(sr0, tag), g1 = row_stats(sr1, tag);
    const unsigned p0 = lane == 0 ? WS_STATS + (256 * ib + gw) * 16 : 0x80000000u;
    const unsigned p1 = (lane == 0 && gw < 32) ? WS_STATS + (256 * ib + 224 + gw) * 16 : 0x80000000u;
    asm volatile("buffer_store_dwordx4 %0, %2, %4, 0 offen sc1\n\tbuffer_store_dwordx4 %1, %3, %4, 0 offen sc1\n\ts_nop 1" ::"v"(g0), "v"(g1), "v"(p0), "v"(p1), "s"(rws) : "memory");
  }
  const int tw = w & 3, fh = w >> 2, r = lane & 31, hh = lane >> 5;
  unsigned xa[4], wa[2];
#pragma unroll
  for (int i = 0; i < 4; ++i) xa[i] = lb + (64 * tw + r) * 128 + 16 * ((2 * i + hh) ^ ((r >> 1) & 7));
  {
    const int blk = (lane >> 4) & 1, q = (lane & 15) >> 2, pp = lane & 3, m = pp >> 1, f = (q >> 1) | ((m ^ hh) << 1);
#pragma unroll
    for (int u = 0; u < 2; ++u) wa[u] = lb + 32768 + m * 8192 + (8 * hh + q) * 128 + 32 * ((2 * fh + u) ^ f) + 16 * blk + 8 * (pp & 1);
  }
  f32x16 acc[2][2] = {};
  if (PREW) {
    f32x16 z = {};
    const bf16x8 o = {0x3c00, 0x3c10, 0x3c20, 0x3c30, 0x3c40, 0x3c50, 0x3c60, 0x3c70};
#pragma unroll 1
    for (int i = 0; i < PREW; ++i) { z = mfma32(o, o, z); z = mfma32(o, o, z); z = mfma32(o, o, z); z = mfma32(o, o, z); }
    asm volatile("" ::"v"(z));
  }
  TS(1);
  PAD(1);
  bar();
  TS(2);
  bf16x8 fb[2][2] = {}, fa[2][2] = {};
  auto rdA = [&](int ks, int u) { fa[ks & 1][u] = cat(lds_tr(wa[u] + (ks & 3) * 2048), lds_tr(wa[u] + (ks & 3) * 2048 + 512)); };
  auto rdB = [&](int ks) { fb[ks & 1][0] = lds_ld<bf16x8>(xa[ks & 3]); fb[ks & 1][1] = lds_ld<bf16x8>(xa[ks & 3] + 4096); };
  auto step = [&](int ks, bool ahead) {
    const int pb = ks & 1;
    SB();
    acc[0][0] = mfma32(fa[pb][0], fb[pb][0], acc[0][0]);
    SB();
    if (ahead) rdA(ks + 1, 0);
    SB();
    acc[1][0] = mfma32(fa[pb][0], fb[pb][1], acc[1][0]);
    SB();
    if (ahead) rdB(ks + 1);
    SB();
    acc[0][1] = mfma32(fa[pb][1], fb[pb][0], acc[0][1]);
    SB();
    if (ahead) rdA(ks + 1, 1);
    SB();
    acc[1][1] = mfma32(fa[pb][1], fb[pb][1], acc[1][1]);
    SB();
  };
  rdA(0, 0); rdB(0); rdA(0, 1);
  int sl = 0;
#pragma unroll 1
  for (int c = 0; c < 6; ++c) {
    step(0, true);
    step(1, true);
    step(2, true);
    {
      const unsigned d = __builtin_amdgcn_readfirstlane(sl == 2 ? -98304 : 49152);
      sl = sl == 2 ? 0 : sl + 1;
#pragma unroll
      for (int i = 0; i < 4; ++i) xa[i] += d;
      wa[0] += d; wa[1] += d;
    }
    bar();
    TS2(16 + c);
    step(3, true);
    if (c == 5 && threadIdx.x == 0) post_flag(p.ws + WS_FU + (((x * 28 + s) % 7) * 32 + (x * 28 + s) / 7) * 128, tag);
  }
  step(0, true); step(1, true); step(2, true); step(3, false);
  TS(3);
  const unsigned csb = lb + L_CS;
  PAD(2);
  bar();
  TS(4);
  float mean[2], rstd[2];
#pragma unroll
  for (int tt = 0; tt < 2; ++tt) { const f32x2 t = lds_ld<f32x2>(lb + L_ST + (64 * tw + 32 * tt + r) * 8); mean[tt] = t[0]; rstd[tt] = t[1]; }
  constexpr float C0 = -2.3022081985f, C1 = -0.1029432397f;
#pragma unroll
  for (int u = 0; u < 2; ++u) {
    f32x4 cg[2], cu[2];
#pragma unroll
    for (int b = 0; b < 2; ++b) {
      cg[b] = lds_ld<f32x4>(csb + (16 * (2 * fh + u) + 8 * b + 4 * hh) * 4);
      cu[b] = lds_ld<f32x4>(csb + 256 + (16 * (2 * fh + u) + 8 * b + 4 * hh) * 4);
    }
#pragma unroll
    for (int tt = 0; tt < 2; ++tt) {
      const float rr = rstd[tt], k0s = rr * C0, k1s = C1 * rr * rr * rr, ir2s = __builtin_amdgcn_rcpf(rr * rr);
      const f32x2 nm = {-mean[tt], -mean[tt]}, k0 = {k0s, k0s}, k1 = {k1s, k1s}, ir2 = {ir2s, ir2s};
      u32x4 o;
#pragma unroll
      for (int b = 0; b < 2; ++b)
#pragma unroll
        for (int e = 0; e < 4; e += 2) {
          const f32x2 ag = {acc[tt][u][8 * b + e], acc[tt][u][8 * b + e + 1]}, au = {acc[tt][u][8 * b + 4 + e], acc[tt][u][8 * b + 5 + e]};
          const f32x2 c2g = {cg[b][e], cg[b][e + 1]}, c2u = {cu[b][e], cu[b][e + 1]};
          const f32x2 a = __builtin_elementwise_fma(nm, c2g, ag), bb = __builtin_elementwise_fma(nm, c2u, au);
          const f32x2 z = a * __builtin_elementwise_fma(a * a, k1, k0);
          const f32x2 ex = {__builtin_amdgcn_exp2f(z[0]), __builtin_amdgcn_exp2f(z[1])};
          const f32x2 dn = __builtin_elementwise_fma(ex, ir2, ir2);
          const f32x2 rc = {__builtin_amdgcn_rcpf(dn[0]), __builtin_amdgcn_rcpf(dn[1])};
          const f32x2 hv = (a * bb) * rc;
          o[2 * b + (e >> 1)] = pk_bf16(hv[0], hv[1]);
        }
      const unsigned oh = WS_H + ((8 * ib + 2 * tw + tt) * 112 + 4 * jb + 2 * fh + u) * 1024 + lane * 16;
      asm volatile("buffer_store_dwordx4 %0, %1, %2, 0 offen" HSPOL "\n\ts_nop 1" ::"v"(o), "v"(oh), "s"(rws) : "memory");
    }
  }
  TS(5);
}

struct WdStage {
  rsrc_t rw; unsigned vw, lb; int kq, th, rot;
  DI void group(int g) const {
#pragma unroll
    for (int n = 0; n < 4; ++n) g2_piece(rw, vw, kq, th, 4 * g + n, rot, lb);
  }
};
#define TAILARG
#define TAILPASS
DI void loader_tail(const Params& p, unsigned lb, int s, int lane, const WdStage& wd, bool poller, unsigned tag TAILARG) {
  TSL(12);
  bar();
  TSL(13);
  wd.group(3);
  TSL(14);
  bar();
  wd.group(4);
  bar();
  wd.group(5);
  TSL(22);
  wait_vm<8>();
  TSL(23);
  bar();
  wd.group(6);
  wait_vm<8>();
  TSL(30);
  bar();
  wait_vm<4>();
  bar();
  if (poller) {
    const unsigned yf = __hip_atomic_load((const GAS unsigned*)(p.ws + WS_FY + (s % 7) * 32 * 128 + flag_off(32, lane)), __ATOMIC_RELAXED, __HIP_MEMORY_SCOPE_AGENT);
    const unsigned ok = flags_ok(yf, tag) ? 1u : 0u;
    if (lane == 0) lds_st<unsigned>(lb + 131072, ok);
  }
  wait_vm<0>();
  bar();
  const int yok = __builtin_amdgcn_readfirstlane(lds_ld<unsigned>(lb + 131072));
  if (poller && !yok) poll_flags(p.ws + WS_FY + (s % 7) * 32 * 128, 32, tag, lane);
  bar();
  if (!yok) bar();
}

DI void loader_x(const Params& p, unsigned lb, int x, int s, int lw, int lane) {
  const int ib = 4 * (x >> 2) + s / 7;
  const rsrc_t rq = mk_rsrc(p.q, S_ * D_ * 2);
  const int L3 = lane >> 3, L7 = lane & 7;
  const unsigned vx0 = (64 * lw + L3) * 896 + 16 * (L7 ^ ((lane >> 4) & 3));
  const unsigned vx1 = (64 * lw + 8 + L3) * 896 + 16 * (L7 ^ (((lane >> 4) & 3) | 4));
  const unsigned sx = ib * 256 * 896, lx = lb + lw * 8192;
  auto piece = [&](auto C, auto I) {
    constexpr int c = decltype(C)::value, i = decltype(I)::value;
    dma16c<(c % 3) * 49152 + i * 1024, c * 128 + (i >> 1) * 16 * 896>(rq, (i & 1) ? vx1 : vx0, sx, lx);
  };
  auto stage = [&](auto C) {
    piece(C, IC<0>{}); piece(C, IC<1>{}); piece(C, IC<2>{}); piece(C, IC<3>{}); piece(C, IC<4>{}); piece(C, IC<5>{}); piece(C, IC<6>{}); piece(C, IC<7>{});
  };
  if (XPRIO) __builtin_amdgcn_s_setprio(XPRIO);
  const rsrc_t rws = mk_rsrc(p.ws, WS_END);
  unsigned etag;
  TSL_DECL
  TS(8);
  { const unsigned oz = WS_EPOCH; asm volatile("buffer_load_dword %0, %1, %2, 0 offen" : "=&v"(etag) : "v"(oz), "s"(rws) : "memory"); }
  stage(IC<0>{});
  stage(IC<1>{});
  wait_vm<8>();
  asm volatile("" : "+v"(etag));
  const unsigned tag = __builtin_amdgcn_readfirstlane(etag) + 1u;
  TS(1);
  bar();
  stage(IC<2>{});
  auto lstage = [&](auto C) {
    constexpr int c = decltype(C)::value;
    TSL(16 + c);
    if (c < 5) wait_vm<8>(); else wait_vm<0>();
    TSL(24 + c);
    bar();
    TSL(2 + c);
    if constexpr (c + 3 <= 6) stage(IC<c + 3>{});
    if (c == 4) {
      const unsigned os = WS_STATS + (256 * ib + 64 * lw + lane) * 16;
      float mean, rstd;
      stats_wait(rws, os, tag, mean, rstd);
      lds_st<f32x2>(lb + L_ST + (64 * lw + lane) * 8, (f32x2){mean, rstd});
    }
  };
  lstage(IC<0>{}); lstage(IC<1>{}); lstage(IC<2>{}); lstage(IC<3>{}); lstage(IC<4>{}); lstage(IC<5>{});
  bar();
  bar();
  const WdStage wd = {mk_rsrc(p.wd, F_ * D_ * 2), g2_vw(lane, s % 7), lb, lw, 0, g2_rot(s)};
  TSL(8);
  wd.group(0); wd.group(1); wd.group(2);
  TSL(9);
  bar();
  bar();
  wait_vm<0>();
  loader_tail(p, lb, s, lane, wd, false, 0u TAILPASS);
  TSL_DUMP();
}

DI void loader_w(const Params& p, unsigned lb, int x, int s, int lw, int lane) {
  const int jb = 7 * (x & 3) + s % 7;
  const rsrc_t rg = mk_rsrc(p.wg, D_ * F_ * 2), ru = mk_rsrc(p.wu, D_ * F_ * 2);
  const rsrc_t rw = mk_rsrc(p.wd, F_ * D_ * 2), rws = mk_rsrc(p.ws, WS_END);
  const int L3 = lane >> 3, L7 = lane & 7;
  const unsigned vwA = (16 * lw + L3) * 3584 + jb * 128 + 32 * ((L7 >> 1) ^ ((lane >> 4) & 1)) + 16 * (L7 & 1);
  const unsigned vwB = (16 * lw + L3) * 3584 + jb * 128 + 32 * ((L7 >> 1) ^ (((lane >> 4) & 1) | 2)) + 16 * (L7 & 1);
  const unsigned lx = lb + 32768 + lw * 2048;
  auto stage = [&](auto C) {
    constexpr int c = decltype(C)::value;
    constexpr int cm = c;
    dma16c<(c % 3) * 49152, cm * 64 * 3584>(rg, vwA, 0, lx);
    dma16c<(c % 3) * 49152 + 1024, cm * 64 * 3584 + 8 * 3584>(rg, vwB, 0, lx);
    dma16c<(c % 3) * 49152 + 8192, cm * 64 * 3584>(ru, vwB, 0, lx);
    dma16c<(c % 3) * 49152 + 8192 + 1024, cm * 64 * 3584 + 8 * 3584>(ru, vwA, 0, lx);
  };
  if (WPRIO) __builtin_amdgcn_s_setprio(WPRIO);
  unsigned etag;
  { const unsigned oz = WS_EPOCH; asm volatile("buffer_load_dword %0, %1, %2, 0 offen" : "=&v"(etag) : "v"(oz), "s"(rws) : "memory"); }
  stage(IC<0>{});
  stage(IC<1>{});
  wait_vm<4>();
  asm volatile("" : "+v"(etag));
  const unsigned tag = __builtin_amdgcn_readfirstlane(etag) + 1u;
  bar();
  const int mi = x * 28 + s, cb = mi % 7, tb = mi / 7, ucg = lane >> 3, urp = lane & 7, ut0 = 64 * tb + 16 * lw + 2 * urp;
  u32x4 kk[5], vv[2];
  stage(IC<2>{});
  {
    const rsrc_t rk = mk_rsrc(p.k, S_ * D_ * 2), rv = mk_rsrc(p.v, S_ * D_ * 2);
    unsigned ok[5];
#pragma unroll
    for (int n = 0; n < 5; ++n) ok[n] = ut0 - 3 + n >= 0 ? (unsigned)((ut0 - 3 + n) * 896 + cb * 128 + ucg * 16) : 0x80000000u;
    const unsigned ov = ut0 * 896 + cb * 128 + ucg * 16;
    asm volatile("buffer_load_dwordx4 %0, %7, %13, 0 offen" KVPOL "\n\tbuffer_load_dwordx4 %1, %8, %13, 0 offen" KVPOL "\n\tbuffer_load_dwordx4 %2, %9, %13, 0 offen" KVPOL "\n\t"
                 "buffer_load_dwordx4 %3, %10, %13, 0 offen" KVPOL "\n\tbuffer_load_dwordx4 %4, %11, %13, 0 offen" KVPOL "\n\t"
                 "buffer_load_dwordx4 %5, %12, %14, 0 offen" KVPOL "\n\tbuffer_load_dwordx4 %6, %12, %14, 0 offen offset:896" KVPOL
                 : "=&v"(kk[0]), "=&v"(kk[1]), "=&v"(kk[2]), "=&v"(kk[3]), "=&v"(kk[4]), "=&v"(vv[0]), "=&v"(vv[1])
                 : "v"(ok[0]), "v"(ok[1]), "v"(ok[2]), "v"(ok[3]), "v"(ok[4]), "v"(ov), "s"(rk), "s"(rv) : "memory");
    dma16(mk_rsrc(p.sk, D_ * 8), lane < 32 ? cb * 512 + lane * 16 : 0x80000000u, 0, lb + L_SK);
  }
  auto wd_touch = [&](int i) {
    const int sh = 2 * i + (lw & 1);
    if ((lw >> 1) == (i & 1) && sh < 7) touch(rw, (unsigned)((s * 7 + sh) * 64 + lane) * 128u, lb + L_TOUCH);
  };
  const int chl = lw >> 1, ch = 2 * mi + chl, tA = (lw & 1) * 128 + lane;
  u32x4 hA, hB; u32x2 gA, gB, cg2; unsigned uf = 0;
  auto upairs = [&](const int j0) {
    const int tl = 16 * lw + 2 * urp, e = (tl & ~7) + 6 - (tl & 7);
    const unsigned ta = lb + L_UT + 8 * ucg * 128 + (((e >> 3) ^ ucg) * 16) + 2 * (e & 7);
    u32x4 wq[2];
#pragma unroll
    for (int j = 0; j < 2; ++j) wq[j] = lds_ld<u32x4>(lb + L_SK + ucg * 64 + (j0 + j) * 16);
    f32x2 kf[2][5], vf[2][2], wt[2][4], s0[2], s1[2];
#pragma unroll
    for (int j = 0; j < 2; ++j) {
#pragma unroll
      for (int n = 0; n < 5; ++n) kf[j][n] = (f32x2){bf_lo(kk[n][j0 + j]), bf_hi(kk[n][j0 + j])};
#pragma unroll
      for (int n = 0; n < 2; ++n) vf[j][n] = (f32x2){bf_lo(vv[n][j0 + j]), bf_hi(vv[n][j0 + j])};
    }
#pragma unroll
    for (int j = 0; j < 2; ++j) {
      wt[j][0] = (f32x2){bf_lo(wq[j][0]), bf_lo(wq[j][2])}; wt[j][1] = (f32x2){bf_hi(wq[j][0]), bf_hi(wq[j][2])};
      wt[j][2] = (f32x2){bf_lo(wq[j][1]), bf_lo(wq[j][3])}; wt[j][3] = (f32x2){bf_hi(wq[j][1]), bf_hi(wq[j][3])};
    }
    SB();
#pragma unroll
    for (int j = 0; j < 2; ++j) { s0[j] = kf[j][0] * wt[j][3]; s1[j] = kf[j][1] * wt[j][3]; }
    SB();
#pragma unroll
    for (int t = 2; t >= 0; --t) {
#pragma unroll
      for (int j = 0; j < 2; ++j) { s0[j] = __builtin_elementwise_fma(kf[j][3 - t], wt[j][t], s0[j]); s1[j] = __builtin_elementwise_fma(kf[j][4 - t], wt[j][t], s1[j]); }
      SB();
    }
#pragma unroll
    for (int j = 0; j < 2; ++j) { s0[j] *= vf[j][0]; s1[j] *= vf[j][1]; }
    SB();
#pragma unroll
    for (int j = 0; j < 2; ++j) {
      lds_st<unsigned>(ta + (2 * (j0 + j)) * 128, pk_bf16(s1[j][0], s0[j][0]));
      lds_st<unsigned>(ta + (2 * (j0 + j) + 1) * 128, pk_bf16(s1[j][1], s0[j][1]));
    }
  };
  TSL_DECL
  auto lstage = [&](auto C) {
    constexpr int c = decltype(C)::value;
    TSL(16 + c);
    if (c == 0) wait_vm<12>(); else if (c < 4) wait_vm<4>(); else if (c == 4) wait_vm<6>(); else wait_vm<5>();
    TSL(24 + c);
    bar();
    TSL(2 + c);
    if (c == 3) {
#pragma unroll
      for (int n = 0; n < 2; ++n) {
        const int cl = 16 * lw + 8 * n + (lane >> 3);
        const u32x4 t = lds_ld<u32x4>(lb + L_UT + cl * 128 + (((lane & 7) ^ (cl >> 3)) * 16));
        const unsigned o = WS_UT + (64 * cb + cl) * TROW + 128 * tb + (lane & 7) * 16;
        asm volatile("buffer_store_dwordx4 %0, %1, %2, 0 offen sc1\n\ts_nop 1" ::"v"(t), "v"(o), "s"(rws) : "memory");
      }
    }
    if (c == 4) {
      const rsrc_t rh = mk_rsrc(p.h, D_ * S_ * 2);
      const unsigned oh = ch * 4096 + tA * 16;
      asm volatile("buffer_load_dwordx4 %0, %4, %5, 0 offen nt\n\tbuffer_load_dwordx2 %1, %4, %5, 0 offen offset:16 nt\n\t"
                   "buffer_load_dwordx4 %2, %4, %5, 0 offen offset:1024 nt\n\tbuffer_load_dwordx2 %3, %4, %5, 0 offen offset:1040 nt"
                   : "=&v"(hA), "=&v"(gA), "=&v"(hB), "=&v"(gB) : "v"(oh), "s"(rh) : "memory");
      const unsigned oc = lw < 2 ? WS_CS + (lw * 1792 + jb * 64 + lane) * 8 : 0x80000000u;
      asm volatile("buffer_load_dwordx2 %0, %1, %2, 0 offen sc1" : "=&v"(cg2) : "v"(oc), "s"(rws) : "memory");
    }
    if (c == 5) {
      const unsigned of = WS_FU + (mi >> 5) * 32 * 128 + flag_off(32, lane);
      asm volatile("buffer_load_dword %0, %1, %2, 0 offen sc1" : "=&v"(uf) : "v"(of), "s"(rws) : "memory");
    }
    if (c == 1) {
      asm volatile("" : "+v"(kk[0]), "+v"(kk[1]), "+v"(kk[2]), "+v"(kk[3]), "+v"(kk[4]), "+v"(vv[0]), "+v"(vv[1]));
      upairs(0);
    }
    if (c == 2) upairs(2);
    if constexpr (c + 3 <= 6) stage(IC<c + 3>{});
    if (WDT && c >= 2) wd_touch(c - 2);
  };
  lstage(IC<0>{}); lstage(IC<1>{}); lstage(IC<2>{}); lstage(IC<3>{}); lstage(IC<4>{}); lstage(IC<5>{});
  asm volatile("s_waitcnt vmcnt(1)" : "+v"(cg2)::"memory");
  if (lw < 2) {
    if (!__all(cg2[0] == tag)) {
      for (unsigned it = 0; it < SPIN_CAP; ++it) {
        cg2 = __builtin_amdgcn_raw_buffer_load_b64(rws, WS_CS + (lw * 1792 + jb * 64 + lane) * 8, 0, 16);
        if (__all(cg2[0] == tag)) break;
        __builtin_amdgcn_s_sleep(1);
      }
    }
    lds_st<unsigned>(lb + L_CS + lw * 256 + lane * 4, cg2[1]);
  }
  if (lw == 0 && lane == 0) { lds_st<unsigned>(lb + L_CNT, 0u); }
  bar();
  wait_vm<0>();
  asm volatile("" : "+v"(hA), "+v"(gA), "+v"(hB), "+v"(gB), "+v"(uf));
  if (!flags_ok(uf, tag)) poll_flags(p.ws + WS_FU + (mi >> 5) * 32 * 128, 32, tag, lane);
  u32x4 uA, uB;
  {
    const unsigned ou = WS_UT + ch * TROW + tA * 16;
    asm volatile("buffer_load_dwordx4 %0, %2, %3, 0 offen sc1\n\tbuffer_load_dwordx4 %1, %2, %3, 0 offen offset:1024 sc1" : "=&v"(uA), "=&v"(uB) : "v"(ou), "s"(rws) : "memory");
  }
  const unsigned up = lb + CV_UP + chl * 4608, hs = lb + CV_HS + chl * 16640;
  {
    auto copies = [&](const u32x4& h4, const u32x2& h2, int t4) {
      const unsigned w0 = h4[0], w1 = h4[1], w2 = h4[2], w3 = h4[3], w4 = h2[0], w5 = h2[1];
      const unsigned a0 = __builtin_amdgcn_alignbit(w1, w0, 16), a1 = __builtin_amdgcn_alignbit(w2, w1, 16), a2 = __builtin_amdgcn_alignbit(w3, w2, 16),
                     a3 = __builtin_amdgcn_alignbit(w4, w3, 16), a4 = __builtin_amdgcn_alignbit(w5, w4, 16);
      const unsigned o = hs + 32 + 16 * t4;
      lds_st<u32x4>(o, (u32x4){w0, w1, w2, w3});
      lds_st<u32x4>(o + 4160, (u32x4){a0, a1, a2, a3});
      lds_st<u32x4>(o + 8320, (u32x4){w1, w2, w3, w4});
      lds_st<u32x4>(o + 12480, (u32x4){a1, a2, a3, a4});
    };
    copies(hA, gA, tA); copies(hB, gB, tA + 64);
    if (tA == 0) {
      const unsigned w0 = hA[0], a0 = __builtin_amdgcn_alignbit(hA[1], hA[0], 16);
      const u32x4 z = {0, 0, 0, 0};
#pragma unroll
      for (int sg = 0; sg < 4; ++sg) lds_st<u32x4>(hs + sg * 4160, z);
      lds_st<u32x4>(hs + 16, z);
      lds_st<u32x4>(hs + 4160 + 16, (u32x4){0, 0, 0, w0 << 16});
      lds_st<u32x4>(hs + 8320 + 16, (u32x4){0, 0, 0, w0});
      lds_st<u32x4>(hs + 12480 + 16, (u32x4){0, 0, w0 << 16, a0});
    }
    if (tA < 30) lds_st<u32x4>(up + 16 * tA, (u32x4){0, 0, 0, 0});
  }
  wait_vm<0>();
  asm volatile("" : "+v"(uA), "+v"(uB));
  lds_st<u32x4>(up + 480 + 16 * tA, uA);
  lds_st<u32x4>(up + 480 + 16 * (tA + 64), uB);
  bar();
  const WdStage wd = {rw, g2_vw(lane, s % 7), lb, lw, 1, g2_rot(s)};
  TSL(8);
  wd.group(0); wd.group(1); wd.group(2);
  TSL(9);
  bar();
  TSL(10);
  if (lw == 0) poll_flags(p.ws + WS_FH + x * 28 * 128, 28, tag, lane);
  TSL(11);
  bar();
  wait_vm<0>();
  loader_tail(p, lb, s, lane, wd, lw == 0, tag TAILPASS);
  TSL_DUMP();
}

DI void phase_conv(const Params& p, unsigned lb, int x, int s, int w, int lane, Ctx& cx) {
  const int mi = x * 28 + s, chl = w >> 2, wq = w & 3, ch = 2 * mi + chl, t4 = wq * 64 + lane;
  const rsrc_t rws = mk_rsrc(p.ws, WS_END);
  const unsigned tag = cx.tag;
  const float dv = cx.dv;
  const unsigned up = lb + CV_UP + chl * 4608, hs = lb + CV_HS + chl * 16640;
  bar();
  TS(6);
  const int i = lane & 15, kg = lane >> 4, sg = (i + 1) & 3;
  f32x4 acc[8] = {};
  {
    const unsigned ga = up + 448 - 128 * wq + 32 * i + 16 * kg;
    unsigned f0 = hs + sg * 4160 + 128 * wq + 2 * (25 + i - 8 * kg - sg), f1 = f0 + 8;
    asm volatile("" : "+v"(f1));
    auto publish_h = [&]() {
      wait_vm<0>();
      if (lane == 0) {
        const unsigned old = __hip_atomic_fetch_add((LAS unsigned*)(size_t)(lb + L_CNT), 1u, __ATOMIC_RELAXED, __HIP_MEMORY_SCOPE_WORKGROUP);
        if (old == 7u) post_flag(p.ws + WS_FH + ((4 * (x >> 2) + s / 7) * 28 + 7 * (x & 3) + s % 7) * 128, tag);
      }
    };
    bf16x8 G[8], Fq[4];
    unsigned gb = ga;
    auto rdF = [&](int a, int dres) { Fq[a & 3] = cat(lds_ld<s16x4>(f0 + 64 * dres + 512 * (a & 7)), lds_ld<s16x4>(f1 + 64 * dres + 512 * (a & 7))); };
    rdF(0, 0); rdF(1, 0);
#pragma unroll
    for (int b = 7; b >= 0; --b) G[b] = lds_ld<bf16x8>(gb + 512 * b);
#pragma unroll 1
    for (int rr = 0; rr < 2; ++rr) {
      gb -= 64;
#pragma unroll
      for (int a = 0; a < 8; ++a) {
        if (a == DRA && rr == DRR) publish_h();
#pragma unroll
        for (int b = 7 - a; b >= 0; --b) {
          acc[a + b] = mfma16(Fq[a & 3], G[b], acc[a + b]);
          SB();
          if (b == 7 - a) {
            rdF(a + 2, a + 2 >= 8 ? 1 : 0);
            G[7 - a] = lds_ld<bf16x8>(gb + 512 * (7 - a));
          }
          SB();
        }
      }
      f0 += 64; f1 += 64;
    }
    if (DRR == 2) publish_h();
  }
  TS2(13);
  bar();
  TS(7);
  const unsigned red = lb + CV_RED + chl * 24576;
  f32x4 y0, y1;
  {
    auto scatter = [&](auto WQ) {
      constexpr int q0 = decltype(WQ)::value;
#pragma unroll
      for (int o = 1; o < 4; ++o) {
        constexpr int dummy = 0; (void)dummy;
        const int dst = (q0 + o) & 3;
        lds_st<f32x4>(red + (dst * 3 + (o - 1)) * 2048 + lane * 16, acc[2 * dst]);
        lds_st<f32x4>(red + (dst * 3 + (o - 1)) * 2048 + 1024 + lane * 16, acc[2 * dst + 1]);
      }
      y0 = acc[2 * q0]; y1 = acc[2 * q0 + 1];
    };
    if (wq == 0) scatter(IC<0>{}); else if (wq == 1) scatter(IC<1>{}); else if (wq == 2) scatter(IC<2>{}); else scatter(IC<3>{});
  }
  const unsigned ua = up + 480 + 1024 * wq + 32 * i + 16 * (kg >> 1) + 8 * (1 - (kg & 1));
  const u32x2 ua0 = lds_ld<u32x2>(ua), ua1 = lds_ld<u32x2>(ua + 512);
  bar();
  {
    const int tok = 256 * x + 64 * (s / 7) + 16 * (w >> 1) + (lane & 15);
    const unsigned ox = tok * 896 + (s % 7) * 128 + (4 * (w & 1) + (lane >> 4)) * 16, os = WS_STATS + tok * 16;
    asm volatile("buffer_load_dwordx4 %0, %2, %4, 0 offen\n\tbuffer_load_dwordx4 %1, %3, %5, 0 offen sc1"
                 : "=&v"(cx.xq), "=&v"(cx.sg) : "v"(ox), "v"(os), "s"(mk_rsrc(p.q, S_ * D_ * 2)), "s"(rws) : "memory");
  }
  h_first(p, x, s, w, lane, cx);
#pragma unroll
  for (int o = 0; o < 3; ++o) {
    y0 += lds_ld<f32x4>(red + (wq * 3 + o) * 2048 + lane * 16);
    y1 += lds_ld<f32x4>(red + (wq * 3 + o) * 2048 + 1024 + lane * 16);
  }
  y0[0] = rbf(y0[0]) + rbf(dv * bf_hi(ua0[1])); y0[1] = rbf(y0[1]) + rbf(dv * bf_lo(ua0[1])); y0[2] = rbf(y0[2]) + rbf(dv * bf_hi(ua0[0])); y0[3] = rbf(y0[3]) + rbf(dv * bf_lo(ua0[0]));
  y1[0] = rbf(y1[0]) + rbf(dv * bf_hi(ua1[1])); y1[1] = rbf(y1[1]) + rbf(dv * bf_lo(ua1[1])); y1[2] = rbf(y1[2]) + rbf(dv * bf_hi(ua1[0])); y1[3] = rbf(y1[3]) + rbf(dv * bf_lo(ua1[0]));
  const unsigned d0 = pk_bf16(y0[0], y0[1]), d1 = pk_bf16(y0[2], y0[3]), e0 = pk_bf16(y1[0], y1[1]), e1 = pk_bf16(y1[2], y1[3]);
  const auto s0 = __builtin_amdgcn_permlane16_swap(d0, e0, false, false);
  const auto s1 = __builtin_amdgcn_permlane16_swap(d1, e1, false, false);
  const u32x4 o = {s0[0], s1[0], s0[1], s1[1]};
  const unsigned oy = WS_YT + ch * TROW + 512 * (2 * wq + (kg & 1)) + 32 * i + 8 * (kg & 2);
  asm volatile("buffer_store_dwordx4 %0, %1, %2, 0 offen sc1\n\ts_nop 1" ::"v"(o), "v"(oy), "s"(rws) : "memory");
}

DI void phase_gemm2(const Params& p, unsigned lb, int x, int s, int w, int lane, Ctx& cx) {
  const int i2 = s / 7, db = s % 7, th = w >> 2, kq = w & 3, rot = g2_rot(s);
  const unsigned tag = cx.tag;
  const rsrc_t rws = mk_rsrc(p.ws, WS_END);
  const rsrc_t rq = mk_rsrc(p.q, S_ * D_ * 2), ro = mk_rsrc(p.out, S_ * D_ * 2);
  const int tk = 16 * (w >> 1) + (lane & 15), ck = 4 * (w & 1) + (lane >> 4), tok = 256 * x + 64 * i2 + tk;
  PAD(3);
  bar();
  TS(10);
  const unsigned hb = WS_H + ((8 * x + 2 * i2 + th) * 112 + 28 * kq) * 1024;
  const unsigned vh = lane * 16;
  u32x4 (&hr)[HD] = cx.hr;
  const int hh = lane >> 5, blk = (lane >> 4) & 1, q = (lane & 15) >> 2, pp = lane & 3;
  unsigned aa[2];
#pragma unroll
  for (int dt = 0; dt < 2; ++dt) aa[dt] = lb + kq * 8192 + (4 * hh + q) * 128 + 64 * (dt ^ ((q >> 1) & 1)) + 32 * blk + 8 * pp;
  f32x16 acc[2] = {};
  bf16x8 fa[2][2];
  static_assert(HD == 4, "one group of four k-steps per loop iteration");
  auto rd1 = [&](int j, int dt) { fa[j & 1][dt] = cat(lds_tr(aa[dt] + (j & 3) * 2048), lds_tr(aa[dt] + (j & 3) * 2048 + 1024)); };
  auto step = [&](int j, bool ahead) {
    SB();
    const bf16x8 b = __builtin_bit_cast(bf16x8, hr[j]);
    acc[0] = mfma32(fa[j & 1][0], b, acc[0]);
    SB();
    if (ahead) rd1(j + 1, 0);
    SB();
    acc[1] = mfma32(fa[j & 1][1], b, acc[1]);
    SB();
    if (ahead) rd1(j + 1, 1);
    SB();
  };
  u32x4 yv; int yok;
  rd1(0, 0); rd1(0, 1);
  int st = rot + 4; st -= st >= 28 ? 28 : 0;
#pragma unroll 1
  for (int g = 0; g < 6; ++g) {
#pragma unroll
    for (int j = 0; j < 4; ++j) {
      if (j == 3) {
        const unsigned d = (g & 3) == 3 ? (unsigned)-98304 : 32768u;
        aa[0] += d; aa[1] += d;
        bar();
        TS2(24 + g);
      }
      asm volatile("s_waitcnt vmcnt(3)" : "+v"(hr[j])::"memory");
      step(j, true);
      asm volatile("buffer_load_dwordx4 %0, %1, %2, %3 offen" HPOL : "=v"(hr[j]) : "v"(vh), "s"(rws), "s"(hb + st * 1024) : "memory");
      st = st == 27 ? 0 : st + 1;
      if (j == 3 && g == 2 && threadIdx.x == 0) post_flag(p.ws + WS_FY + (x * 28 + s) * 128, tag);
      SB();
    }
  }
  yok = __builtin_amdgcn_readfirstlane(lds_ld<unsigned>(lb + 131072));
  {
    const unsigned oy = yok ? WS_YT + (64 * db + (threadIdx.x >> 3)) * TROW + (256 * x + 64 * i2) * 2 + (threadIdx.x & 7) * 16 : 0x80000000u;
    asm volatile("buffer_load_dwordx4 %0, %1, %2, 0 offen sc1" : "=&v"(yv) : "v"(oy), "s"(rws) : "memory");
  }
  asm volatile("s_waitcnt vmcnt(4)" : "+v"(hr[0])::"memory"); step(0, true);
  asm volatile("s_waitcnt vmcnt(3)" : "+v"(hr[1])::"memory"); step(1, true);
  asm volatile("s_waitcnt vmcnt(2)" : "+v"(hr[2])::"memory"); step(2, true);
  asm volatile("s_waitcnt vmcnt(1)" : "+v"(hr[3])::"memory"); step(3, false);
  u32x4 xq = cx.xq, sg = cx.sg;
  TS(11);
  const unsigned pb = lb, yb = lb + 135168;
  {
    const int row = 32 * th + (lane & 31);
#pragma unroll
    for (int dt = 0; dt < 2; ++dt)
#pragma unroll
      for (int G = 0; G < 4; ++G) {
        const f32x4 t = {acc[dt][4 * G], acc[dt][4 * G + 1], acc[dt][4 * G + 2], acc[dt][4 * G + 3]};
        lds_st<f32x4>(pb + kq * 16384 + row * 256 + (((8 * dt + 2 * G + hh) ^ (row & 15)) * 16), t);
      }
  }
  wait_vm<0>();
  asm volatile("" : "+v"(xq), "+v"(sg), "+v"(yv));
  if (yok) lds_st<u32x4>(yb + (threadIdx.x >> 3) * 144 + (threadIdx.x & 7) * 16, yv);
  float mean = __uint_as_float(sg[1]), rstd = __uint_as_float(sg[3]);
  if (!__all(sg[0] == tag && sg[2] == tag)) stats_wait(rws, WS_STATS + tok * 16, tag, mean, rstd);
  bar();
  if (!yok) {
    yv = __builtin_amdgcn_raw_buffer_load_b128(rws, WS_YT + (64 * db + (threadIdx.x >> 3)) * TROW + (256 * x + 64 * i2) * 2 + (threadIdx.x & 7) * 16, 0, 16);
    lds_st<u32x4>(yb + (threadIdx.x >> 3) * 144 + (threadIdx.x & 7) * 16, yv);
    bar();
  }
  f32x4 o0 = {}, o1 = {};
#pragma unroll
  for (int k4 = 0; k4 < 4; ++k4) {
    o0 += lds_ld<f32x4>(pb + k4 * 16384 + tk * 256 + (((2 * ck) ^ (tk & 15)) * 16));
    o1 += lds_ld<f32x4>(pb + k4 * 16384 + tk * 256 + (((2 * ck + 1) ^ (tk & 15)) * 16));
  }
  const unsigned ya = yb + (8 * ck + q) * 144 + (16 * (w >> 1) + 4 * pp) * 2;
  const s16x4 y0 = lds_tr(ya), y1 = lds_tr(ya + 4 * 144);
  u32x4 ov;
#pragma unroll
  for (int e = 0; e < 8; e += 2) {
    const f32x2 o = e < 4 ? (f32x2){o0[e & 3], o0[(e & 3) + 1]} : (f32x2){o1[e & 3], o1[(e & 3) + 1]};
    const f32x2 xv = {bf_lo(xq[e >> 1]), bf_hi(xq[e >> 1])};
    const unsigned yp = e < 4 ? __builtin_bit_cast(u32x2, y0)[(e & 3) >> 1] : __builtin_bit_cast(u32x2, y1)[(e & 3) >> 1];
    const f32x2 yy = {bf_lo(yp), bf_hi(yp)};
    const f32x2 t = o * (f32x2){-1.4426950409f, -1.4426950409f};
    const f32x2 dn = (f32x2){__builtin_amdgcn_exp2f(t[0]), __builtin_amdgcn_exp2f(t[1])} + (f32x2){1.f, 1.f};
    const f32x2 g = o * (f32x2){__builtin_amdgcn_rcpf(dn[0]), __builtin_amdgcn_rcpf(dn[1])};
    const f32x2 xn = rbf2(rbf2(xv - (f32x2){mean, mean}) * (f32x2){rstd, rstd});
    const f32x2 r2 = __builtin_elementwise_fma(rbf2(xn * yy), g, xv);
    ov[e >> 1] = pk_bf16(r2[0], r2[1]);
  }
  __builtin_amdgcn_raw_buffer_store_b128(ov, ro, tok * 896 + db * 128 + ck * 16, 0, OUTAUX);
  TS(12);
}

__global__ __launch_bounds__(1024, 4) void h3_geglu_kernel(Params p) {
  __shared__ __attribute__((aligned(16))) char smem[LDS_BYTES];
  const unsigned lb = (unsigned)(size_t)(LAS char*)smem;
  const int tid = threadIdx.x, lane = tid & 63, w = __builtin_amdgcn_readfirstlane(tid >> 6);
  const int b = blockIdx.x, x = b & 7, s = b >> 3;
  const unsigned long long t_start = rtime();
  const char* pc;
  asm volatile("s_getpc_b64 %0" : "=s"(pc));
  if (CODEW == 2 && w == 15 && s < 28) touch_g(pc + (8 * s + (lane & 7)) * 128, lb + L_TOUCH);
  TS(0);
  if (s < 28) {
    if (w >= 12) {
      loader_w(p, lb, x, s, w - 12, lane);
    } else if (w >= 8) {
      loader_x(p, lb, x, s, w - 8, lane);
    } else {
      Ctx cx; cx.t0 = t_start;
      phase_gemm1(p, lb, x, s, w, lane, cx);
      phase_conv(p, lb, x, s, w, lane, cx);
      TS(9);
      phase_gemm2(p, lb, x, s, w, lane, cx);
    }
  } else {
    const int j = b - 224;
    if (CODEW == 1 && (j >> 3) == 0 && w < 4) {
      const int ln = w * 64 + lane;
      touch_g(pc + (ln < CODE_LINES ? ln : 0) * 128, lb + 65536);
    }
    if (TLBW && (j >> 3) == 0 && w >= 12) {
      if (w < 15) touch(mk_rsrc(p.ws, WS_END), (unsigned)((w - 12) * 64 + lane) * 65536u, lb + 65536);
      else touch(mk_rsrc(p.out, S_ * D_ * 2), (unsigned)lane * 65536u, lb + 65536);
    }
    if (WARM && w < 14) {
      const int q = j >> 3;
      touch(mk_rsrc((w & 1) ? p.v : p.k, S_ * D_ * 2), (unsigned)((256 * x + 64 * q) * 896 + ((w >> 1) * 64 + lane) * 128), lb + 65536);
    }
    if (j < 28) {
      const unsigned tag = __builtin_amdgcn_readfirstlane(*(const GAS unsigned*)(p.ws + WS_EPOCH) + 1u);
      const int m = w & 1, r0 = (w >> 1) * 8 + (lane >> 3);
      const rsrc_t rm = mk_rsrc(m ? p.wu : p.wg, D_ * F_ * 2);
      u32x4 t[7];
#pragma unroll
      for (int n = 0; n < 7; ++n) t[n] = __builtin_amdgcn_raw_buffer_load_b128(rm, (r0 + 64 * n) * 3584 + j * 128 + (lane & 7) * 16, 0, 0);
      float a[8] = {};
#pragma unroll
      for (int n = 0; n < 7; ++n)
#pragma unroll
        for (int i = 0; i < 4; ++i) { a[2 * i] += bf_lo(t[n][i]); a[2 * i + 1] += bf_hi(t[n][i]); }
      lds_st<f32x4>(lb + (w * 8 + (lane >> 3)) * 256 + (lane & 7) * 32, (f32x4){a[0], a[1], a[2], a[3]});
      lds_st<f32x4>(lb + (w * 8 + (lane >> 3)) * 256 + (lane & 7) * 32 + 16, (f32x4){a[4], a[5], a[6], a[7]});
      bar();
      if (w < 2) {
        float sum = 0.f;
#pragma unroll
        for (int q = 0; q < 64; ++q) sum += lds_ld<float>(lb + (((q >> 3) * 2 + w) * 8 + (q & 7)) * 256 + lane * 4);
        const u32x2 g = {tag, __float_as_uint(sum)};
        __builtin_amdgcn_raw_buffer_store_b64(g, mk_rsrc(p.ws, WS_END), WS_CS + (w * 1792 + j * 64 + lane) * 8, 0, 16);
      }
    }
    if (WARM && w < 14) {
      const int blk = w * 32 + j;
      if (blk < 196) touch(mk_rsrc(p.wd, F_ * D_ * 2), (unsigned)(blk * 64 + lane) * 128u, lb + 65536);
      else touch(mk_rsrc(p.h, D_ * S_ * 2), (unsigned)((blk - 196) * 64 + lane) * 128u, lb + 65536);
    }
    if (b == 224 && w == 15) {
      const unsigned tag = __builtin_amdgcn_readfirstlane(*(const GAS unsigned*)(p.ws + WS_EPOCH) + 1u);
#pragma unroll 1
      for (int n = 0; n < 4; ++n) poll_flags_slow(p.ws + WS_FH + n * 56 * 128, 56, tag, lane);
      if (lane == 0) post_flag(p.ws + WS_EPOCH, tag);
    }
    wait_vm<0>();
  }
  TS(15);
}

extern "C" void kernel_launch(void* const* d_in, const int* in_sizes, int n_in, void* d_out, int out_size, void* d_ws, size_t ws_size, hipStream_t stream) {
  Params p;
  p.q = (const u16*)d_in[0]; p.k = (const u16*)d_in[1]; p.v = (const u16*)d_in[2]; p.sk = (const u16*)d_in[3];
  p.h = (const u16*)d_in[4]; p.dv = (const u16*)d_in[5];
  p.wg = (const u16*)d_in[7]; p.wu = (const u16*)d_in[8]; p.wd = (const u16*)d_in[9];
  p.out = (u16*)d_out; p.ws = (char*)d_ws;
  h3_geglu_kernel<<<256, 1024, 0, stream>>>(p);
}
```

```cpp
#include <hip/hip_runtime.h>
#include <hip/hip_bf16.h>
#include <stdint.h>

typedef unsigned short u16;
typedef __attribute__((ext_vector_type(8))) short bf16x8;
typedef __attribute__((ext_vector_type(4))) short s16x4;
typedef __attribute__((ext_vector_type(4))) float f32x4;
typedef __attribute__((ext_vector_type(2))) float f32x2;
typedef __attribute__((ext_vector_type(16))) float f32x16;
typedef __attribute__((ext_vector_type(4))) unsigned u32x4;
typedef __attribute__((ext_vector_type(2))) unsigned u32x2;
typedef __attribute__((ext_vector_type(2))) __bf16 bf16x2;
typedef __amdgpu_buffer_rsrc_t rsrc_t;
#define HSPOL " sc1"
#define LAS __attribute__((address_space(3)))
#define GAS __attribute__((address_space(1)))
#define DI __device__ __forceinline__

#define OPT_OWN 0
#define KVT 1
#define KVFIRST 0
#define WDLATE 1
#define PAUX 2
#define USTAUX 16
#define HSLEEP 14
#define LPRIO 0
#define WARM 1
#define UPRIO 2
#define KVPOL " nt"
#define WPRIO 0
#define XPRIO 0
#define WDT 0
#define DRT 3
#define DRA 2
#define DRR 1
#define TLBW 0
#define OUTAUX 0
#define PREW 0
#define CODEW 1
constexpr int CODE_LINES = 224;
#define PFN 17

constexpr int S_ = 2048, D_ = 448, F_ = 1792;
constexpr unsigned WS_EPOCH = 0, WS_FH = 8192, WS_FY = 36864, WS_STATS = 65536, WS_FU = 98304, WS_CS = 2031616;
constexpr unsigned TROW = 4224;
constexpr unsigned WS_UT = 131072, WS_YT = 2097152, WS_H = 4194304, WS_END = WS_H + 7340032;
constexpr unsigned WS_DBG = 16u << 20;
constexpr int LDS_BYTES = 163840;
constexpr unsigned SPIN_CAP = 1u << 14;

struct Params {
  const u16 *q, *k, *v, *sk, *h, *dv, *wg, *wu, *wd;
  u16* out;
  char* ws;
};

DI rsrc_t mk_rsrc(const void* p, unsigned bytes) { return __builtin_amdgcn_make_buffer_rsrc((void*)p, (short)0, (int)bytes, 0x00020000); }
DI void dma16(rsrc_t r, unsigned voff, unsigned soff, unsigned lds) {
  asm volatile("s_mov_b32 m0, %0\n\ts_nop 0\n\tbuffer_load_dwordx4 %1, %2, %3 offen lds" ::"s"(lds), "v"(voff), "s"(r), "s"(soff) : "memory");
}
DI void touch(rsrc_t r, unsigned voff, unsigned lds) {
  asm volatile("s_mov_b32 m0, %0\n\ts_nop 0\n\tbuffer_load_dword %1, %2, 0 offen lds" ::"s"(lds), "v"(voff), "s"(r) : "memory");
}
template <int NT> DI void touch_p(rsrc_t r, unsigned voff, unsigned lds) {
  if (NT) asm volatile("s_mov_b32 m0, %0\n\ts_nop 0\n\tbuffer_load_dword %1, %2, 0 offen nt lds" ::"s"(lds), "v"(voff), "s"(r) : "memory");
  else touch(r, voff, lds);
}
DI void touch_g(const char* a, unsigned lds) {
  asm volatile("s_mov_b32 m0, %0\n\ts_nop 0\n\tglobal_load_lds_dword %1, off" ::"s"(lds), "v"(a) : "memory");
}
template <unsigned LA, unsigned SA>
DI void dma16c(rsrc_t r, unsigned voff, unsigned sbase, unsigned lbase) {
  unsigned t;
  asm volatile("s_add_u32 m0, %2, %5\n\ts_add_u32 %0, %3, %6\n\tbuffer_load_dwordx4 %1, %4, %0 offen lds"
               : "=&s"(t) : "v"(voff), "s"(lbase), "s"(sbase), "s"(r), "i"(LA), "i"(SA) : "memory", "scc");
}
template <int N> DI void wait_vm() { asm volatile("s_waitcnt vmcnt(%0)" ::"i"(N) : "memory"); }
DI void bar() { asm volatile("s_waitcnt lgkmcnt(0)\n\ts_barrier" ::: "memory"); }
DI float bf_lo(unsigned x) { return __uint_as_float(x << 16); }
DI float bf_hi(unsigned x) { return __uint_as_float(x & 0xffff0000u); }
DI unsigned pk_bf16(float lo, float hi) { f32x2 t = {lo, hi}; return __builtin_bit_cast(unsigned, __builtin_convertvector(t, bf16x2)); }
DI bf16x8 lds_b128(const char* p) { return *(const LAS bf16x8*)(p); }
DI s16x4 lds_tr(unsigned a) { return __builtin_amdgcn_ds_read_tr16_b64_v4i16((LAS s16x4*)(size_t)a); }
DI bf16x8 cat(s16x4 a, s16x4 b) { return __builtin_shufflevector(a, b, 0, 1, 2, 3, 4, 5, 6, 7); }
template <class T> DI T lds_ld(unsigned a) { return *(const LAS T*)(size_t)a; }
template <class T> DI void lds_st(unsigned a, T v) { *(LAS T*)(size_t)a = v; }
DI f32x16 mfma32(bf16x8 a, bf16x8 b, f32x16 c) { return __builtin_amdgcn_mfma_f32_32x32x16_bf16(a, b, c, 0, 0, 0); }
DI f32x4 mfma16(bf16x8 a, bf16x8 b, f32x4 c) { return __builtin_amdgcn_mfma_f32_16x16x32_bf16(a, b, c, 0, 0, 0); }
DI unsigned long long rtime() { return __builtin_amdgcn_s_memrealtime(); }
DI float dpp_add(float v, const int ctrl_row_shr) { return v; }
template <int CTRL, int RM> DI float dpp_mov(float v) { return __builtin_bit_cast(float, __builtin_amdgcn_update_dpp(0, __builtin_bit_cast(int, v), CTRL, RM, 0xf, true)); }
DI float wave_sum(float v) {
  v += dpp_mov<0x111, 0xf>(v); v += dpp_mov<0x112, 0xf>(v); v += dpp_mov<0x114, 0xf>(v); v += dpp_mov<0x118, 0xf>(v);
  v += dpp_mov<0x142, 0xa>(v); v += dpp_mov<0x143, 0xc>(v);
  return __builtin_bit_cast(float, __builtin_amdgcn_readlane(__builtin_bit_cast(int, v), 63));
}
#define TS(i)
#define TSL(i)
#define TSL_DECL
#define TSL_DUMP()
#define TS2(i)

DI void poll_flags(const char* base, int n, unsigned tag, int lane) {
  const GAS unsigned* f = (const GAS unsigned*)(base + (lane < n ? lane : 0) * 128);
  for (unsigned it = 0; it < SPIN_CAP; ++it) {
    unsigned x = __hip_atomic_load(f, __ATOMIC_RELAXED, __HIP_MEMORY_SCOPE_AGENT);
    if (__all(x == tag)) break;
    __builtin_amdgcn_s_sleep(2);
  }
}
DI void poll_flags_slow(const char* base, int n, unsigned tag, int lane) {
  const GAS unsigned* f = (const GAS unsigned*)(base + (lane < n ? lane : 0) * 128);
  for (unsigned it = 0; it < SPIN_CAP; ++it) {
    unsigned x = __hip_atomic_load(f, __ATOMIC_RELAXED, __HIP_MEMORY_SCOPE_AGENT);
    if (__all(x == tag)) break;
    __builtin_amdgcn_s_sleep(32);
  }
}
DI void post_flag(char* addr, unsigned tag) { __hip_atomic_store((GAS unsigned*)addr, tag, __ATOMIC_RELAXED, __HIP_MEMORY_SCOPE_AGENT); }

DI bool flags_ok(unsigned v, unsigned tag) { return __all(v == tag); }
DI unsigned flag_off(int n, int lane) { return (lane < n ? lane : 0) * 128; }
#define SB() __builtin_amdgcn_sched_barrier(0)
template <int V> struct IC { static constexpr int value = V; };
DI void asm_pin(unsigned& v) { asm volatile("" : "+v"(v)); }
DI float rbf(float x) { return __uint_as_float(pk_bf16(x, 0.f) << 16); }

DI f32x2 rbf2(f32x2 v) { const unsigned u = pk_bf16(v[0], v[1]); return (f32x2){bf_lo(u), bf_hi(u)}; }
DI u32x4 row_stats(u32x4 d, unsigned tag) {
  float e[8];
#pragma unroll
  for (int i = 0; i < 4; ++i) { e[2 * i] = bf_lo(d[i]); e[2 * i + 1] = bf_hi(d[i]); }
  float s = ((e[0] + e[1]) + (e[2] + e[3])) + ((e[4] + e[5]) + (e[6] + e[7]));
  const float mean = wave_sum(s) * (1.f / D_);
  const float on = (threadIdx.x & 63) < 56 ? 1.f : 0.f;
  float ss = 0.f;
#pragma unroll
  for (int i = 0; i < 8; ++i) { const float t = e[i] - mean; ss = fmaf(t, t, ss); }
  const float var = wave_sum(ss * on) * (1.f / D_);
  const float den = rbf(__builtin_amdgcn_sqrtf(rbf(rbf(var) + 1e-5f)));
  u32x4 g = {tag, __float_as_uint(rbf(mean)), tag, __float_as_uint(__builtin_amdgcn_rcpf(den))};
  return g;
}
DI void stats_wait(rsrc_t rs, unsigned off, unsigned tag, float& mean, float& rstd) {
  for (unsigned it = 0;; ++it) {
    const u32x4 g = __builtin_amdgcn_raw_buffer_load_b128(rs, off, 0, 16);
    mean = __uint_as_float(g[1]); rstd = __uint_as_float(g[3]);
    if (__all(g[0] == tag && g[2] == tag) || it >= SPIN_CAP) break;
    __builtin_amdgcn_s_sleep(1);
  }
}

constexpr unsigned CV_BASE = 98304;
constexpr unsigned CV_UP = CV_BASE;
constexpr unsigned CV_HS = CV_BASE + 9216;
constexpr unsigned CV_RED = CV_HS;
constexpr unsigned L_UT = 147456, L_SK = 155648, L_ST = 159744;
constexpr unsigned L_CS = 157696, L_OK = 158720, L_CNT = 158728, L_TOUCH = 158976;

#define HPOL " sc1"
#define HDEPTH 8
constexpr int HD = HDEPTH;
#define PADP 0
#define PADT 0
#define PAD(pt) do { if (PADP == (pt)) { while (rtime() - cx.t0 < (unsigned long long)PADT) __builtin_amdgcn_s_sleep(1); } } while (0)
struct Ctx {
  unsigned long long t0; unsigned tag; float dv; u32x4 hr[HD], xq, sg;
};
DI void g2_stage(rsrc_t rw, unsigned vw, int kq, int th, int grp, int rot, unsigned lb) {
#pragma unroll
  for (int n = 0; n < 4; ++n) {
    int st = 4 * grp + n + rot; st -= st >= 28 ? 28 : 0;
    dma16(rw, vw, (unsigned)((28 * kq + st) * 16 + 8 * th) * 896u, lb + (grp & 3) * 32768 + kq * 8192 + n * 2048 + th * 1024);
  }
}
DI void g2_piece(rsrc_t rw, unsigned vw, int kq, int th, int step, int rot, unsigned lb) {
  int st = step + rot; st -= st >= 28 ? 28 : 0;
  dma16(rw, vw, (unsigned)((28 * kq + st) * 16 + 8 * th) * 896u, lb + ((step >> 2) & 3) * 32768 + kq * 8192 + (step & 3) * 2048 + th * 1024);
}
DI void h_load(const Params& p, u32x4& dst, unsigned hb, int step, int rot, unsigned vh) {
  int st = step + rot; st -= st >= 28 ? 28 : 0;
  asm volatile("buffer_load_dwordx4 %0, %1, %2, %3 offen" HPOL : "=v"(dst) : "v"(vh), "s"(mk_rsrc(p.ws, WS_END)), "s"(hb + st * 1024) : "memory");
}
DI int g2_rot(int s);
DI void h_first(const Params& p, int x, int s, int w, int lane, Ctx& cx) {
  const unsigned hb = WS_H + ((8 * x + 2 * (s / 7) + (w >> 2)) * 112 + 28 * (w & 3)) * 1024;
#pragma unroll
  for (int n = 0; n < HD; ++n) h_load(p, cx.hr[n], hb, n, g2_rot(s), lane * 16);
}
DI unsigned g2_vw(int lane, int db) { const int L3 = lane >> 3, L7 = lane & 7; return L3 * 896 + db * 128 + 16 * (L7 ^ (((L3 >> 1) & 1) << 2)); }
DI int g2_rot(int s) { const int r = 4 * (s % 7) + 7 * (s / 7); return r >= 28 ? r - 28 : r; }

DI void phase_gemm1(const Params& p, unsigned lb, int x, int s, int w, int lane, Ctx& cx) {
  const int ib = 4 * (x >> 2) + s / 7, jb = 7 * (x & 3) + s % 7;
  const rsrc_t rq = mk_rsrc(p.q, S_ * D_ * 2), rws = mk_rsrc(p.ws, WS_END);
  const int gw = jb * 8 + w;
  unsigned tag;
  {
    u32x4 sr0, sr1; unsigned etag;
    const unsigned o0 = lane < 56 ? (256 * ib + gw) * 896 + 16 * lane : 0x80000000u;
    const unsigned o1 = (lane < 56 && gw < 32) ? (256 * ib + 224 + gw) * 896 + 16 * lane : 0x80000000u;
    const unsigned oz = WS_EPOCH;
    unsigned dvb; const unsigned od = (2 * (x * 28 + s) + (w >> 2)) * 2;
    asm volatile("buffer_load_dword %2, %6, %9, 0 offen\n\tbuffer_load_dwordx4 %0, %4, %8, 0 offen\n\tbuffer_load_dwordx4 %1, %5, %8, 0 offen\n\t"
                 "buffer_load_ushort %3, %7, %10, 0 offen\n\ts_waitcnt vmcnt(0)"
                 : "=&v"(sr0), "=&v"(sr1), "=&v"(etag), "=&v"(dvb) : "v"(o0), "v"(o1), "v"(oz), "v"(od), "s"(rq), "s"(rws), "s"(mk_rsrc(p.dv, D_ * 2)) : "memory");
    cx.dv = bf_lo(dvb);
    tag = __builtin_amdgcn_readfirstlane(etag) + 1u;
    cx.tag = tag;
    const u32x4 g0 = row_stats(sr0, tag), g1 = row_stats(sr1, tag);
    const unsigned p0 = lane == 0 ? WS_STATS + (256 * ib + gw) * 16 : 0x80000000u;
    const unsigned p1 = (lane == 0 && gw < 32) ? WS_STATS + (256 * ib + 224 + gw) * 16 : 0x80000000u;
    asm volatile("buffer_store_dwordx4 %0, %2, %4, 0 offen sc1\n\tbuffer_store_dwordx4 %1, %3, %4, 0 offen sc1\n\ts_nop 1" ::"v"(g0), "v"(g1), "v"(p0), "v"(p1), "s"(rws) : "memory");
  }
  const int tw = w & 3, fh = w >> 2, r = lane & 31, hh = lane >> 5;
  unsigned xa[4], wa[2];
#pragma unroll
  for (int i = 0; i < 4; ++i) xa[i] = lb + (64 * tw + r) * 128 + 16 * ((2 * i + hh) ^ ((r >> 1) & 7));
  {
    const int blk = (lane >> 4) & 1, q = (lane & 15) >> 2, pp = lane & 3, m = pp >> 1, f = (q >> 1) | ((m ^ hh) << 1);
#pragma unroll
    for (int u = 0; u < 2; ++u) wa[u] = lb + 32768 + m * 8192 + (8 * hh + q) * 128 + 32 * ((2 * fh + u) ^ f) + 16 * blk + 8 * (pp & 1);
  }
  f32x16 acc[2][2] = {};
  if (PREW) {
    f32x16 z = {};
    const bf16x8 o = {0x3c00, 0x3c10, 0x3c20, 0x3c30, 0x3c40, 0x3c50, 0x3c60, 0x3c70};
#pragma unroll 1
    for (int i = 0; i < PREW; ++i) { z = mfma32(o, o, z); z = mfma32(o, o, z); z = mfma32(o, o, z); z = mfma32(o, o, z); }
    asm volatile("" ::"v"(z));
  }
  TS(1);
  PAD(1);
  bar();
  TS(2);
  bf16x8 fb[2][2] = {}, fa[2][2] = {};
  auto rdA = [&](int ks, int u) { fa[ks & 1][u] = cat(lds_tr(wa[u] + (ks & 3) * 2048), lds_tr(wa[u] + (ks & 3) * 2048 + 512)); };
  auto rdB = [&](int ks) { fb[ks & 1][0] = lds_ld<bf16x8>(xa[ks & 3]); fb[ks & 1][1] = lds_ld<bf16x8>(xa[ks & 3] + 4096); };
  auto step = [&](int ks, bool ahead) {
    const int pb = ks & 1;
    SB();
    acc[0][0] = mfma32(fa[pb][0], fb[pb][0], acc[0][0]);
    SB();
    if (ahead) rdA(ks + 1, 0);
    SB();
    acc[1][0] = mfma32(fa[pb][0], fb[pb][1], acc[1][0]);
    SB();
    if (ahead) rdB(ks + 1);
    SB();
    acc[0][1] = mfma32(fa[pb][1], fb[pb][0], acc[0][1]);
    SB();
    if (ahead) rdA(ks + 1, 1);
    SB();
    acc[1][1] = mfma32(fa[pb][1], fb[pb][1], acc[1][1]);
    SB();
  };
  rdA(0, 0); rdB(0); rdA(0, 1);
  int sl = 0;
#pragma unroll 1
  for (int c = 0; c < 6; ++c) {
    step(0, true);
    step(1, true);
    step(2, true);
    {
      const unsigned d = __builtin_amdgcn_readfirstlane(sl == 2 ? -98304 : 49152);
      sl = sl == 2 ? 0 : sl + 1;
#pragma unroll
      for (int i = 0; i < 4; ++i) xa[i] += d;
      wa[0] += d; wa[1] += d;
    }
    bar();
    TS2(16 + c);
    step(3, true);
    if (c == 5 && threadIdx.x == 0) post_flag(p.ws + WS_FU + (((x * 28 + s) % 7) * 32 + (x * 28 + s) / 7) * 128, tag);
  }
  step(0, true); step(1, true); step(2, true); step(3, false);
  TS(3);
  const unsigned csb = lb + L_CS;
  PAD(2);
  bar();
  TS(4);
  float mean[2], rstd[2];
#pragma unroll
  for (int tt = 0; tt < 2; ++tt) { const f32x2 t = lds_ld<f32x2>(lb + L_ST + (64 * tw + 32 * tt + r) * 8); mean[tt] = t[0]; rstd[tt] = t[1]; }
  constexpr float C0 = -2.3022081985f, C1 = -0.1029432397f;
#pragma unroll
  for (int u = 0; u < 2; ++u) {
    f32x4 cg[2], cu[2];
#pragma unroll
    for (int b = 0; b < 2; ++b) {
      cg[b] = lds_ld<f32x4>(csb + (16 * (2 * fh + u) + 8 * b + 4 * hh) * 4);
      cu[b] = lds_ld<f32x4>(csb + 256 + (16 * (2 * fh + u) + 8 * b + 4 * hh) * 4);
    }
#pragma unroll
    for (int tt = 0; tt < 2; ++tt) {
      const float rr = rstd[tt], k0s = rr * C0, k1s = C1 * rr * rr * rr, ir2s = __builtin_amdgcn_rcpf(rr * rr);
      const f32x2 nm = {-mean[tt], -mean[tt]}, k0 = {k0s, k0s}, k1 = {k1s, k1s}, ir2 = {ir2s, ir2s};
      u32x4 o;
#pragma unroll
      for (int b = 0; b < 2; ++b)
#pragma unroll
        for (int e = 0; e < 4; e += 2) {
          const f32x2 ag = {acc[tt][u][8 * b + e], acc[tt][u][8 * b + e + 1]}, au = {acc[tt][u][8 * b + 4 + e], acc[tt][u][8 * b + 5 + e]};
          const f32x2 c2g = {cg[b][e], cg[b][e + 1]}, c2u = {cu[b][e], cu[b][e + 1]};
          const f32x2 a = __builtin_elementwise_fma(nm, c2g, ag), bb = __builtin_elementwise_fma(nm, c2u, au);
          const f32x2 z = a * __builtin_elementwise_fma(a * a, k1, k0);
          const f32x2 ex = {__builtin_amdgcn_exp2f(z[0]), __builtin_amdgcn_exp2f(z[1])};
          const f32x2 dn = __builtin_elementwise_fma(ex, ir2, ir2);
          const f32x2 rc = {__builtin_amdgcn_rcpf(dn[0]), __builtin_amdgcn_rcpf(dn[1])};
          const f32x2 hv = (a * bb) * rc;
          o[2 * b + (e >> 1)] = pk_bf16(hv[0], hv[1]);
        }
      const unsigned oh = WS_H + ((8 * ib + 2 * tw + tt) * 112 + 4 * jb + 2 * fh + u) * 1024 + lane * 16;
      asm volatile("buffer_store_dwordx4 %0, %1, %2, 0 offen" HSPOL "\n\ts_nop 1" ::"v"(o), "v"(oh), "s"(rws) : "memory");
    }
  }
  TS(5);
}

struct WdStage {
  rsrc_t rw; unsigned vw, lb; int kq, th, rot;
  DI void group(int g) const {
#pragma unroll
    for (int n = 0; n < 4; ++n) g2_piece(rw, vw, kq, th, 4 * g + n, rot, lb);
  }
};
#define TAILARG
#define TAILPASS
DI void loader_tail(const Params& p, unsigned lb, int s, int lane, const WdStage& wd, bool poller, unsigned tag TAILARG) {
  TSL(12);
  bar();
  TSL(13);
  wd.group(3);
  TSL(14);
  bar();
  wd.group(4);
  bar();
  wd.group(5);
  TSL(22);
  wait_vm<8>();
  TSL(23);
  bar();
  wd.group(6);
  wait_vm<8>();
  TSL(30);
  bar();
  wait_vm<4>();
  bar();
  if (poller) {
    const unsigned yf = __hip_atomic_load((const GAS unsigned*)(p.ws + WS_FY + (s % 7) * 32 * 128 + flag_off(32, lane)), __ATOMIC_RELAXED, __HIP_MEMORY_SCOPE_AGENT);
    const unsigned ok = flags_ok(yf, tag) ? 1u : 0u;
    if (lane == 0) lds_st<unsigned>(lb + 131072, ok);
  }
  wait_vm<0>();
  bar();
  const int yok = __builtin_amdgcn_readfirstlane(lds_ld<unsigned>(lb + 131072));
  if (poller && !yok) poll_flags(p.ws + WS_FY + (s % 7) * 32 * 128, 32, tag, lane);
  bar();
  if (!yok) bar();
}

DI void loader_x(const Params& p, unsigned lb, int x, int s, int lw, int lane) {
  const int ib = 4 * (x >> 2) + s / 7;
  const rsrc_t rq = mk_rsrc(p.q, S_ * D_ * 2);
  const int L3 = lane >> 3, L7 = lane & 7;
  const unsigned vx0 = (64 * lw + L3) * 896 + 16 * (L7 ^ ((lane >> 4) & 3));
  const unsigned vx1 = (64 * lw + 8 + L3) * 896 + 16 * (L7 ^ (((lane >> 4) & 3) | 4));
  const unsigned sx = ib * 256 * 896, lx = lb + lw * 8192;
  auto piece = [&](auto C, auto I) {
    constexpr int c = decltype(C)::value, i = decltype(I)::value;
    dma16c<(c % 3) * 49152 + i * 1024, c * 128 + (i >> 1) * 16 * 896>(rq, (i & 1) ? vx1 : vx0, sx, lx);
  };
  auto stage = [&](auto C) {
    piece(C, IC<0>{}); piece(C, IC<1>{}); piece(C, IC<2>{}); piece(C, IC<3>{}); piece(C, IC<4>{}); piece(C, IC<5>{}); piece(C, IC<6>{}); piece(C, IC<7>{});
  };
  if (XPRIO) __builtin_amdgcn_s_setprio(XPRIO);
  const rsrc_t rws = mk_rsrc(p.ws, WS_END);
  unsigned etag;
  TSL_DECL
  TS(8);
  { const unsigned oz = WS_EPOCH; asm volatile("buffer_load_dword %0, %1, %2, 0 offen" : "=&v"(etag) : "v"(oz), "s"(rws) : "memory"); }
  stage(IC<0>{});
  stage(IC<1>{});
  wait_vm<8>();
  asm volatile("" : "+v"(etag));
  const unsigned tag = __builtin_amdgcn_readfirstlane(etag) + 1u;
  TS(1);
  bar();
  stage(IC<2>{});
  auto lstage = [&](auto C) {
    constexpr int c = decltype(C)::value;
    TSL(16 + c);
    if (c < 5) wait_vm<8>(); else wait_vm<0>();
    TSL(24 + c);
    bar();
    TSL(2 + c);
    if constexpr (c + 3 <= 6) stage(IC<c + 3>{});
    if (c == 4) {
      const unsigned os = WS_STATS + (256 * ib + 64 * lw + lane) * 16;
      float mean, rstd;
      stats_wait(rws, os, tag, mean, rstd);
      lds_st<f32x2>(lb + L_ST + (64 * lw + lane) * 8, (f32x2){mean, rstd});
    }
  };
  lstage(IC<0>{}); lstage(IC<1>{}); lstage(IC<2>{}); lstage(IC<3>{}); lstage(IC<4>{}); lstage(IC<5>{});
  bar();
  bar();
  const WdStage wd = {mk_rsrc(p.wd, F_ * D_ * 2), g2_vw(lane, s % 7), lb, lw, 0, g2_rot(s)};
  TSL(8);
  wd.group(0); wd.group(1); wd.group(2);
  TSL(9);
  bar();
  bar();
  wait_vm<0>();
  loader_tail(p, lb, s, lane, wd, false, 0u TAILPASS);
  TSL_DUMP();
}

DI void loader_w(const Params& p, unsigned lb, int x, int s, int lw, int lane) {
  const int jb = 7 * (x & 3) + s % 7;
  const rsrc_t rg = mk_rsrc(p.wg, D_ * F_ * 2), ru = mk_rsrc(p.wu, D_ * F_ * 2);
  const rsrc_t rw = mk_rsrc(p.wd, F_ * D_ * 2), rws = mk_rsrc(p.ws, WS_END);
  const int L3 = lane >> 3, L7 = lane & 7;
  const unsigned vwA = (16 * lw + L3) * 3584 + jb * 128 + 32 * ((L7 >> 1) ^ ((lane >> 4) & 1)) + 16 * (L7 & 1);
  const unsigned vwB = (16 * lw + L3) * 3584 + jb * 128 + 32 * ((L7 >> 1) ^ (((lane >> 4) & 1) | 2)) + 16 * (L7 & 1);
  const unsigned lx = lb + 32768 + lw * 2048;
  auto stage = [&](auto C) {
    constexpr int c = decltype(C)::value;
    constexpr int cm = c;
    dma16c<(c % 3) * 49152, cm * 64 * 3584>(rg, vwA, 0, lx);
    dma16c<(c % 3) * 49152 + 1024, cm * 64 * 3584 + 8 * 3584>(rg, vwB, 0, lx);
    dma16c<(c % 3) * 49152 + 8192, cm * 64 * 3584>(ru, vwB, 0, lx);
    dma16c<(c % 3) * 49152 + 8192 + 1024, cm * 64 * 3584 + 8 * 3584>(ru, vwA, 0, lx);
  };
  if (WPRIO) __builtin_amdgcn_s_setprio(WPRIO);
  unsigned etag;
  { const unsigned oz = WS_EPOCH; asm volatile("buffer_load_dword %0, %1, %2, 0 offen" : "=&v"(etag) : "v"(oz), "s"(rws) : "memory"); }
  stage(IC<0>{});
  stage(IC<1>{});
  wait_vm<4>();
  asm volatile("" : "+v"(etag));
  const unsigned tag = __builtin_amdgcn_readfirstlane(etag) + 1u;
  bar();
  const int mi = x * 28 + s, cb = mi % 7, tb = mi / 7, ucg = lane >> 3, urp = lane & 7, ut0 = 64 * tb + 16 * lw + 2 * urp;
  u32x4 kk[5], vv[2];
  stage(IC<2>{});
  {
    const rsrc_t rk = mk_rsrc(p.k, S_ * D_ * 2), rv = mk_rsrc(p.v, S_ * D_ * 2);
    unsigned ok[5];
#pragma unroll
    for (int n = 0; n < 5; ++n) ok[n] = ut0 - 3 + n >= 0 ? (unsigned)((ut0 - 3 + n) * 896 + cb * 128 + ucg * 16) : 0x80000000u;
    const unsigned ov = ut0 * 896 + cb * 128 + ucg * 16;
    asm volatile("buffer_load_dwordx4 %0, %7, %13, 0 offen" KVPOL "\n\tbuffer_load_dwordx4 %1, %8, %13, 0 offen" KVPOL "\n\tbuffer_load_dwordx4 %2, %9, %13, 0 offen" KVPOL "\n\t"
                 "buffer_load_dwordx4 %3, %10, %13, 0 offen" KVPOL "\n\tbuffer_load_dwordx4 %4, %11, %13, 0 offen" KVPOL "\n\t"
                 "buffer_load_dwordx4 %5, %12, %14, 0 offen" KVPOL "\n\tbuffer_load_dwordx4 %6, %12, %14, 0 offen offset:896" KVPOL
                 : "=&v"(kk[0]), "=&v"(kk[1]), "=&v"(kk[2]), "=&v"(kk[3]), "=&v"(kk[4]), "=&v"(vv[0]), "=&v"(vv[1])
                 : "v"(ok[0]), "v"(ok[1]), "v"(ok[2]), "v"(ok[3]), "v"(ok[4]), "v"(ov), "s"(rk), "s"(rv) : "memory");
    dma16(mk_rsrc(p.sk, D_ * 8), lane < 32 ? cb * 512 + lane * 16 : 0x80000000u, 0, lb + L_SK);
  }
  auto wd_touch = [&](int i) {
    const int sh = 2 * i + (lw & 1);
    if ((lw >> 1) == (i & 1) && sh < 7) touch(rw, (unsigned)((s * 7 + sh) * 64 + lane) * 128u, lb + L_TOUCH);
  };
  const int chl = lw >> 1, ch = 2 * mi + chl, tA = (lw & 1) * 128 + lane;
  u32x4 hA, hB; u32x2 gA, gB, cg2; unsigned uf = 0;
  auto upairs = [&](const int j0) {
    const int tl = 16 * lw + 2 * urp, e = (tl & ~7) + 6 - (tl & 7);
    const unsigned ta = lb + L_UT + 8 * ucg * 128 + (((e >> 3) ^ ucg) * 16) + 2 * (e & 7);
    u32x4 wq[2];
#pragma unroll
    for (int j = 0; j < 2; ++j) wq[j] = lds_ld<u32x4>(lb + L_SK + ucg * 64 + (j0 + j) * 16);
    f32x2 kf[2][5], vf[2][2], wt[2][4], s0[2], s1[2];
#pragma unroll
    for (int j = 0; j < 2; ++j) {
#pragma unroll
      for (int n = 0; n < 5; ++n) kf[j][n] = (f32x2){bf_lo(kk[n][j0 + j]), bf_hi(kk[n][j0 + j])};
#pragma unroll
      for (int n = 0; n < 2; ++n) vf[j][n] = (f32x2){bf_lo(vv[n][j0 + j]), bf_hi(vv[n][j0 + j])};
    }
#pragma unroll
    for (int j = 0; j < 2; ++j) {
      wt[j][0] = (f32x2){bf_lo(wq[j][0]), bf_lo(wq[j][2])}; wt[j][1] = (f32x2){bf_hi(wq[j][0]), bf_hi(wq[j][2])};
      wt[j][2] = (f32x2){bf_lo(wq[j][1]), bf_lo(wq[j][3])}; wt[j][3] = (f32x2){bf_hi(wq[j][1]), bf_hi(wq[j][3])};
    }
    SB();
#pragma unroll
    for (int j = 0; j < 2; ++j) { s0[j] = kf[j][0] * wt[j][3]; s1[j] = kf[j][1] * wt[j][3]; }
    SB();
#pragma unroll
    for (int t = 2; t >= 0; --t) {
#pragma unroll
      for (int j = 0; j < 2; ++j) { s0[j] = __builtin_elementwise_fma(kf[j][3 - t], wt[j][t], s0[j]); s1[j] = __builtin_elementwise_fma(kf[j][4 - t], wt[j][t], s1[j]); }
      SB();
    }
#pragma unroll
    for (int j = 0; j < 2; ++j) { s0[j] *= vf[j][0]; s1[j] *= vf[j][1]; }
    SB();
#pragma unroll
    for (int j = 0; j < 2; ++j) {
      lds_st<unsigned>(ta + (2 * (j0 + j)) * 128, pk_bf16(s1[j][0], s0[j][0]));
      lds_st<unsigned>(ta + (2 * (j0 + j) + 1) * 128, pk_bf16(s1[j][1], s0[j][1]));
    }
  };
  TSL_DECL
  auto lstage = [&](auto C) {
    constexpr int c = decltype(C)::value;
    TSL(16 + c);
    if (c == 0) wait_vm<12>(); else if (c < 4) wait_vm<4>(); else if (c == 4) wait_vm<6>(); else wait_vm<5>();
    TSL(24 + c);
    bar();
    TSL(2 + c);
    if (c == 3) {
#pragma unroll
      for (int n = 0; n < 2; ++n) {
        const int cl = 16 * lw + 8 * n + (lane >> 3);
        const u32x4 t = lds_ld<u32x4>(lb + L_UT + cl * 128 + (((lane & 7) ^ (cl >> 3)) * 16));
        const unsigned o = WS_UT + (64 * cb + cl) * TROW + 128 * tb + (lane & 7) * 16;
        asm volatile("buffer_store_dwordx4 %0, %1, %2, 0 offen sc1\n\ts_nop 1" ::"v"(t), "v"(o), "s"(rws) : "memory");
      }
    }
    if (c == 4) {
      const rsrc_t rh = mk_rsrc(p.h, D_ * S_ * 2);
      const unsigned oh = ch * 4096 + tA * 16;
      asm volatile("buffer_load_dwordx4 %0, %4, %5, 0 offen nt\n\tbuffer_load_dwordx2 %1, %4, %5, 0 offen offset:16 nt\n\t"
                   "buffer_load_dwordx4 %2, %4, %5, 0 offen offset:1024 nt\n\tbuffer_load_dwordx2 %3, %4, %5, 0 offen offset:1040 nt"
                   : "=&v"(hA), "=&v"(gA), "=&v"(hB), "=&v"(gB) : "v"(oh), "s"(rh) : "memory");
      const unsigned oc = lw < 2 ? WS_CS + (lw * 1792 + jb * 64 + lane) * 8 : 0x80000000u;
      asm volatile("buffer_load_dwordx2 %0, %1, %2, 0 offen sc1" : "=&v"(cg2) : "v"(oc), "s"(rws) : "memory");
    }
    if (c == 5) {
      const unsigned of = WS_FU + (mi >> 5) * 32 * 128 + flag_off(32, lane);
      asm volatile("buffer_load_dword %0, %1, %2, 0 offen sc1" : "=&v"(uf) : "v"(of), "s"(rws) : "memory");
    }
    if (c == 1) {
      asm volatile("" : "+v"(kk[0]), "+v"(kk[1]), "+v"(kk[2]), "+v"(kk[3]), "+v"(kk[4]), "+v"(vv[0]), "+v"(vv[1]));
      upairs(0);
    }
    if (c == 2) upairs(2);
    if constexpr (c + 3 <= 6) stage(IC<c + 3>{});
    if (WDT && c >= 2) wd_touch(c - 2);
  };
  lstage(IC<0>{}); lstage(IC<1>{}); lstage(IC<2>{}); lstage(IC<3>{}); lstage(IC<4>{}); lstage(IC<5>{});
  asm volatile("s_waitcnt vmcnt(1)" : "+v"(cg2)::"memory");
  if (lw < 2) {
    if (!__all(cg2[0] == tag)) {
      for (unsigned it = 0; it < SPIN_CAP; ++it) {
        cg2 = __builtin_amdgcn_raw_buffer_load_b64(rws, WS_CS + (lw * 1792 + jb * 64 + lane) * 8, 0, 16);
        if (__all(cg2[0] == tag)) break;
        __builtin_amdgcn_s_sleep(1);
      }
    }
    lds_st<unsigned>(lb + L_CS + lw * 256 + lane * 4, cg2[1]);
  }
  if (lw == 0 && lane == 0) { lds_st<unsigned>(lb + L_CNT, 0u); }
  bar();
  wait_vm<0>();
  asm volatile("" : "+v"(hA), "+v"(gA), "+v"(hB), "+v"(gB), "+v"(uf));
  if (!flags_ok(uf, tag)) poll_flags(p.ws + WS_FU + (mi >> 5) * 32 * 128, 32, tag, lane);
  u32x4 uA, uB;
  {
    const unsigned ou = WS_UT + ch * TROW + tA * 16;
    asm volatile("buffer_load_dwordx4 %0, %2, %3, 0 offen sc1\n\tbuffer_load_dwordx4 %1, %2, %3, 0 offen offset:1024 sc1" : "=&v"(uA), "=&v"(uB) : "v"(ou), "s"(rws) : "memory");
  }
  const unsigned up = lb + CV_UP + chl * 4608, hs = lb + CV_HS + chl * 16640;
  {
    auto copies = [&](const u32x4& h4, const u32x2& h2, int t4) {
      const unsigned w0 = h4[0], w1 = h4[1], w2 = h4[2], w3 = h4[3], w4 = h2[0], w5 = h2[1];
      const unsigned a0 = __builtin_amdgcn_alignbit(w1, w0, 16), a1 = __builtin_amdgcn_alignbit(w2, w1, 16), a2 = __builtin_amdgcn_alignbit(w3, w2, 16),
                     a3 = __builtin_amdgcn_alignbit(w4, w3, 16), a4 = __builtin_amdgcn_alignbit(w5, w4, 16);
      const unsigned o = hs + 32 + 16 * t4;
      lds_st<u32x4>(o, (u32x4){w0, w1, w2, w3});
      lds_st<u32x4>(o + 4160, (u32x4){a0, a1, a2, a3});
      lds_st<u32x4>(o + 8320, (u32x4){w1, w2, w3, w4});
      lds_st<u32x4>(o + 12480, (u32x4){a1, a2, a3, a4});
    };
    copies(hA, gA, tA); copies(hB, gB, tA + 64);
    if (tA == 0) {
      const unsigned w0 = hA[0], a0 = __builtin_amdgcn_alignbit(hA[1], hA[0], 16);
      const u32x4 z = {0, 0, 0, 0};
#pragma unroll
      for (int sg = 0; sg < 4; ++sg) lds_st<u32x4>(hs + sg * 4160, z);
      lds_st<u32x4>(hs + 16, z);
      lds_st<u32x4>(hs + 4160 + 16, (u32x4){0, 0, 0, w0 << 16});
      lds_st<u32x4>(hs + 8320 + 16, (u32x4){0, 0, 0, w0});
      lds_st<u32x4>(hs + 12480 + 16, (u32x4){0, 0, w0 << 16, a0});
    }
    if (tA < 30) lds_st<u32x4>(up + 16 * tA, (u32x4){0, 0, 0, 0});
  }
  wait_vm<0>();
  asm volatile("" : "+v"(uA), "+v"(uB));
  lds_st<u32x4>(up + 480 + 16 * tA, uA);
  lds_st<u32x4>(up + 480 + 16 * (tA + 64), uB);
  bar();
  const WdStage wd = {rw, g2_vw(lane, s % 7), lb, lw, 1, g2_rot(s)};
  TSL(8);
  wd.group(0); wd.group(1); wd.group(2);
  TSL(9);
  if (lw == 0) {
    unsigned hf;
    if (HSLEEP) __builtin_amdgcn_s_sleep(HSLEEP);
    const unsigned of = WS_FH + x * 28 * 128 + flag_off(28, lane);
    asm volatile("buffer_load_dword %0, %1, %2, 0 offen sc1" : "=&v"(hf) : "v"(of), "s"(rws) : "memory");
    bar();
    TSL(10);
    asm volatile("s_waitcnt vmcnt(0)" : "+v"(hf)::"memory");
    if (!flags_ok(hf, tag)) poll_flags(p.ws + WS_FH + x * 28 * 128, 28, tag, lane);
    TSL(11);
  } else {
    bar();
  }
  bar();
  wait_vm<0>();
  loader_tail(p, lb, s, lane, wd, lw == 0, tag TAILPASS);
  TSL_DUMP();
}

DI void phase_conv(const Params& p, unsigned lb, int x, int s, int w, int lane, Ctx& cx) {
  const int mi = x * 28 + s, chl = w >> 2, wq = w & 3, ch = 2 * mi + chl, t4 = wq * 64 + lane;
  const rsrc_t rws = mk_rsrc(p.ws, WS_END);
  const unsigned tag = cx.tag;
  const float dv = cx.dv;
  const unsigned up = lb + CV_UP + chl * 4608, hs = lb + CV_HS + chl * 16640;
  {
    const int tok = 256 * x + 64 * (s / 7) + 16 * (w >> 1) + (lane & 15);
    const unsigned ox = tok * 896 + (s % 7) * 128 + (4 * (w & 1) + (lane >> 4)) * 16, os = WS_STATS + tok * 16;
    asm volatile("buffer_load_dwordx4 %0, %2, %4, 0 offen\n\tbuffer_load_dwordx4 %1, %3, %5, 0 offen sc1"
                 : "=&v"(cx.xq), "=&v"(cx.sg) : "v"(ox), "v"(os), "s"(mk_rsrc(p.q, S_ * D_ * 2)), "s"(rws) : "memory");
  }
  bar();
  TS(6);
  const int i = lane & 15, kg = lane >> 4, sg = (i + 1) & 3;
  f32x4 acc[8] = {};
  {
    const unsigned ga = up + 448 - 128 * wq + 32 * i + 16 * kg;
    unsigned f0 = hs + sg * 4160 + 128 * wq + 2 * (25 + i - 8 * kg - sg), f1 = f0 + 8;
    asm volatile("" : "+v"(f1));
    auto publish_h = [&]() {
      wait_vm<0>();
      if (lane == 0) {
        const unsigned old = __hip_atomic_fetch_add((LAS unsigned*)(size_t)(lb + L_CNT), 1u, __ATOMIC_RELAXED, __HIP_MEMORY_SCOPE_WORKGROUP);
        if (old == 7u) post_flag(p.ws + WS_FH + ((4 * (x >> 2) + s / 7) * 28 + 7 * (x & 3) + s % 7) * 128, tag);
      }
    };
    bf16x8 G[9], FA[2], FB[2];
    const unsigned gbb = ga - 64;
    auto rdA = [&](int t) { FA[t & 1] = cat(lds_ld<s16x4>(f0 + 512 * t), lds_ld<s16x4>(f1 + 512 * t)); };
    auto rdB = [&](int t) { FB[t & 1] = cat(lds_ld<s16x4>(f0 + 64 + 512 * (7 - t)), lds_ld<s16x4>(f1 + 64 + 512 * (7 - t))); };
    rdA(0);
#pragma unroll
    for (int b = 7; b >= 0; --b) G[b] = lds_ld<bf16x8>(ga + 512 * b);
    rdB(0); G[8] = lds_ld<bf16x8>(gbb);
#pragma unroll
    for (int t = 0; t < 8; ++t) {
      if (t == DRT) publish_h();
#pragma unroll
      for (int b = 7 - t; b >= 0; --b) {
        acc[t + b] = mfma16(FA[t & 1], G[b], acc[t + b]);
        SB();
        if (b == 7 - t && t < 7) { G[7 - t] = lds_ld<bf16x8>(gbb + 512 * (t + 1)); rdA(t + 1); }
        if (b == 6 - t && t < 7) rdB(t + 1);
        SB();
      }
#pragma unroll
      for (int b = 0; b <= t; ++b) {
        acc[7 - t + b] = mfma16(FB[t & 1], G[b == 0 ? 8 : 8 - b], acc[7 - t + b]);
        SB();
      }
    }
    if (DRT == 8) publish_h();
  }
  TS2(13);
  bar();
  TS(7);
  const unsigned red = lb + CV_RED + chl * 24576;
  f32x4 y0, y1;
  {
    auto scatter = [&](auto WQ) {
      constexpr int q0 = decltype(WQ)::value;
#pragma unroll
      for (int o = 1; o < 4; ++o) {
        constexpr int dummy = 0; (void)dummy;
        const int dst = (q0 + o) & 3;
        lds_st<f32x4>(red + (dst * 3 + (o - 1)) * 2048 + lane * 16, acc[2 * dst]);
        lds_st<f32x4>(red + (dst * 3 + (o - 1)) * 2048 + 1024 + lane * 16, acc[2 * dst + 1]);
      }
      y0 = acc[2 * q0]; y1 = acc[2 * q0 + 1];
    };
    if (wq == 0) scatter(IC<0>{}); else if (wq == 1) scatter(IC<1>{}); else if (wq == 2) scatter(IC<2>{}); else scatter(IC<3>{});
  }
  const unsigned ua = up + 480 + 1024 * wq + 32 * i + 16 * (kg >> 1) + 8 * (1 - (kg & 1));
  const u32x2 ua0 = lds_ld<u32x2>(ua), ua1 = lds_ld<u32x2>(ua + 512);
  bar();
  h_first(p, x, s, w, lane, cx);
#pragma unroll
  for (int o = 0; o < 3; ++o) {
    y0 += lds_ld<f32x4>(red + (wq * 3 + o) * 2048 + lane * 16);
    y1 += lds_ld<f32x4>(red + (wq * 3 + o) * 2048 + 1024 + lane * 16);
  }
  y0[0] = rbf(y0[0]) + rbf(dv * bf_hi(ua0[1])); y0[1] = rbf(y0[1]) + rbf(dv * bf_lo(ua0[1])); y0[2] = rbf(y0[2]) + rbf(dv * bf_hi(ua0[0])); y0[3] = rbf(y0[3]) + rbf(dv * bf_lo(ua0[0]));
  y1[0] = rbf(y1[0]) + rbf(dv * bf_hi(ua1[1])); y1[1] = rbf(y1[1]) + rbf(dv * bf_lo(ua1[1])); y1[2] = rbf(y1[2]) + rbf(dv * bf_hi(ua1[0])); y1[3] = rbf(y1[3]) + rbf(dv * bf_lo(ua1[0]));
  const unsigned d0 = pk_bf16(y0[0], y0[1]), d1 = pk_bf16(y0[2], y0[3]), e0 = pk_bf16(y1[0], y1[1]), e1 = pk_bf16(y1[2], y1[3]);
  const auto s0 = __builtin_amdgcn_permlane16_swap(d0, e0, false, false);
  const auto s1 = __builtin_amdgcn_permlane16_swap(d1, e1, false, false);
  const u32x4 o = {s0[0], s1[0], s0[1], s1[1]};
  const unsigned oy = WS_YT + ch * TROW + 512 * (2 * wq + (kg & 1)) + 32 * i + 8 * (kg & 2);
  asm volatile("buffer_store_dwordx4 %0, %1, %2, 0 offen sc1\n\ts_nop 1" ::"v"(o), "v"(oy), "s"(rws) : "memory");
}

DI void phase_gemm2(const Params& p, unsigned lb, int x, int s, int w, int lane, Ctx& cx) {
  const int i2 = s / 7, db = s % 7, th = w >> 2, kq = w & 3, rot = g2_rot(s);
  const unsigned tag = cx.tag;
  const rsrc_t rws = mk_rsrc(p.ws, WS_END);
  const rsrc_t rq = mk_rsrc(p.q, S_ * D_ * 2), ro = mk_rsrc(p.out, S_ * D_ * 2);
  const int tk = 16 * (w >> 1) + (lane & 15), ck = 4 * (w & 1) + (lane >> 4), tok = 256 * x + 64 * i2 + tk;
  PAD(3);
  bar();
  TS(10);
  const unsigned hb = WS_H + ((8 * x + 2 * i2 + th) * 112 + 28 * kq) * 1024;
  const unsigned vh = lane * 16;
  u32x4 (&hr)[HD] = cx.hr;
  const int hh = lane >> 5, blk = (lane >> 4) & 1, q = (lane & 15) >> 2, pp = lane & 3;
  unsigned aa[2];
#pragma unroll
  for (int dt = 0; dt < 2; ++dt) aa[dt] = lb + kq * 8192 + (4 * hh + q) * 128 + 64 * (dt ^ ((q >> 1) & 1)) + 32 * blk + 8 * pp;
  f32x16 acc[2] = {};
  bf16x8 fa[2][2];
  static_assert(HD == 8, "two groups of four k-steps per loop iteration");
  auto rd1 = [&](int j, int dt) { fa[j & 1][dt] = cat(lds_tr(aa[dt] + (j & 3) * 2048), lds_tr(aa[dt] + (j & 3) * 2048 + 1024)); };
  auto step = [&](int j, bool ahead) {
    SB();
    const bf16x8 b = __builtin_bit_cast(bf16x8, hr[j & 7]);
    acc[0] = mfma32(fa[j & 1][0], b, acc[0]);
    SB();
    if (ahead) rd1(j + 1, 0);
    SB();
    acc[1] = mfma32(fa[j & 1][1], b, acc[1]);
    SB();
    if (ahead) rd1(j + 1, 1);
    SB();
  };
  u32x4 yv; int yok;
  rd1(0, 0); rd1(0, 1);
  int cnt = 8, st = rot + 8; st -= st >= 28 ? 28 : 0;
#pragma unroll 1
  for (int gg = 0; gg < 3; ++gg) {
#pragma unroll
    for (int j = 0; j < 8; ++j) {
      if ((j & 3) == 3) {
        const int g = 2 * gg + (j >> 2);
        const unsigned d = (g & 3) == 3 ? (unsigned)-98304 : 32768u;
        aa[0] += d; aa[1] += d;
        bar();
        TS2(24 + g);
      }
      asm volatile("s_waitcnt vmcnt(7)" : "+v"(hr[j])::"memory");
      step(j, true);
      asm volatile("buffer_load_dwordx4 %0, %1, %2, %3 offen" HPOL : "=v"(hr[j]) : "v"(vh), "s"(rws), "s"(cnt < 28 ? hb + st * 1024 : 0x80000000u) : "memory");
      st = st == 27 ? 0 : st + 1; ++cnt;
      if (j == 3 && gg == 1 && threadIdx.x == 0) post_flag(p.ws + WS_FY + (x * 28 + s) * 128, tag);
      SB();
    }
  }
  yok = __builtin_amdgcn_readfirstlane(lds_ld<unsigned>(lb + 131072));
  {
    const unsigned oy = yok ? WS_YT + (64 * db + (threadIdx.x >> 3)) * TROW + (256 * x + 64 * i2) * 2 + (threadIdx.x & 7) * 16 : 0x80000000u;
    asm volatile("buffer_load_dwordx4 %0, %1, %2, 0 offen sc1" : "=&v"(yv) : "v"(oy), "s"(rws) : "memory");
  }
  asm volatile("s_waitcnt vmcnt(8)" : "+v"(hr[0])::"memory"); step(0, true);
  asm volatile("s_waitcnt vmcnt(7)" : "+v"(hr[1])::"memory"); step(1, true);
  asm volatile("s_waitcnt vmcnt(6)" : "+v"(hr[2])::"memory"); step(2, true);
  asm volatile("s_waitcnt vmcnt(5)" : "+v"(hr[3])::"memory"); step(3, false);
  u32x4 xq = cx.xq, sg = cx.sg;
  TS(11);
  const unsigned pb = lb, yb = lb + 135168;
  {
    const int row = 32 * th + (lane & 31);
#pragma unroll
    for (int dt = 0; dt < 2; ++dt)
#pragma unroll
      for (int G = 0; G < 4; ++G) {
        const f32x4 t = {acc[dt][4 * G], acc[dt][4 * G + 1], acc[dt][4 * G + 2], acc[dt][4 * G + 3]};
        lds_st<f32x4>(pb + kq * 16384 + row * 256 + (((8 * dt + 2 * G + hh) ^ (row & 15)) * 16), t);
      }
  }
  wait_vm<0>();
  asm volatile("" : "+v"(xq), "+v"(sg), "+v"(yv), "+v"(hr[4]), "+v"(hr[5]), "+v"(hr[6]), "+v"(hr[7]));
  if (yok) lds_st<u32x4>(yb + (threadIdx.x >> 3) * 144 + (threadIdx.x & 7) * 16, yv);
  float mean = __uint_as_float(sg[1]), rstd = __uint_as_float(sg[3]);
  if (!__all(sg[0] == tag && sg[2] == tag)) stats_wait(rws, WS_STATS + tok * 16, tag, mean, rstd);
  bar();
  if (!yok) {
    yv = __builtin_amdgcn_raw_buffer_load_b128(rws, WS_YT + (64 * db + (threadIdx.x >> 3)) * TROW + (256 * x + 64 * i2) * 2 + (threadIdx.x & 7) * 16, 0, 16);
    lds_st<u32x4>(yb + (threadIdx.x >> 3) * 144 + (threadIdx.x & 7) * 16, yv);
    bar();
  }
  f32x4 o0 = {}, o1 = {};
#pragma unroll
  for (int k4 = 0; k4 < 4; ++k4) {
    o0 += lds_ld<f32x4>(pb + k4 * 16384 + tk * 256 + (((2 * ck) ^ (tk & 15)) * 16));
    o1 += lds_ld<f32x4>(pb + k4 * 16384 + tk * 256 + (((2 * ck + 1) ^ (tk & 15)) * 16));
  }
  const unsigned ya = yb + (8 * ck + q) * 144 + (16 * (w >> 1) + 4 * pp) * 2;
  const s16x4 y0 = lds_tr(ya), y1 = lds_tr(ya + 4 * 144);
  u32x4 ov;
#pragma unroll
  for (int e = 0; e < 8; e += 2) {
    const f32x2 o = rbf2(e < 4 ? (f32x2){o0[e & 3], o0[(e & 3) + 1]} : (f32x2){o1[e & 3], o1[(e & 3) + 1]});
    const f32x2 xv = {bf_lo(xq[e >> 1]), bf_hi(xq[e >> 1])};
    const unsigned yp = e < 4 ? __builtin_bit_cast(u32x2, y0)[(e & 3) >> 1] : __builtin_bit_cast(u32x2, y1)[(e & 3) >> 1];
    const f32x2 yy = {bf_lo(yp), bf_hi(yp)};
    const f32x2 t = o * (f32x2){-1.4426950409f, -1.4426950409f};
    const f32x2 dn = (f32x2){__builtin_amdgcn_exp2f(t[0]), __builtin_amdgcn_exp2f(t[1])} + (f32x2){1.f, 1.f};
    const f32x2 g = rbf2(o * (f32x2){__builtin_amdgcn_rcpf(dn[0]), __builtin_amdgcn_rcpf(dn[1])});
    const f32x2 xn = rbf2(rbf2(xv - (f32x2){mean, mean}) * (f32x2){rstd, rstd});
    const f32x2 r2 = xv + rbf2(rbf2(xn * yy) * g);
    ov[e >> 1] = pk_bf16(r2[0], r2[1]);
  }
  __builtin_amdgcn_raw_buffer_store_b128(ov, ro, tok * 896 + db * 128 + ck * 16, 0, OUTAUX);
  TS(12);
}

__global__ __launch_bounds__(1024, 4) void h3_geglu_kernel(Params p) {
  __shared__ __attribute__((aligned(16))) char smem[LDS_BYTES];
  const unsigned lb = (unsigned)(size_t)(LAS char*)smem;
  const int tid = threadIdx.x, lane = tid & 63, w = __builtin_amdgcn_readfirstlane(tid >> 6);
  const int b = blockIdx.x, x = b & 7, s = b >> 3;
  const unsigned long long t_start = rtime();
  const char* pc;
  asm volatile("s_getpc_b64 %0" : "=s"(pc));
  if (CODEW == 2 && w == 15 && s < 28) touch_g(pc + (8 * s + (lane & 7)) * 128, lb + L_TOUCH);
  TS(0);
  if (s < 28) {
    if (w >= 12) {
      loader_w(p, lb, x, s, w - 12, lane);
    } else if (w >= 8) {
      loader_x(p, lb, x, s, w - 8, lane);
    } else {
      Ctx cx; cx.t0 = t_start;
      phase_gemm1(p, lb, x, s, w, lane, cx);
      phase_conv(p, lb, x, s, w, lane, cx);
      TS(9);
      phase_gemm2(p, lb, x, s, w, lane, cx);
    }
  } else {
    const int j = b - 224;
    if (CODEW == 1 && (j >> 3) == 0 && w < 4) {
      const int ln = w * 64 + lane;
      touch_g(pc + (ln < CODE_LINES ? ln : 0) * 128, lb + 65536);
    }
    if (TLBW && (j >> 3) == 0 && w >= 12) {
      if (w < 15) touch(mk_rsrc(p.ws, WS_END), (unsigned)((w - 12) * 64 + lane) * 65536u, lb + 65536);
      else touch(mk_rsrc(p.out, S_ * D_ * 2), (unsigned)lane * 65536u, lb + 65536);
    }
    if (WARM && w < 14) {
      const int q = j >> 3;
      touch(mk_rsrc((w & 1) ? p.v : p.k, S_ * D_ * 2), (unsigned)((256 * x + 64 * q) * 896 + ((w >> 1) * 64 + lane) * 128), lb + 65536);
    }
    if (j < 28) {
      const unsigned tag = __builtin_amdgcn_readfirstlane(*(const GAS unsigned*)(p.ws + WS_EPOCH) + 1u);
      const int m = w & 1, r0 = (w >> 1) * 8 + (lane >> 3);
      const rsrc_t rm = mk_rsrc(m ? p.wu : p.wg, D_ * F_ * 2);
      u32x4 t[7];
#pragma unroll
      for (int n = 0; n < 7; ++n) t[n] = __builtin_amdgcn_raw_buffer_load_b128(rm, (r0 + 64 * n) * 3584 + j * 128 + (lane & 7) * 16, 0, 0);
      float a[8] = {};
#pragma unroll
      for (int n = 0; n < 7; ++n)
#pragma unroll
        for (int i = 0; i < 4; ++i) { a[2 * i] += bf_lo(t[n][i]); a[2 * i + 1] += bf_hi(t[n][i]); }
      lds_st<f32x4>(lb + (w * 8 + (lane >> 3)) * 256 + (lane & 7) * 32, (f32x4){a[0], a[1], a[2], a[3]});
      lds_st<f32x4>(lb + (w * 8 + (lane >> 3)) * 256 + (lane & 7) * 32 + 16, (f32x4){a[4], a[5], a[6], a[7]});
      bar();
      if (w < 2) {
        float sum = 0.f;
#pragma unroll
        for (int q = 0; q < 64; ++q) sum += lds_ld<float>(lb + (((q >> 3) * 2 + w) * 8 + (q & 7)) * 256 + lane * 4);
        const u32x2 g = {tag, __float_as_uint(sum)};
        __builtin_amdgcn_raw_buffer_store_b64(g, mk_rsrc(p.ws, WS_END), WS_CS + (w * 1792 + j * 64 + lane) * 8, 0, 16);
      }
    }
    if (WARM && w < 14) {
      const int blk = w * 32 + j;
      if (blk < 196) touch(mk_rsrc(p.wd, F_ * D_ * 2), (unsigned)(blk * 64 + lane) * 128u, lb + 65536);
      else touch(mk_rsrc(p.h, D_ * S_ * 2), (unsigned)((blk - 196) * 64 + lane) * 128u, lb + 65536);
    }
    if (b == 224 && w == 15) {
      const unsigned tag = __builtin_amdgcn_readfirstlane(*(const GAS unsigned*)(p.ws + WS_EPOCH) + 1u);
#pragma unroll 1
      for (int n = 0; n < 4; ++n) poll_flags_slow(p.ws + WS_FH + n * 56 * 128, 56, tag, lane);
      if (lane == 0) post_flag(p.ws + WS_EPOCH, tag);
    }
    wait_vm<0>();
  }
  TS(15);
}

extern "C" void kernel_launch(void* const* d_in, const int* in_sizes, int n_in, void* d_out, int out_size, void* d_ws, size_t ws_size, hipStream_t stream) {
  Params p;
  p.q = (const u16*)d_in[0]; p.k = (const u16*)d_in[1]; p.v = (const u16*)d_in[2]; p.sk = (const u16*)d_in[3];
  p.h = (const u16*)d_in[4]; p.dv = (const u16*)d_in[5];
  p.wg = (const u16*)d_in[7]; p.wu = (const u16*)d_in[8]; p.wd = (const u16*)d_in[9];
  p.out = (u16*)d_out; p.ws = (char*)d_ws;
  h3_geglu_kernel<<<256, 1024, 0, stream>>>(p);
}
```

```cpp
#include <hip/hip_runtime.h>
#include <hip/hip_bf16.h>
#include <stdint.h>

typedef unsigned short u16;
typedef __attribute__((ext_vector_type(8))) short bf16x8;
typedef __attribute__((ext_vector_type(4))) short s16x4;
typedef __attribute__((ext_vector_type(4))) float f32x4;
typedef __attribute__((ext_vector_type(2))) float f32x2;
typedef __attribute__((ext_vector_type(16))) float f32x16;
typedef __attribute__((ext_vector_type(4))) unsigned u32x4;
typedef __attribute__((ext_vector_type(2))) unsigned u32x2;
typedef __attribute__((ext_vector_type(2))) __bf16 bf16x2;
typedef __amdgpu_buffer_rsrc_t rsrc_t;
#define HSPOL " sc1"
#define LAS __attribute__((address_space(3)))
#define GAS __attribute__((address_space(1)))
#define DI __device__ __forceinline__

#define OPT_OWN 0
#define KVT 1
#define KVFIRST 0
#define WDLATE 1
#define PAUX 2
#define USTAUX 16
#define HSLEEP 14
#define LPRIO 0
#define WARM 1
#define UPRIO 2
#define KVPOL " nt"
#define WPRIO 0
#define XPRIO 0
#define WDT 0
#define DRT 3
#define DRA 2
#define DRR 1
#define TLBW 0
#define OUTAUX 0
#define PREW 0
#define CODEW 1
constexpr int CODE_LINES = 224;
#define PFN 17

constexpr int S_ = 2048, D_ = 448, F_ = 1792;
constexpr unsigned WS_EPOCH = 0, WS_FH = 8192, WS_FY = 36864, WS_STATS = 65536, WS_FU = 98304, WS_CS = 2031616;
constexpr unsigned TROW = 4224;
constexpr unsigned WS_UT = 131072, WS_YT = 2097152, WS_H = 4194304, WS_END = WS_H + 7340032;
constexpr unsigned WS_DBG = 16u << 20;
constexpr int LDS_BYTES = 163840;
constexpr unsigned SPIN_CAP = 1u << 14;

struct Params {
  const u16 *q, *k, *v, *sk, *h, *dv, *wg, *wu, *wd;
  u16* out;
  char* ws;
};

DI rsrc_t mk_rsrc(const void* p, unsigned bytes) { return __builtin_amdgcn_make_buffer_rsrc((void*)p, (short)0, (int)bytes, 0x00020000); }
DI void dma16(rsrc_t r, unsigned voff, unsigned soff, unsigned lds) {
  asm volatile("s_mov_b32 m0, %0\n\ts_nop 0\n\tbuffer_load_dwordx4 %1, %2, %3 offen lds" ::"s"(lds), "v"(voff), "s"(r), "s"(soff) : "memory");
}
DI void touch(rsrc_t r, unsigned voff, unsigned lds) {
  asm volatile("s_mov_b32 m0, %0\n\ts_nop 0\n\tbuffer_load_dword %1, %2, 0 offen lds" ::"s"(lds), "v"(voff), "s"(r) : "memory");
}
template <int NT> DI void touch_p(rsrc_t r, unsigned voff, unsigned lds) {
  if (NT) asm volatile("s_mov_b32 m0, %0\n\ts_nop 0\n\tbuffer_load_dword %1, %2, 0 offen nt lds" ::"s"(lds), "v"(voff), "s"(r) : "memory");
  else touch(r, voff, lds);
}
DI void touch_g(const char* a, unsigned lds) {
  asm volatile("s_mov_b32 m0, %0\n\ts_nop 0\n\tglobal_load_lds_dword %1, off" ::"s"(lds), "v"(a) : "memory");
}
template <unsigned LA, unsigned SA>
DI void dma16c(rsrc_t r, unsigned voff, unsigned sbase, unsigned lbase) {
  unsigned t;
  asm volatile("s_add_u32 m0, %2, %5\n\ts_add_u32 %0, %3, %6\n\tbuffer_load_dwordx4 %1, %4, %0 offen lds"
               : "=&s"(t) : "v"(voff), "s"(lbase), "s"(sbase), "s"(r), "i"(LA), "i"(SA) : "memory", "scc");
}
template <int N> DI void wait_vm() { asm volatile("s_waitcnt vmcnt(%0)" ::"i"(N) : "memory"); }
DI void bar() { asm volatile("s_waitcnt lgkmcnt(0)\n\ts_barrier" ::: "memory"); }
DI float bf_lo(unsigned x) { return __uint_as_float(x << 16); }
DI float bf_hi(unsigned x) { return __uint_as_float(x & 0xffff0000u); }
DI unsigned pk_bf16(float lo, float hi) { f32x2 t = {lo, hi}; return __builtin_bit_cast(unsigned, __builtin_convertvector(t, bf16x2)); }
DI bf16x8 lds_b128(const char* p) { return *(const LAS bf16x8*)(p); }
DI s16x4 lds_tr(unsigned a) { return __builtin_amdgcn_ds_read_tr16_b64_v4i16((LAS s16x4*)(size_t)a); }
DI bf16x8 cat(s16x4 a, s16x4 b) { return __builtin_shufflevector(a, b, 0, 1, 2, 3, 4, 5, 6, 7); }
template <class T> DI T lds_ld(unsigned a) { return *(const LAS T*)(size_t)a; }
template <class T> DI void lds_st(unsigned a, T v) { *(LAS T*)(size_t)a = v; }
DI f32x16 mfma32(bf16x8 a, bf16x8 b, f32x16 c) { return __builtin_amdgcn_mfma_f32_32x32x16_bf16(a, b, c, 0, 0, 0); }
DI f32x4 mfma16(bf16x8 a, bf16x8 b, f32x4 c) { return __builtin_amdgcn_mfma_f32_16x16x32_bf16(a, b, c, 0, 0, 0); }
DI unsigned long long rtime() { return __builtin_amdgcn_s_memrealtime(); }
DI float dpp_add(float v, const int ctrl_row_shr) { return v; }
template <int CTRL, int RM> DI float dpp_mov(float v) { return __builtin_bit_cast(float, __builtin_amdgcn_update_dpp(0, __builtin_bit_cast(int, v), CTRL, RM, 0xf, true)); }
DI float wave_sum(float v) {
  v += dpp_mov<0x111, 0xf>(v); v += dpp_mov<0x112, 0xf>(v); v += dpp_mov<0x114, 0xf>(v); v += dpp_mov<0x118, 0xf>(v);
  v += dpp_mov<0x142, 0xa>(v); v += dpp_mov<0x143, 0xc>(v);
  return __builtin_bit_cast(float, __builtin_amdgcn_readlane(__builtin_bit_cast(int, v), 63));
}
#define TS(i)
#define TSL(i)
#define TSL_DECL
#define TSL_DUMP()
#define TS2(i)

DI void poll_flags(const char* base, int n, unsigned tag, int lane) {
  const GAS unsigned* f = (const GAS unsigned*)(base + (lane < n ? lane : 0) * 128);
  for (unsigned it = 0; it < SPIN_CAP; ++it) {
    unsigned x = __hip_atomic_load(f, __ATOMIC_RELAXED, __HIP_MEMORY_SCOPE_AGENT);
    if (__all(x == tag)) break;
    __builtin_amdgcn_s_sleep(2);
  }
}
DI void poll_flags_slow(const char* base, int n, unsigned tag, int lane) {
  const GAS unsigned* f = (const GAS unsigned*)(base + (lane < n ? lane : 0) * 128);
  for (unsigned it = 0; it < SPIN_CAP; ++it) {
    unsigned x = __hip_atomic_load(f, __ATOMIC_RELAXED, __HIP_MEMORY_SCOPE_AGENT);
    if (__all(x == tag)) break;
    __builtin_amdgcn_s_sleep(32);
  }
}
DI void post_flag(char* addr, unsigned tag) { __hip_atomic_store((GAS unsigned*)addr, tag, __ATOMIC_RELAXED, __HIP_MEMORY_SCOPE_AGENT); }

DI bool flags_ok(unsigned v, unsigned tag) { return __all(v == tag); }
DI unsigned flag_off(int n, int lane) { return (lane < n ? lane : 0) * 128; }
#define SB() __builtin_amdgcn_sched_barrier(0)
template <int V> struct IC { static constexpr int value = V; };
DI void asm_pin(unsigned& v) { asm volatile("" : "+v"(v)); }
DI float rbf(float x) { return __uint_as_float(pk_bf16(x, 0.f) << 16); }

DI f32x2 rbf2(f32x2 v) { const unsigned u = pk_bf16(v[0], v[1]); return (f32x2){bf_lo(u), bf_hi(u)}; }
DI u32x4 row_stats(u32x4 d, unsigned tag) {
  float e[8];
#pragma unroll
  for (int i = 0; i < 4; ++i) { e[2 * i] = bf_lo(d[i]); e[2 * i + 1] = bf_hi(d[i]); }
  float s = ((e[0] + e[1]) + (e[2] + e[3])) + ((e[4] + e[5]) + (e[6] + e[7]));
  const float mean = wave_sum(s) * (1.f / D_);
  const float on = (threadIdx.x & 63) < 56 ? 1.f : 0.f;
  float ss = 0.f;
#pragma unroll
  for (int i = 0; i < 8; ++i) { const float t = e[i] - mean; ss = fmaf(t, t, ss); }
  const float var = wave_sum(ss * on) * (1.f / D_);
  const float den = rbf(__builtin_amdgcn_sqrtf(rbf(rbf(var) + 1e-5f)));
  u32x4 g = {tag, __float_as_uint(rbf(mean)), tag, __float_as_uint(__builtin_amdgcn_rcpf(den))};
  return g;
}
DI void stats_wait(rsrc_t rs, unsigned off, unsigned tag, float& mean, float& rstd) {
  for (unsigned it = 0;; ++it) {
    const u32x4 g = __builtin_amdgcn_raw_buffer_load_b128(rs, off, 0, 16);
    mean = __uint_as_float(g[1]); rstd = __uint_as_float(g[3]);
    if (__all(g[0] == tag && g[2] == tag) || it >= SPIN_CAP) break;
    __builtin_amdgcn_s_sleep(1);
  }
}

constexpr unsigned CV_BASE = 98304;
constexpr unsigned CV_UP = CV_BASE;
constexpr unsigned CV_HS = CV_BASE + 9216;
constexpr unsigned CV_RED = CV_HS;
constexpr unsigned L_UT = 147456, L_SK = 155648, L_ST = 159744;
constexpr unsigned L_CS = 157696, L_OK = 158720, L_CNT = 158728, L_TOUCH = 158976;

#define HPOL " sc1"
#define HDEPTH 4
constexpr int HD = HDEPTH;
#define PADP 0
#define PADT 0
#define PAD(pt) do { if (PADP == (pt)) { while (rtime() - cx.t0 < (unsigned long long)PADT) __builtin_amdgcn_s_sleep(1); } } while (0)
struct Ctx {
  unsigned long long t0; unsigned tag; float dv; u32x4 hr[HD], xq, sg;
};
DI void g2_stage(rsrc_t rw, unsigned vw, int kq, int th, int grp, int rot, unsigned lb) {
#pragma unroll
  for (int n = 0; n < 4; ++n) {
    int st = 4 * grp + n + rot; st -= st >= 28 ? 28 : 0;
    dma16(rw, vw, (unsigned)((28 * kq + st) * 16 + 8 * th) * 896u, lb + (grp & 3) * 32768 + kq * 8192 + n * 2048 + th * 1024);
  }
}
DI void g2_piece(rsrc_t rw, unsigned vw, int kq, int th, int step, int rot, unsigned lb) {
  int st = step + rot; st -= st >= 28 ? 28 : 0;
  dma16(rw, vw, (unsigned)((28 * kq + st) * 16 + 8 * th) * 896u, lb + ((step >> 2) & 3) * 32768 + kq * 8192 + (step & 3) * 2048 + th * 1024);
}
DI void h_load(const Params& p, u32x4& dst, unsigned hb, int step, int rot, unsigned vh) {
  int st = step + rot; st -= st >= 28 ? 28 : 0;
  asm volatile("buffer_load_dwordx4 %0, %1, %2, %3 offen" HPOL : "=v"(dst) : "v"(vh), "s"(mk_rsrc(p.ws, WS_END)), "s"(hb + st * 1024) : "memory");
}
DI int g2_rot(int s);
DI void h_first(const Params& p, int x, int s, int w, int lane, Ctx& cx) {
  const unsigned hb = WS_H + ((8 * x + 2 * (s / 7) + (w >> 2)) * 112 + 28 * (w & 3)) * 1024;
#pragma unroll
  for (int n = 0; n < HD; ++n) h_load(p, cx.hr[n], hb, n, g2_rot(s), lane * 16);
}
DI unsigned g2_vw(int lane, int db) { const int L3 = lane >> 3, L7 = lane & 7; return L3 * 896 + db * 128 + 16 * (L7 ^ (((L3 >> 1) & 1) << 2)); }
DI int g2_rot(int s) { const int r = 4 * (s % 7) + 7 * (s / 7); return r >= 28 ? r - 28 : r; }

DI void phase_gemm1(const Params& p, unsigned lb, int x, int s, int w, int lane, Ctx& cx) {
  const int ib = 4 * (x >> 2) + s / 7, jb = 7 * (x & 3) + s % 7;
  const rsrc_t rq = mk_rsrc(p.q, S_ * D_ * 2), rws = mk_rsrc(p.ws, WS_END);
  const int gw = jb * 8 + w;
  unsigned tag;
  {
    u32x4 sr0, sr1; unsigned etag;
    const unsigned o0 = lane < 56 ? (256 * ib + gw) * 896 + 16 * lane : 0x80000000u;
    const unsigned o1 = (lane < 56 && gw < 32) ? (256 * ib + 224 + gw) * 896 + 16 * lane : 0x80000000u;
    const unsigned oz = WS_EPOCH;
    unsigned dvb; const unsigned od = (2 * (x * 28 + s) + (w >> 2)) * 2;
    asm volatile("buffer_load_dword %2, %6, %9, 0 offen\n\tbuffer_load_dwordx4 %0, %4, %8, 0 offen\n\tbuffer_load_dwordx4 %1, %5, %8, 0 offen\n\t"
                 "buffer_load_ushort %3, %7, %10, 0 offen\n\ts_waitcnt vmcnt(0)"
                 : "=&v"(sr0), "=&v"(sr1), "=&v"(etag), "=&v"(dvb) : "v"(o0), "v"(o1), "v"(oz), "v"(od), "s"(rq), "s"(rws), "s"(mk_rsrc(p.dv, D_ * 2)) : "memory");
    cx.dv = bf_lo(dvb);
    tag = __builtin_amdgcn_readfirstlane(etag) + 1u;
    cx.tag = tag;
    const u32x4 g0 = row_stats(sr0, tag), g1 = row_stats(sr1, tag);
    const unsigned p0 = lane == 0 ? WS_STATS + (256 * ib + gw) * 16 : 0x80000000u;
    const unsigned p1 = (lane == 0 && gw < 32) ? WS_STATS + (256 * ib + 224 + gw) * 16 : 0x80000000u;
    asm volatile("buffer_store_dwordx4 %0, %2, %4, 0 offen sc1\n\tbuffer_store_dwordx4 %1, %3, %4, 0 offen sc1\n\ts_nop 1" ::"v"(g0), "v"(g1), "v"(p0), "v"(p1), "s"(rws) : "memory");
  }
  const int tw = w & 3, fh = w >> 2, r = lane & 31, hh = lane >> 5;
  unsigned xa[4], wa[2];
#pragma unroll
  for (int i = 0; i < 4; ++i) xa[i] = lb + (64 * tw + r) * 128 + 16 * ((2 * i + hh) ^ ((r >> 1) & 7));
  {
    const int blk = (lane >> 4) & 1, q = (lane & 15) >> 2, pp = lane & 3, m = pp >> 1, f = (q >> 1) | ((m ^ hh) << 1);
#pragma unroll
    for (int u = 0; u < 2; ++u) wa[u] = lb + 32768 + m * 8192 + (8 * hh + q) * 128 + 32 * ((2 * fh + u) ^ f) + 16 * blk + 8 * (pp & 1);
  }
  f32x16 acc[2][2] = {};
  if (PREW) {
    f32x16 z = {};
    const bf16x8 o = {0x3c00, 0x3c10, 0x3c20, 0x3c30, 0x3c40, 0x3c50, 0x3c60, 0x3c70};
#pragma unroll 1
    for (int i = 0; i < PREW; ++i) { z = mfma32(o, o, z); z = mfma32(o, o, z); z = mfma32(o, o, z); z = mfma32(o, o, z); }
    asm volatile("" ::"v"(z));
  }
  TS(1);
  PAD(1);
  bar();
  TS(2);
  bf16x8 fb[2][2] = {}, fa[2][2] = {};
  auto rdA = [&](int ks, int u) { fa[ks & 1][u] = cat(lds_tr(wa[u] + (ks & 3) * 2048), lds_tr(wa[u] + (ks & 3) * 2048 + 512)); };
  auto rdB = [&](int ks) { fb[ks & 1][0] = lds_ld<bf16x8>(xa[ks & 3]); fb[ks & 1][1] = lds_ld<bf16x8>(xa[ks & 3] + 4096); };
  auto step = [&](int ks, bool ahead) {
    const int pb = ks & 1;
    SB();
    acc[0][0] = mfma32(fa[pb][0], fb[pb][0], acc[0][0]);
    SB();
    if (ahead) rdA(ks + 1, 0);
    SB();
    acc[1][0] = mfma32(fa[pb][0], fb[pb][1], acc[1][0]);
    SB();
    if (ahead) rdB(ks + 1);
    SB();
    acc[0][1] = mfma32(fa[pb][1], fb[pb][0], acc[0][1]);
    SB();
    if (ahead) rdA(ks + 1, 1);
    SB();
    acc[1][1] = mfma32(fa[pb][1], fb[pb][1], acc[1][1]);
    SB();
  };
  rdA(0, 0); rdB(0); rdA(0, 1);
  int sl = 0;
#pragma unroll 1
  for (int c = 0; c < 6; ++c) {
    step(0, true);
    step(1, true);
    step(2, true);
    {
      const unsigned d = __builtin_amdgcn_readfirstlane(sl == 2 ? -98304 : 49152);
      sl = sl == 2 ? 0 : sl + 1;
#pragma unroll
      for (int i = 0; i < 4; ++i) xa[i] += d;
      wa[0] += d; wa[1] += d;
    }
    bar();
    TS2(16 + c);
    step(3, true);
    if (c == 5 && threadIdx.x == 0) post_flag(p.ws + WS_FU + (((x * 28 + s) % 7) * 32 + (x * 28 + s) / 7) * 128, tag);
  }
  step(0, true); step(1, true); step(2, true); step(3, false);
  TS(3);
  const unsigned csb = lb + L_CS;
  PAD(2);
  bar();
  TS(4);
  float mean[2], rstd[2];
#pragma unroll
  for (int tt = 0; tt < 2; ++tt) { const f32x2 t = lds_ld<f32x2>(lb + L_ST + (64 * tw + 32 * tt + r) * 8); mean[tt] = t[0]; rstd[tt] = t[1]; }
  constexpr float C0 = -2.3022081985f, C1 = -0.1029432397f;
#pragma unroll
  for (int u = 0; u < 2; ++u) {
    f32x4 cg[2], cu[2];
#pragma unroll
    for (int b = 0; b < 2; ++b) {
      cg[b] = lds_ld<f32x4>(csb + (16 * (2 * fh + u) + 8 * b + 4 * hh) * 4);
      cu[b] = lds_ld<f32x4>(csb + 256 + (16 * (2 * fh + u) + 8 * b + 4 * hh) * 4);
    }
#pragma unroll
    for (int tt = 0; tt < 2; ++tt) {
      const float rr = rstd[tt], k0s = rr * C0, k1s = C1 * rr * rr * rr, ir2s = __builtin_amdgcn_rcpf(rr * rr);
      const f32x2 nm = {-mean[tt], -mean[tt]}, k0 = {k0s, k0s}, k1 = {k1s, k1s}, ir2 = {ir2s, ir2s};
      u32x4 o;
#pragma unroll
      for (int b = 0; b < 2; ++b)
#pragma unroll
        for (int e = 0; e < 4; e += 2) {
          const f32x2 ag = {acc[tt][u][8 * b + e], acc[tt][u][8 * b + e + 1]}, au = {acc[tt][u][8 * b + 4 + e], acc[tt][u][8 * b + 5 + e]};
          const f32x2 c2g = {cg[b][e], cg[b][e + 1]}, c2u = {cu[b][e], cu[b][e + 1]};
          const f32x2 a = __builtin_elementwise_fma(nm, c2g, ag), bb = __builtin_elementwise_fma(nm, c2u, au);
          const f32x2 z = a * __builtin_elementwise_fma(a * a, k1, k0);
          const f32x2 ex = {__builtin_amdgcn_exp2f(z[0]), __builtin_amdgcn_exp2f(z[1])};
          const f32x2 dn = __builtin_elementwise_fma(ex, ir2, ir2);
          const f32x2 rc = {__builtin_amdgcn_rcpf(dn[0]), __builtin_amdgcn_rcpf(dn[1])};
          const f32x2 hv = (a * bb) * rc;
          o[2 * b + (e >> 1)] = pk_bf16(hv[0], hv[1]);
        }
      const unsigned oh = WS_H + ((8 * ib + 2 * tw + tt) * 112 + 4 * jb + 2 * fh + u) * 1024 + lane * 16;
      asm volatile("buffer_store_dwordx4 %0, %1, %2, 0 offen" HSPOL "\n\ts_nop 1" ::"v"(o), "v"(oh), "s"(rws) : "memory");
    }
  }
  TS(5);
}

struct WdStage {
  rsrc_t rw; unsigned vw, lb; int kq, th, rot;
  DI void group(int g) const {
#pragma unroll
    for (int n = 0; n < 4; ++n) g2_piece(rw, vw, kq, th, 4 * g + n, rot, lb);
  }
};
#define TAILARG
#define TAILPASS
DI void loader_tail(const Params& p, unsigned lb, int s, int lane, const WdStage& wd, bool poller, unsigned tag TAILARG) {
  TSL(12);
  bar();
  TSL(13);
  wd.group(3);
  TSL(14);
  bar();
  wd.group(4);
  bar();
  wd.group(5);
  TSL(22);
  wait_vm<8>();
  TSL(23);
  bar();
  wd.group(6);
  wait_vm<8>();
  TSL(30);
  bar();
  wait_vm<4>();
  bar();
  if (poller) {
    const unsigned yf = __hip_atomic_load((const GAS unsigned*)(p.ws + WS_FY + (s % 7) * 32 * 128 + flag_off(32, lane)), __ATOMIC_RELAXED, __HIP_MEMORY_SCOPE_AGENT);
    const unsigned ok = flags_ok(yf, tag) ? 1u : 0u;
    if (lane == 0) lds_st<unsigned>(lb + 131072, ok);
  }
  wait_vm<0>();
  bar();
  const int yok = __builtin_amdgcn_readfirstlane(lds_ld<unsigned>(lb + 131072));
  if (poller && !yok) poll_flags(p.ws + WS_FY + (s % 7) * 32 * 128, 32, tag, lane);
  bar();
  if (!yok) bar();
}

DI void loader_x(const Params& p, unsigned lb, int x, int s, int lw, int lane) {
  const int ib = 4 * (x >> 2) + s / 7;
  const rsrc_t rq = mk_rsrc(p.q, S_ * D_ * 2);
  const int L3 = lane >> 3, L7 = lane & 7;
  const unsigned vx0 = (64 * lw + L3) * 896 + 16 * (L7 ^ ((lane >> 4) & 3));
  const unsigned vx1 = (64 * lw + 8 + L3) * 896 + 16 * (L7 ^ (((lane >> 4) & 3) | 4));
  const unsigned sx = ib * 256 * 896, lx = lb + lw * 8192;
  auto piece = [&](auto C, auto I) {
    constexpr int c = decltype(C)::value, i = decltype(I)::value;
    dma16c<(c % 3) * 49152 + i * 1024, c * 128 + (i >> 1) * 16 * 896>(rq, (i & 1) ? vx1 : vx0, sx, lx);
  };
  auto stage = [&](auto C) {
    piece(C, IC<0>{}); piece(C, IC<1>{}); piece(C, IC<2>{}); piece(C, IC<3>{}); piece(C, IC<4>{}); piece(C, IC<5>{}); piece(C, IC<6>{}); piece(C, IC<7>{});
  };
  if (XPRIO) __builtin_amdgcn_s_setprio(XPRIO);
  const rsrc_t rws = mk_rsrc(p.ws, WS_END);
  unsigned etag;
  TSL_DECL
  TS(8);
  { const unsigned oz = WS_EPOCH; asm volatile("buffer_load_dword %0, %1, %2, 0 offen" : "=&v"(etag) : "v"(oz), "s"(rws) : "memory"); }
  stage(IC<0>{});
  stage(IC<1>{});
  wait_vm<8>();
  asm volatile("" : "+v"(etag));
  const unsigned tag = __builtin_amdgcn_readfirstlane(etag) + 1u;
  TS(1);
  bar();
  stage(IC<2>{});
  auto lstage = [&](auto C) {
    constexpr int c = decltype(C)::value;
    TSL(16 + c);
    if (c < 5) wait_vm<8>(); else wait_vm<0>();
    TSL(24 + c);
    bar();
    TSL(2 + c);
    if constexpr (c + 3 <= 6) stage(IC<c + 3>{});
    if (c == 4) {
      const unsigned os = WS_STATS + (256 * ib + 64 * lw + lane) * 16;
      float mean, rstd;
      stats_wait(rws, os, tag, mean, rstd);
      lds_st<f32x2>(lb + L_ST + (64 * lw + lane) * 8, (f32x2){mean, rstd});
    }
  };
  lstage(IC<0>{}); lstage(IC<1>{}); lstage(IC<2>{}); lstage(IC<3>{}); lstage(IC<4>{}); lstage(IC<5>{});
  bar();
  bar();
  const WdStage wd = {mk_rsrc(p.wd, F_ * D_ * 2), g2_vw(lane, s % 7), lb, lw, 0, g2_rot(s)};
  TSL(8);
  wd.group(0); wd.group(1); wd.group(2);
  TSL(9);
  bar();
  bar();
  wait_vm<0>();
  loader_tail(p, lb, s, lane, wd, false, 0u TAILPASS);
  TSL_DUMP();
}

DI void loader_w(const Params& p, unsigned lb, int x, int s, int lw, int lane) {
  const int jb = 7 * (x & 3) + s % 7;
  const rsrc_t rg = mk_rsrc(p.wg, D_ * F_ * 2), ru = mk_rsrc(p.wu, D_ * F_ * 2);
  const rsrc_t rw = mk_rsrc(p.wd, F_ * D_ * 2), rws = mk_rsrc(p.ws, WS_END);
  const int L3 = lane >> 3, L7 = lane & 7;
  const unsigned vwA = (16 * lw + L3) * 3584 + jb * 128 + 32 * ((L7 >> 1) ^ ((lane >> 4) & 1)) + 16 * (L7 & 1);
  const unsigned vwB = (16 * lw + L3) * 3584 + jb * 128 + 32 * ((L7 >> 1) ^ (((lane >> 4) & 1) | 2)) + 16 * (L7 & 1);
  const unsigned lx = lb + 32768 + lw * 2048;
  auto stage = [&](auto C) {
    constexpr int c = decltype(C)::value;
    constexpr int cm = c;
    dma16c<(c % 3) * 49152, cm * 64 * 3584>(rg, vwA, 0, lx);
    dma16c<(c % 3) * 49152 + 1024, cm * 64 * 3584 + 8 * 3584>(rg, vwB, 0, lx);
    dma16c<(c % 3) * 49152 + 8192, cm * 64 * 3584>(ru, vwB, 0, lx);
    dma16c<(c % 3) * 49152 + 8192 + 1024, cm * 64 * 3584 + 8 * 3584>(ru, vwA, 0, lx);
  };
  if (WPRIO) __builtin_amdgcn_s_setprio(WPRIO);
  unsigned etag;
  { const unsigned oz = WS_EPOCH; asm volatile("buffer_load_dword %0, %1, %2, 0 offen" : "=&v"(etag) : "v"(oz), "s"(rws) : "memory"); }
  stage(IC<0>{});
  stage(IC<1>{});
  wait_vm<4>();
  asm volatile("" : "+v"(etag));
  const unsigned tag = __builtin_amdgcn_readfirstlane(etag) + 1u;
  bar();
  const int mi = x * 28 + s, cb = mi % 7, tb = mi / 7, ucg = lane >> 3, urp = lane & 7, ut0 = 64 * tb + 16 * lw + 2 * urp;
  u32x4 kk[5], vv[2];
  stage(IC<2>{});
  {
    const rsrc_t rk = mk_rsrc(p.k, S_ * D_ * 2), rv = mk_rsrc(p.v, S_ * D_ * 2);
    unsigned ok[5];
#pragma unroll
    for (int n = 0; n < 5; ++n) ok[n] = ut0 - 3 + n >= 0 ? (unsigned)((ut0 - 3 + n) * 896 + cb * 128 + ucg * 16) : 0x80000000u;
    const unsigned ov = ut0 * 896 + cb * 128 + ucg * 16;
    asm volatile("buffer_load_dwordx4 %0, %7, %13, 0 offen" KVPOL "\n\tbuffer_load_dwordx4 %1, %8, %13, 0 offen" KVPOL "\n\tbuffer_load_dwordx4 %2, %9, %13, 0 offen" KVPOL "\n\t"
                 "buffer_load_dwordx4 %3, %10, %13, 0 offen" KVPOL "\n\tbuffer_load_dwordx4 %4, %11, %13, 0 offen" KVPOL "\n\t"
                 "buffer_load_dwordx4 %5, %12, %14, 0 offen" KVPOL "\n\tbuffer_load_dwordx4 %6, %12, %14, 0 offen offset:896" KVPOL
                 : "=&v"(kk[0]), "=&v"(kk[1]), "=&v"(kk[2]), "=&v"(kk[3]), "=&v"(kk[4]), "=&v"(vv[0]), "=&v"(vv[1])
                 : "v"(ok[0]), "v"(ok[1]), "v"(ok[2]), "v"(ok[3]), "v"(ok[4]), "v"(ov), "s"(rk), "s"(rv) : "memory");
    dma16(mk_rsrc(p.sk, D_ * 8), lane < 32 ? cb * 512 + lane * 16 : 0x80000000u, 0, lb + L_SK);
  }
  auto wd_touch = [&](int i) {
    const int sh = 2 * i + (lw & 1);
    if ((lw >> 1) == (i & 1) && sh < 7) touch(rw, (unsigned)((s * 7 + sh) * 64 + lane) * 128u, lb + L_TOUCH);
  };
  const int chl = lw >> 1, ch = 2 * mi + chl, tA = (lw & 1) * 128 + lane;
  u32x4 hA, hB; u32x2 gA, gB, cg2; unsigned uf = 0;
  auto upairs = [&](const int j0) {
    const int tl = 16 * lw + 2 * urp, e = (tl & ~7) + 6 - (tl & 7);
    const unsigned ta = lb + L_UT + 8 * ucg * 128 + (((e >> 3) ^ ucg) * 16) + 2 * (e & 7);
    u32x4 wq[2];
#pragma unroll
    for (int j = 0; j < 2; ++j) wq[j] = lds_ld<u32x4>(lb + L_SK + ucg * 64 + (j0 + j) * 16);
    f32x2 kf[2][5], vf[2][2], wt[2][4], s0[2], s1[2];
#pragma unroll
    for (int j = 0; j < 2; ++j) {
#pragma unroll
      for (int n = 0; n < 5; ++n) kf[j][n] = (f32x2){bf_lo(kk[n][j0 + j]), bf_hi(kk[n][j0 + j])};
#pragma unroll
      for (int n = 0; n < 2; ++n) vf[j][n] = (f32x2){bf_lo(vv[n][j0 + j]), bf_hi(vv[n][j0 + j])};
    }
#pragma unroll
    for (int j = 0; j < 2; ++j) {
      wt[j][0] = (f32x2){bf_lo(wq[j][0]), bf_lo(wq[j][2])}; wt[j][1] = (f32x2){bf_hi(wq[j][0]), bf_hi(wq[j][2])};
      wt[j][2] = (f32x2){bf_lo(wq[j][1]), bf_lo(wq[j][3])}; wt[j][3] = (f32x2){bf_hi(wq[j][1]), bf_hi(wq[j][3])};
    }
    SB();
#pragma unroll
    for (int j = 0; j < 2; ++j) { s0[j] = kf[j][0] * wt[j][3]; s1[j] = kf[j][1] * wt[j][3]; }
    SB();
#pragma unroll
    for (int t = 2; t >= 0; --t) {
#pragma unroll
      for (int j = 0; j < 2; ++j) { s0[j] = __builtin_elementwise_fma(kf[j][3 - t], wt[j][t], s0[j]); s1[j] = __builtin_elementwise_fma(kf[j][4 - t], wt[j][t], s1[j]); }
      SB();
    }
#pragma unroll
    for (int j = 0; j < 2; ++j) { s0[j] *= vf[j][0]; s1[j] *= vf[j][1]; }
    SB();
#pragma unroll
    for (int j = 0; j < 2; ++j) {
      lds_st<unsigned>(ta + (2 * (j0 + j)) * 128, pk_bf16(s1[j][0], s0[j][0]));
      lds_st<unsigned>(ta + (2 * (j0 + j) + 1) * 128, pk_bf16(s1[j][1], s0[j][1]));
    }
  };
  TSL_DECL
  auto lstage = [&](auto C) {
    constexpr int c = decltype(C)::value;
    TSL(16 + c);
    if (c == 0) wait_vm<12>(); else if (c < 4) wait_vm<4>(); else if (c == 4) wait_vm<6>(); else wait_vm<5>();
    TSL(24 + c);
    bar();
    TSL(2 + c);
    if (c == 3) {
#pragma unroll
      for (int n = 0; n < 2; ++n) {
        const int cl = 16 * lw + 8 * n + (lane >> 3);
        const u32x4 t = lds_ld<u32x4>(lb + L_UT + cl * 128 + (((lane & 7) ^ (cl >> 3)) * 16));
        const unsigned o = WS_UT + (64 * cb + cl) * TROW + 128 * tb + (lane & 7) * 16;
        asm volatile("buffer_store_dwordx4 %0, %1, %2, 0 offen sc1\n\ts_nop 1" ::"v"(t), "v"(o), "s"(rws) : "memory");
      }
    }
    if (c == 4) {
      const rsrc_t rh = mk_rsrc(p.h, D_ * S_ * 2);
      const unsigned oh = ch * 4096 + tA * 16;
      asm volatile("buffer_load_dwordx4 %0, %4, %5, 0 offen nt\n\tbuffer_load_dwordx2 %1, %4, %5, 0 offen offset:16 nt\n\t"
                   "buffer_load_dwordx4 %2, %4, %5, 0 offen offset:1024 nt\n\tbuffer_load_dwordx2 %3, %4, %5, 0 offen offset:1040 nt"
                   : "=&v"(hA), "=&v"(gA), "=&v"(hB), "=&v"(gB) : "v"(oh), "s"(rh) : "memory");
      const unsigned oc = lw < 2 ? WS_CS + (lw * 1792 + jb * 64 + lane) * 8 : 0x80000000u;
      asm volatile("buffer_load_dwordx2 %0, %1, %2, 0 offen sc1" : "=&v"(cg2) : "v"(oc), "s"(rws) : "memory");
    }
    if (c == 5) {
      const unsigned of = WS_FU + (mi >> 5) * 32 * 128 + flag_off(32, lane);
      asm volatile("buffer_load_dword %0, %1, %2, 0 offen sc1" : "=&v"(uf) : "v"(of), "s"(rws) : "memory");
    }
    if (c == 1) {
      asm volatile("" : "+v"(kk[0]), "+v"(kk[1]), "+v"(kk[2]), "+v"(kk[3]), "+v"(kk[4]), "+v"(vv[0]), "+v"(vv[1]));
      upairs(0);
    }
    if (c == 2) upairs(2);
    if constexpr (c + 3 <= 6) stage(IC<c + 3>{});
    if (WDT && c >= 2) wd_touch(c - 2);
  };
  lstage(IC<0>{}); lstage(IC<1>{}); lstage(IC<2>{}); lstage(IC<3>{}); lstage(IC<4>{}); lstage(IC<5>{});
  asm volatile("s_waitcnt vmcnt(1)" : "+v"(cg2)::"memory");
  if (lw < 2) {
    if (!__all(cg2[0] == tag)) {
      for (unsigned it = 0; it < SPIN_CAP; ++it) {
        cg2 = __builtin_amdgcn_raw_buffer_load_b64(rws, WS_CS + (lw * 1792 + jb * 64 + lane) * 8, 0, 16);
        if (__all(cg2[0] == tag)) break;
        __builtin_amdgcn_s_sleep(1);
      }
    }
    lds_st<unsigned>(lb + L_CS + lw * 256 + lane * 4, cg2[1]);
  }
  if (lw == 0 && lane == 0) { lds_st<unsigned>(lb + L_CNT, 0u); }
  bar();
  wait_vm<0>();
  asm volatile("" : "+v"(hA), "+v"(gA), "+v"(hB), "+v"(gB), "+v"(uf));
  if (!flags_ok(uf, tag)) poll_flags(p.ws + WS_FU + (mi >> 5) * 32 * 128, 32, tag, lane);
  u32x4 uA, uB;
  {
    const unsigned ou = WS_UT + ch * TROW + tA * 16;
    asm volatile("buffer_load_dwordx4 %0, %2, %3, 0 offen sc1\n\tbuffer_load_dwordx4 %1, %2, %3, 0 offen offset:1024 sc1" : "=&v"(uA), "=&v"(uB) : "v"(ou), "s"(rws) : "memory");
  }
  const unsigned up = lb + CV_UP + chl * 4608, hs = lb + CV_HS + chl * 16640;
  {
    auto copies = [&](const u32x4& h4, const u32x2& h2, int t4) {
      const unsigned w0 = h4[0], w1 = h4[1], w2 = h4[2], w3 = h4[3], w4 = h2[0], w5 = h2[1];
      const unsigned a0 = __builtin_amdgcn_alignbit(w1, w0, 16), a1 = __builtin_amdgcn_alignbit(w2, w1, 16), a2 = __builtin_amdgcn_alignbit(w3, w2, 16),
                     a3 = __builtin_amdgcn_alignbit(w4, w3, 16), a4 = __builtin_amdgcn_alignbit(w5, w4, 16);
      const unsigned o = hs + 32 + 16 * t4;
      lds_st<u32x4>(o, (u32x4){w0, w1, w2, w3});
      lds_st<u32x4>(o + 4160, (u32x4){a0, a1, a2, a3});
      lds_st<u32x4>(o + 8320, (u32x4){w1, w2, w3, w4});
      lds_st<u32x4>(o + 12480, (u32x4){a1, a2, a3, a4});
    };
    copies(hA, gA, tA); copies(hB, gB, tA + 64);
    if (tA == 0) {
      const unsigned w0 = hA[0], a0 = __builtin_amdgcn_alignbit(hA[1], hA[0], 16);
      const u32x4 z = {0, 0, 0, 0};
#pragma unroll
      for (int sg = 0; sg < 4; ++sg) lds_st<u32x4>(hs + sg * 4160, z);
      lds_st<u32x4>(hs + 16, z);
      lds_st<u32x4>(hs + 4160 + 16, (u32x4){0, 0, 0, w0 << 16});
      lds_st<u32x4>(hs + 8320 + 16, (u32x4){0, 0, 0, w0});
      lds_st<u32x4>(hs + 12480 + 16, (u32x4){0, 0, w0 << 16, a0});
    }
    if (tA < 30) lds_st<u32x4>(up + 16 * tA, (u32x4){0, 0, 0, 0});
  }
  wait_vm<0>();
  asm volatile("" : "+v"(uA), "+v"(uB));
  lds_st<u32x4>(up + 480 + 16 * tA, uA);
  lds_st<u32x4>(up + 480 + 16 * (tA + 64), uB);
  bar();
  const WdStage wd = {rw, g2_vw(lane, s % 7), lb, lw, 1, g2_rot(s)};
  TSL(8);
  wd.group(0); wd.group(1); wd.group(2);
  TSL(9);
  if (lw == 0) {
    unsigned hf;
    if (HSLEEP) __builtin_amdgcn_s_sleep(HSLEEP);
    const unsigned of = WS_FH + x * 28 * 128 + flag_off(28, lane);
    asm volatile("buffer_load_dword %0, %1, %2, 0 offen sc1" : "=&v"(hf) : "v"(of), "s"(rws) : "memory");
    bar();
    TSL(10);
    asm volatile("s_waitcnt vmcnt(0)" : "+v"(hf)::"memory");
    if (!flags_ok(hf, tag)) poll_flags(p.ws + WS_FH + x * 28 * 128, 28, tag, lane);
    TSL(11);
  } else {
    bar();
  }
  bar();
  wait_vm<0>();
  loader_tail(p, lb, s, lane, wd, lw == 0, tag TAILPASS);
  TSL_DUMP();
}

DI void phase_conv(const Params& p, unsigned lb, int x, int s, int w, int lane, Ctx& cx) {
  const int mi = x * 28 + s, chl = w >> 2, wq = w & 3, ch = 2 * mi + chl, t4 = wq * 64 + lane;
  const rsrc_t rws = mk_rsrc(p.ws, WS_END);
  const unsigned tag = cx.tag;
  const float dv = cx.dv;
  const unsigned up = lb + CV_UP + chl * 4608, hs = lb + CV_HS + chl * 16640;
  {
    const int tok = 256 * x + 64 * (s / 7) + 16 * (w >> 1) + (lane & 15);
    const unsigned ox = tok * 896 + (s % 7) * 128 + (4 * (w & 1) + (lane >> 4)) * 16, os = WS_STATS + tok * 16;
    asm volatile("buffer_load_dwordx4 %0, %2, %4, 0 offen\n\tbuffer_load_dwordx4 %1, %3, %5, 0 offen sc1"
                 : "=&v"(cx.xq), "=&v"(cx.sg) : "v"(ox), "v"(os), "s"(mk_rsrc(p.q, S_ * D_ * 2)), "s"(rws) : "memory");
  }
  bar();
  TS(6);
  const int i = lane & 15, kg = lane >> 4, sg = (i + 1) & 3;
  f32x4 acc[8] = {};
  {
    const unsigned ga = up + 448 - 128 * wq + 32 * i + 16 * kg;
    unsigned f0 = hs + sg * 4160 + 128 * wq + 2 * (25 + i - 8 * kg - sg), f1 = f0 + 8;
    asm volatile("" : "+v"(f1));
    auto publish_h = [&]() {
      wait_vm<0>();
      if (lane == 0) {
        const unsigned old = __hip_atomic_fetch_add((LAS unsigned*)(size_t)(lb + L_CNT), 1u, __ATOMIC_RELAXED, __HIP_MEMORY_SCOPE_WORKGROUP);
        if (old == 7u) post_flag(p.ws + WS_FH + ((4 * (x >> 2) + s / 7) * 28 + 7 * (x & 3) + s % 7) * 128, tag);
      }
    };
    bf16x8 G[9], FA[2], FB[2];
    const unsigned gbb = ga - 64;
    auto rdA = [&](int t) { FA[t & 1] = cat(lds_ld<s16x4>(f0 + 512 * t), lds_ld<s16x4>(f1 + 512 * t)); };
    auto rdB = [&](int t) { FB[t & 1] = cat(lds_ld<s16x4>(f0 + 64 + 512 * (7 - t)), lds_ld<s16x4>(f1 + 64 + 512 * (7 - t))); };
    rdA(0);
#pragma unroll
    for (int b = 7; b >= 0; --b) G[b] = lds_ld<bf16x8>(ga + 512 * b);
    rdB(0); G[8] = lds_ld<bf16x8>(gbb);
#pragma unroll
    for (int t = 0; t < 8; ++t) {
      if (t == DRT) publish_h();
#pragma unroll
      for (int b = 7 - t; b >= 0; --b) {
        acc[t + b] = mfma16(FA[t & 1], G[b], acc[t + b]);
        SB();
        if (b == 7 - t && t < 7) { G[7 - t] = lds_ld<bf16x8>(gbb + 512 * (t + 1)); rdA(t + 1); }
        if (b == 6 - t && t < 7) rdB(t + 1);
        SB();
      }
#pragma unroll
      for (int b = 0; b <= t; ++b) {
        acc[7 - t + b] = mfma16(FB[t & 1], G[b == 0 ? 8 : 8 - b], acc[7 - t + b]);
        SB();
      }
    }
    if (DRT == 8) publish_h();
  }
  TS2(13);
  bar();
  TS(7);
  const unsigned red = lb + CV_RED + chl * 24576;
  f32x4 y0, y1;
  {
    auto scatter = [&](auto WQ) {
      constexpr int q0 = decltype(WQ)::value;
#pragma unroll
      for (int o = 1; o < 4; ++o) {
        constexpr int dummy = 0; (void)dummy;
        const int dst = (q0 + o) & 3;
        lds_st<f32x4>(red + (dst * 3 + (o - 1)) * 2048 + lane * 16, acc[2 * dst]);
        lds_st<f32x4>(red + (dst * 3 + (o - 1)) * 2048 + 1024 + lane * 16, acc[2 * dst + 1]);
      }
      y0 = acc[2 * q0]; y1 = acc[2 * q0 + 1];
    };
    if (wq == 0) scatter(IC<0>{}); else if (wq == 1) scatter(IC<1>{}); else if (wq == 2) scatter(IC<2>{}); else scatter(IC<3>{});
  }
  const unsigned ua = up + 480 + 1024 * wq + 32 * i + 16 * (kg >> 1) + 8 * (1 - (kg & 1));
  const u32x2 ua0 = lds_ld<u32x2>(ua), ua1 = lds_ld<u32x2>(ua + 512);
  bar();
  h_first(p, x, s, w, lane, cx);
#pragma unroll
  for (int o = 0; o < 3; ++o) {
    y0 += lds_ld<f32x4>(red + (wq * 3 + o) * 2048 + lane * 16);
    y1 += lds_ld<f32x4>(red + (wq * 3 + o) * 2048 + 1024 + lane * 16);
  }
  y0[0] = rbf(y0[0]) + rbf(dv * bf_hi(ua0[1])); y0[1] = rbf(y0[1]) + rbf(dv * bf_lo(ua0[1])); y0[2] = rbf(y0[2]) + rbf(dv * bf_hi(ua0[0])); y0[3] = rbf(y0[3]) + rbf(dv * bf_lo(ua0[0]));
  y1[0] = rbf(y1[0]) + rbf(dv * bf_hi(ua1[1])); y1[1] = rbf(y1[1]) + rbf(dv * bf_lo(ua1[1])); y1[2] = rbf(y1[2]) + rbf(dv * bf_hi(ua1[0])); y1[3] = rbf(y1[3]) + rbf(dv * bf_lo(ua1[0]));
  const unsigned d0 = pk_bf16(y0[0], y0[1]), d1 = pk_bf16(y0[2], y0[3]), e0 = pk_bf16(y1[0], y1[1]), e1 = pk_bf16(y1[2], y1[3]);
  const auto s0 = __builtin_amdgcn_permlane16_swap(d0, e0, false, false);
  const auto s1 = __builtin_amdgcn_permlane16_swap(d1, e1, false, false);
  const u32x4 o = {s0[0], s1[0], s0[1], s1[1]};
  const unsigned oy = WS_YT + ch * TROW + 512 * (2 * wq + (kg & 1)) + 32 * i + 8 * (kg & 2);
  asm volatile("buffer_store_dwordx4 %0, %1, %2, 0 offen sc1\n\ts_nop 1" ::"v"(o), "v"(oy), "s"(rws) : "memory");
}

DI void phase_gemm2(const Params& p, unsigned lb, int x, int s, int w, int lane, Ctx& cx) {
  const int i2 = s / 7, db = s % 7, th = w >> 2, kq = w & 3, rot = g2_rot(s);
  const unsigned tag = cx.tag;
  const rsrc_t rws = mk_rsrc(p.ws, WS_END);
  const rsrc_t rq = mk_rsrc(p.q, S_ * D_ * 2), ro = mk_rsrc(p.out, S_ * D_ * 2);
  const int tk = 16 * (w >> 1) + (lane & 15), ck = 4 * (w & 1) + (lane >> 4), tok = 256 * x + 64 * i2 + tk;
  PAD(3);
  bar();
  TS(10);
  const unsigned hb = WS_H + ((8 * x + 2 * i2 + th) * 112 + 28 * kq) * 1024;
  const unsigned vh = lane * 16;
  u32x4 (&hr)[HD] = cx.hr;
  const int hh = lane >> 5, blk = (lane >> 4) & 1, q = (lane & 15) >> 2, pp = lane & 3;
  unsigned aa[2];
#pragma unroll
  for (int dt = 0; dt < 2; ++dt) aa[dt] = lb + kq * 8192 + (4 * hh + q) * 128 + 64 * (dt ^ ((q >> 1) & 1)) + 32 * blk + 8 * pp;
  f32x16 acc[2] = {};
  bf16x8 fa[2][2];
  static_assert(HD == 4, "one group of four k-steps per loop iteration");
  auto rd1 = [&](int j, int dt) { fa[j & 1][dt] = cat(lds_tr(aa[dt] + (j & 3) * 2048), lds_tr(aa[dt] + (j & 3) * 2048 + 1024)); };
  auto step = [&](int j, bool ahead) {
    SB();
    const bf16x8 b = __builtin_bit_cast(bf16x8, hr[j]);
    acc[0] = mfma32(fa[j & 1][0], b, acc[0]);
    SB();
    if (ahead) rd1(j + 1, 0);
    SB();
    acc[1] = mfma32(fa[j & 1][1], b, acc[1]);
    SB();
    if (ahead) rd1(j + 1, 1);
    SB();
  };
  u32x4 yv; int yok;
  rd1(0, 0); rd1(0, 1);
  int st = rot + 4; st -= st >= 28 ? 28 : 0;
#pragma unroll 1
  for (int g = 0; g < 6; ++g) {
#pragma unroll
    for (int j = 0; j < 4; ++j) {
      if (j == 3) {
        const unsigned d = (g & 3) == 3 ? (unsigned)-98304 : 32768u;
        aa[0] += d; aa[1] += d;
        bar();
        TS2(24 + g);
      }
      asm volatile("s_waitcnt vmcnt(3)" : "+v"(hr[j])::"memory");
      step(j, true);
      asm volatile("buffer_load_dwordx4 %0, %1, %2, %3 offen" HPOL : "=v"(hr[j]) : "v"(vh), "s"(rws), "s"(hb + st * 1024) : "memory");
      st = st == 27 ? 0 : st + 1;
      if (j == 3 && g == 2 && threadIdx.x == 0) post_flag(p.ws + WS_FY + (x * 28 + s) * 128, tag);
      SB();
    }
  }
  yok = __builtin_amdgcn_readfirstlane(lds_ld<unsigned>(lb + 131072));
  {
    const unsigned oy = yok ? WS_YT + (64 * db + (threadIdx.x >> 3)) * TROW + (256 * x + 64 * i2) * 2 + (threadIdx.x & 7) * 16 : 0x80000000u;
    asm volatile("buffer_load_dwordx4 %0, %1, %2, 0 offen sc1" : "=&v"(yv) : "v"(oy), "s"(rws) : "memory");
  }
  asm volatile("s_waitcnt vmcnt(4)" : "+v"(hr[0])::"memory"); step(0, true);
  asm volatile("s_waitcnt vmcnt(3)" : "+v"(hr[1])::"memory"); step(1, true);
  asm volatile("s_waitcnt vmcnt(2)" : "+v"(hr[2])::"memory"); step(2, true);
  asm volatile("s_waitcnt vmcnt(1)" : "+v"(hr[3])::"memory"); step(3, false);
  u32x4 xq = cx.xq, sg = cx.sg;
  TS(11);
  const unsigned pb = lb, yb = lb + 135168;
  {
    const int row = 32 * th + (lane & 31);
#pragma unroll
    for (int dt = 0; dt < 2; ++dt)
#pragma unroll
      for (int G = 0; G < 4; ++G) {
        const f32x4 t = {acc[dt][4 * G], acc[dt][4 * G + 1], acc[dt][4 * G + 2], acc[dt][4 * G + 3]};
        lds_st<f32x4>(pb + kq * 16384 + row * 256 + (((8 * dt + 2 * G + hh) ^ (row & 15)) * 16), t);
      }
  }
  wait_vm<0>();
  asm volatile("" : "+v"(xq), "+v"(sg), "+v"(yv));
  if (yok) lds_st<u32x4>(yb + (threadIdx.x >> 3) * 144 + (threadIdx.x & 7) * 16, yv);
  float mean = __uint_as_float(sg[1]), rstd = __uint_as_float(sg[3]);
  if (!__all(sg[0] == tag && sg[2] == tag)) stats_wait(rws, WS_STATS + tok * 16, tag, mean, rstd);
  bar();
  if (!yok) {
    yv = __builtin_amdgcn_raw_buffer_load_b128(rws, WS_YT + (64 * db + (threadIdx.x >> 3)) * TROW + (256 * x + 64 * i2) * 2 + (threadIdx.x & 7) * 16, 0, 16);
    lds_st<u32x4>(yb + (threadIdx.x >> 3) * 144 + (threadIdx.x & 7) * 16, yv);
    bar();
  }
  f32x4 o0 = {}, o1 = {};
#pragma unroll
  for (int k4 = 0; k4 < 4; ++k4) {
    o0 += lds_ld<f32x4>(pb + k4 * 16384 + tk * 256 + (((2 * ck) ^ (tk & 15)) * 16));
    o1 += lds_ld<f32x4>(pb + k4 * 16384 + tk * 256 + (((2 * ck + 1) ^ (tk & 15)) * 16));
  }
  const unsigned ya = yb + (8 * ck + q) * 144 + (16 * (w >> 1) + 4 * pp) * 2;
  const s16x4 y0 = lds_tr(ya), y1 = lds_tr(ya + 4 * 144);
  u32x4 ov;
#pragma unroll
  for (int e = 0; e < 8; e += 2) {
    const f32x2 o = rbf2(e < 4 ? (f32x2){o0[e & 3], o0[(e & 3) + 1]} : (f32x2){o1[e & 3], o1[(e & 3) + 1]});
    const f32x2 xv = {bf_lo(xq[e >> 1]), bf_hi(xq[e >> 1])};
    const unsigned yp = e < 4 ? __builtin_bit_cast(u32x2, y0)[(e & 3) >> 1] : __builtin_bit_cast(u32x2, y1)[(e & 3) >> 1];
    const f32x2 yy = {bf_lo(yp), bf_hi(yp)};
    const f32x2 t = o * (f32x2){-1.4426950409f, -1.4426950409f};
    const f32x2 dn = (f32x2){__builtin_amdgcn_exp2f(t[0]), __builtin_amdgcn_exp2f(t[1])} + (f32x2){1.f, 1.f};
    const f32x2 g = rbf2(o * (f32x2){__builtin_amdgcn_rcpf(dn[0]), __builtin_amdgcn_rcpf(dn[1])});
    const f32x2 xn = rbf2(rbf2(xv - (f32x2){mean, mean}) * (f32x2){rstd, rstd});
    const f32x2 r2 = xv + rbf2(rbf2(xn * yy) * g);
    ov[e >> 1] = pk_bf16(r2[0], r2[1]);
  }
  __builtin_amdgcn_raw_buffer_store_b128(ov, ro, tok * 896 + db * 128 + ck * 16, 0, OUTAUX);
  TS(12);
}

__global__ __launch_bounds__(1024, 4) void h3_geglu_kernel(Params p) {
  __shared__ __attribute__((aligned(16))) char smem[LDS_BYTES];
  const unsigned lb = (unsigned)(size_t)(LAS char*)smem;
  const int tid = threadIdx.x, lane = tid & 63, w = __builtin_amdgcn_readfirstlane(tid >> 6);
  const int b = blockIdx.x, x = b & 7, s = b >> 3;
  const unsigned long long t_start = rtime();
  const char* pc;
  asm volatile("s_getpc_b64 %0" : "=s"(pc));
  if (CODEW == 2 && w == 15 && s < 28) touch_g(pc + (8 * s + (lane & 7)) * 128, lb + L_TOUCH);
  TS(0);
  if (s < 28) {
    if (w >= 12) {
      loader_w(p, lb, x, s, w - 12, lane);
    } else if (w >= 8) {
      loader_x(p, lb, x, s, w - 8, lane);
    } else {
      Ctx cx; cx.t0 = t_start;
      phase_gemm1(p, lb, x, s, w, lane, cx);
      phase_conv(p, lb, x, s, w, lane, cx);
      TS(9);
      phase_gemm2(p, lb, x, s, w, lane, cx);
    }
  } else {
    const int j = b - 224;
    if (CODEW == 1 && (j >> 3) == 0 && w < 4) {
      const int ln = w * 64 + lane;
      touch_g(pc + (ln < CODE_LINES ? ln : 0) * 128, lb + 65536);
    }
    if (TLBW && (j >> 3) == 0 && w >= 12) {
      if (w < 15) touch(mk_rsrc(p.ws, WS_END), (unsigned)((w - 12) * 64 + lane) * 65536u, lb + 65536);
      else touch(mk_rsrc(p.out, S_ * D_ * 2), (unsigned)lane * 65536u, lb + 65536);
    }
    if (WARM && w < 14) {
      const int q = j >> 3;
      touch(mk_rsrc((w & 1) ? p.v : p.k, S_ * D_ * 2), (unsigned)((256 * x + 64 * q) * 896 + ((w >> 1) * 64 + lane) * 128), lb + 65536);
    }
    if (j < 28) {
      const unsigned tag = __builtin_amdgcn_readfirstlane(*(const GAS unsigned*)(p.ws + WS_EPOCH) + 1u);
      const int m = w & 1, r0 = (w >> 1) * 8 + (lane >> 3);
      const rsrc_t rm = mk_rsrc(m ? p.wu : p.wg, D_ * F_ * 2);
      u32x4 t[7];
#pragma unroll
      for (int n = 0; n < 7; ++n) t[n] = __builtin_amdgcn_raw_buffer_load_b128(rm, (r0 + 64 * n) * 3584 + j * 128 + (lane & 7) * 16, 0, 0);
      float a[8] = {};
#pragma unroll
      for (int n = 0; n < 7; ++n)
#pragma unroll
        for (int i = 0; i < 4; ++i) { a[2 * i] += bf_lo(t[n][i]); a[2 * i + 1] += bf_hi(t[n][i]); }
      lds_st<f32x4>(lb + (w * 8 + (lane >> 3)) * 256 + (lane & 7) * 32, (f32x4){a[0], a[1], a[2], a[3]});
      lds_st<f32x4>(lb + (w * 8 + (lane >> 3)) * 256 + (lane & 7) * 32 + 16, (f32x4){a[4], a[5], a[6], a[7]});
      bar();
      if (w < 2) {
        float sum = 0.f;
#pragma unroll
        for (int q = 0; q < 64; ++q) sum += lds_ld<float>(lb + (((q >> 3) * 2 + w) * 8 + (q & 7)) * 256 + lane * 4);
        const u32x2 g = {tag, __float_as_uint(sum)};
        __builtin_amdgcn_raw_buffer_store_b64(g, mk_rsrc(p.ws, WS_END), WS_CS + (w * 1792 + j * 64 + lane) * 8, 0, 16);
      }
    }
    if (WARM && w < 14) {
      const int blk = w * 32 + j;
      if (blk < 196) touch(mk_rsrc(p.wd, F_ * D_ * 2), (unsigned)(blk * 64 + lane) * 128u, lb + 65536);
      else touch(mk_rsrc(p.h, D_ * S_ * 2), (unsigned)((blk - 196) * 64 + lane) * 128u, lb + 65536);
    }
    if (b == 224 && w == 15) {
      const unsigned tag = __builtin_amdgcn_readfirstlane(*(const GAS unsigned*)(p.ws + WS_EPOCH) + 1u);
#pragma unroll 1
      for (int n = 0; n < 4; ++n) poll_flags_slow(p.ws + WS_FH + n * 56 * 128, 56, tag, lane);
      if (lane == 0) post_flag(p.ws + WS_EPOCH, tag);
    }
    wait_vm<0>();
  }
  TS(15);
}

extern "C" void kernel_launch(void* const* d_in, const int* in_sizes, int n_in, void* d_out, int out_size, void* d_ws, size_t ws_size, hipStream_t stream) {
  Params p;
  p.q = (const u16*)d_in[0]; p.k = (const u16*)d_in[1]; p.v = (const u16*)d_in[2]; p.sk = (const u16*)d_in[3];
  p.h = (const u16*)d_in[4]; p.dv = (const u16*)d_in[5];
  p.wg = (const u16*)d_in[7]; p.wu = (const u16*)d_in[8]; p.wd = (const u16*)d_in[9];
  p.out = (u16*)d_out; p.ws = (char*)d_ws;
  h3_geglu_kernel<<<256, 1024, 0, stream>>>(p);
}
```
